```python
import torch

def _softmax(x, axis=-1):
    return torch.softmax(x, dim=axis)
def _gelu(x):
    return torch.nn.functional.gelu(x, approximate="tanh")
def _silu(x):
    return torch.nn.functional.silu(x)
def _sigmoid(x):
    return torch.sigmoid(x)
def _layernorm(x, axis=-1, eps=1e-5):
    m = x.mean(dim=axis, keepdim=True)
    v = x.var(dim=axis, keepdim=True, unbiased=False)
    return (x - m) / torch.sqrt(v + eps)
def _scatter_add(tgt, idx, src, axis=0):
    return tgt.clone().index_add_(axis, idx, src)
def _segment_sum(src, seg, n):
    out = torch.zeros((n,) + src.shape[1:], dtype=src.dtype)
    return out.index_add_(0, seg, src)
def _pad(x, pw):
    flat = tuple(v for pair in reversed(pw) for v in pair)
    return torch.nn.functional.pad(x, flat)

kernel_name = 'recurrent_scan__sliding_block_cached__dense_activation_squared_relu__prompt'

def setup_inputs():

    _w = (lambda a: (lambda t: t.to(torch.bfloat16) if t.is_floating_point() else t)(torch.as_tensor(a)))
    def _A_setup():
        B, Sq, D = 1, 2048, 512
        Hq, Hkv, Sk, Dh = 32, 32, 2048, 128
        assert Hq % Hkv == 0, (Hq, Hkv)
        return {
            "x": _w((lambda *s: torch.randn(*s, dtype=torch.bfloat16) * 0.05)(B, Sq, D)),
            "k_cache": _w((lambda *s: torch.randn(*s, dtype=torch.bfloat16) * 0.8)(B, Hkv, Sk, Dh)),
            "v_cache": _w((lambda *s: torch.randn(*s, dtype=torch.bfloat16) * 0.2)(B, Hkv, Sk, Dh)),
            "wq": _w((lambda *s: torch.randn(*s, dtype=torch.bfloat16) * 0.8)(D, Hq * Dh) * (D ** -0.5)),
            "wo": _w((lambda *s: torch.randn(*s, dtype=torch.bfloat16) * 0.2)(Hq * Dh, D) * ((Hq * Dh) ** -0.5)),
        }
    _A_in = _A_setup()

    def _B_setup():
        B, S, D, F = 1, 2048, 512, 1376
        return {
            "x": _w((lambda *s: torch.randn(*s, dtype=torch.bfloat16) * 0.05)(B, S, D)),
            "w1": _w((lambda *s: torch.randn(*s, dtype=torch.bfloat16) * 0.1)(D, F)),
            "b1": _w((lambda *s: torch.randn(*s, dtype=torch.bfloat16) * 0.8)(F)),
            "w2": _w((lambda *s: torch.randn(*s, dtype=torch.bfloat16) * 0.1)(F, D)),
            "b2": _w((lambda *s: torch.randn(*s, dtype=torch.bfloat16) * 6.4)(D)),
        }
    _B_in = _B_setup()

    return {
        "A_x": _A_in["x"],
        "A_k_cache": _A_in["k_cache"],
        "A_v_cache": _A_in["v_cache"],
        "A_wq": _A_in["wq"],
        "A_wo": _A_in["wo"],
        "B_x": _B_in["x"],
        "B_w1": _B_in["w1"],
        "B_b1": _B_in["b1"],
        "B_w2": _B_in["w2"],
        "B_b2": _B_in["b2"],
    }

def reference(A_x, A_k_cache, A_v_cache, A_wq, A_wo, B_x, B_w1, B_b1, B_w2, B_b2):
    def _A_ref(x, k_cache, v_cache, wq, wo):
        B, Sq, D = x.shape
        Hq, Dh = 32, 128
        Hkv, Sk = k_cache.shape[1], k_cache.shape[2]
        W = min(4096, max(256, Sk // 4))
        rep = Hq // Hkv
        q = (lambda x, *d: x.permute(*d))((x @ wq).reshape(B, Sq, Hq, Dh), 0, 2, 1, 3)
        k = (lambda x, n, axis: torch.repeat_interleave(x, n, dim=axis))(k_cache[:, :, Sk - W:, :], rep, axis=1)
        v = (lambda x, n, axis: torch.repeat_interleave(x, n, dim=axis))(v_cache[:, :, Sk - W:, :], rep, axis=1)
        scores = torch.einsum("bhqd,bhkd->bhqk", q, k) * (Dh ** -0.5)
        p = _softmax(scores, axis=-1)
        o = torch.einsum("bhqk,bhkd->bhqd", p, v)
        return (lambda x, *d: x.permute(*d))(o, 0, 2, 1, 3).reshape(B, Sq, Hq * Dh) @ wo

    def _B_ref(x, w1, b1, w2, b2):
        h = torch.einsum("bsd,df->bsf", x, w1) + b1
        zero = torch.zeros_like(h)
        r = torch.where(h > zero, h, zero)
        h = r * r
        out = torch.einsum("bsf,fd->bsd", h, w2) + b2
        return out

    x = A_x
    xn = _layernorm(x, axis=-1)
    s = _A_ref(xn, A_k_cache, A_v_cache, A_wq, A_wo)
    g = _silu(_B_ref(xn, B_w1, B_b1, B_w2, B_b2))
    return x + s * g
```

```cpp
#include <hip/hip_runtime.h>
#include <hip/hip_bf16.h>
typedef unsigned short u16;
__device__ inline float b2f(u16 v){ return __uint_as_float(((unsigned)v)<<16); }
__device__ inline u16 f2b(float f){ unsigned u=__float_as_uint(f); return (u16)((u+0x7FFF+((u>>16)&1))>>16); }
__global__ void ln_k(const u16* x, float* xn){
  int r=blockIdx.x; __shared__ float s[256],s2[256];
  float a=b2f(x[r*512+threadIdx.x]), b=b2f(x[r*512+256+threadIdx.x]);
  s[threadIdx.x]=a+b; __syncthreads();
  for(int o=128;o>0;o>>=1){ if(threadIdx.x<o) s[threadIdx.x]+=s[threadIdx.x+o]; __syncthreads(); }
  float m=s[0]/512.f;
  s2[threadIdx.x]=(a-m)*(a-m)+(b-m)*(b-m); __syncthreads();
  for(int o=128;o>0;o>>=1){ if(threadIdx.x<o) s2[threadIdx.x]+=s2[threadIdx.x+o]; __syncthreads(); }
  float rs=rsqrtf(s2[0]/512.f+1e-5f);
  xn[r*512+threadIdx.x]=(a-m)*rs; xn[r*512+256+threadIdx.x]=(b-m)*rs;
}
__global__ void gemm_k(const float* A,const u16* B,float* C,int M,int N,int K,const u16* bias,int mode){
  __shared__ float As[16][17], Bs[16][17];
  int tx=threadIdx.x&15, ty=threadIdx.x>>4; int row=blockIdx.y*16+ty, col=blockIdx.x*16+tx; float acc=0;
  for(int k=0;k<K;k+=16){ As[ty][tx]=A[row*K+k+tx]; Bs[ty][tx]=b2f(B[(k+ty)*N+col]); __syncthreads();
    for(int j=0;j<16;j++) acc+=As[ty][j]*Bs[j][tx]; __syncthreads(); }
  if(mode==1){ acc+=b2f(bias[col]); acc=acc>0?acc*acc:0; acc=b2f(f2b(acc)); }
  if(mode==0) acc=b2f(f2b(acc));
  C[row*N+col]=acc;
}
__global__ void attn_k(const float* q,const u16* kc,const u16* vc,float* o){
  int r=blockIdx.x,h=blockIdx.y,t=threadIdx.x; __shared__ float qs[128],p[512],red[512];
  if(t<128) qs[t]=q[r*4096+h*128+t]; __syncthreads();
  const u16* kp=kc+((size_t)h*2048+1536+t)*128; float s=0; for(int d=0;d<128;d++) s+=qs[d]*b2f(kp[d]); s*=0.08838834764831845f;
  red[t]=s; __syncthreads(); for(int o2=256;o2>0;o2>>=1){ if(t<o2) red[t]=fmaxf(red[t],red[t+o2]); __syncthreads(); }
  float m=red[0]; __syncthreads(); float e=expf(s-m); p[t]=b2f(f2b(e)); red[t]=e; __syncthreads();
  for(int o2=256;o2>0;o2>>=1){ if(t<o2) red[t]+=red[t+o2]; __syncthreads(); }
  if(t<128){ float a=0; for(int k=0;k<512;k++) a+=p[k]*b2f(vc[((size_t)h*2048+1536+k)*128+t]); o[r*4096+h*128+t]=b2f(f2b(a/red[0])); }
}
__global__ void fin_k(const u16* x,const float* s,const float* f,const u16* b2,u16* out){
  int i=blockIdx.x*256+threadIdx.x; float z=f[i]+b2f(b2[i&511]); float g=z/(1.f+expf(-z)); out[i]=f2b(b2f(x[i])+s[i]*g);
}
extern "C" void kernel_launch(void* const* d_in, const int* in_sizes, int n_in, void* d_out, int out_size, void* d_ws, size_t ws_size, hipStream_t stream){
  const u16* x=(const u16*)d_in[0]; const u16* kc=(const u16*)d_in[1]; const u16* vc=(const u16*)d_in[2]; const u16* wq=(const u16*)d_in[3]; const u16* wo=(const u16*)d_in[4];
  const u16* w1=(const u16*)d_in[6]; const u16* b1=(const u16*)d_in[7]; const u16* w2=(const u16*)d_in[8]; const u16* b2=(const u16*)d_in[9];
  float* xn=(float*)d_ws; float* q=xn+2048*512; float* o=q+2048*4096; float* h=o+2048*4096; float* s=h+2048*1376; float* f=s+2048*512;
  ln_k<<<2048,256,0,stream>>>(x,xn);
  gemm_k<<<dim3(256,128),256,0,stream>>>(xn,wq,q,2048,4096,512,nullptr,0);
  attn_k<<<dim3(2048,32),512,0,stream>>>(q,kc,vc,o);
  gemm_k<<<dim3(32,128),256,0,stream>>>(o,wo,s,2048,512,4096,nullptr,2);
  gemm_k<<<dim3(86,128),256,0,stream>>>(xn,w1,h,2048,1376,512,b1,1);
  gemm_k<<<dim3(32,128),256,0,stream>>>(h,w2,f,2048,512,1376,nullptr,2);
  fin_k<<<4096,256,0,stream>>>(x,s,f,b2,(u16*)d_out);
}
```

```cpp
#include <hip/hip_runtime.h>
#include <hip/hip_bf16.h>

extern "C" __global__ __launch_bounds__(256, 1) void fused_block_kernel(
    const void* x, const void* kc, const void* vc, const void* wq, const void* wo, const void* w1, const void* b1,
    const void* w2, const void* b2, void* out, void* ws) {
  __shared__ __attribute__((aligned(16))) char lds[163840];
  asm volatile(
"s_load_dwordx16 s[20:35], %0, 0\n"
"s_load_dwordx4 s[36:39], %0, 64\n"
"s_load_dwordx2 s[40:41], %0, 80\n"
"v_and_b32 v1, 63, %2\n"
"v_lshrrev_b32 v46, 6, %2\n"
"v_and_b32 v2, 15, v1\n"
"v_readfirstlane_b32 s4, v46\n"
"s_getpc_b64 s[18:19]\n"
"s_bfe_u32 s16, %1, 0x20003\n"
"s_mul_i32 s16, s16, 84\n"
"v_mad_u32_u24 v48, v46, 21, v1\n"
"v_add_lshl_u32 v48, v48, s16, 7\n"
"s_mov_b64 exec, 2097151\n"
"global_load_dword v0, v48, s[18:19]\n"
"s_mov_b64 exec, -1\n"
"v_lshrrev_b32 v3, 4, v1\n"
"v_lshlrev_b32 v4, 4, v1\n"
"s_and_b32 s16, %1, 7\n"
"s_lshr_b32 s17, %1, 3\n"
"s_and_b32 s42, s17, 3\n"
"s_lshl_b32 s5, s16, 2\n"
"s_add_u32 s5, s5, s42\n"
"s_lshr_b32 s6, s17, 2\n"
"s_lshr_b32 s12, s16, 1\n"
"s_and_b32 s16, s16, 1\n"
"s_lshl_b32 s10, s6, 1\n"
"s_add_u32 s10, s10, s16\n"
"s_mov_b32 s11, s42\n"
"s_mov_b32 s13, s5\n"
"s_sub_u32 s42, s13, 11\n"
"s_cmp_lt_u32 s42, 11\n"
"s_cselect_b32 s7, 32, 0\n"
"s_cselect_b32 s9, s42, s13\n"
"s_cmp_lt_u32 s13, 22\n"
"s_cselect_b32 s9, s9, 0\n"
"s_cselect_b32 s70, 50331648, 0\n"
"s_add_u32 s9, s9, 32\n"
"v_lshlrev_b32 v46, 2, v3\n"
"v_xor_b32 v46, v46, v2\n"
"v_lshlrev_b32 v46, 4, v46\n"
"v_lshl_add_u32 v46, v3, 13, v46\n"
"s_lshl_b32 s16, s4, 17\n"
"s_add_u32 s17, s16, 0\n"
"v_add_u32 v14, s17, v46\n"
"s_add_u32 s17, s16, 31744\n"
"v_add_u32 v15, s17, v46\n"
"s_add_u32 s17, s16, 63480\n"
"v_add_u32 v16, s17, v46\n"
"s_add_u32 s17, s16, 95224\n"
"v_add_u32 v17, s17, v46\n"
"v_lshrrev_b32 v46, 5, v1\n"
"v_lshlrev_b32 v46, 1, v46\n"
"v_and_b32 v47, 3, v1\n"
"v_xor_b32 v46, v46, v47\n"
"v_lshlrev_b32 v46, 4, v46\n"
"v_lshrrev_b32 v48, 2, v1\n"
"s_lshl_b32 s16, s4, 4\n"
"v_add_u32 v48, s16, v48\n"
"s_movk_i32 s17, 2752\n"
"v_mad_u32_u24 v18, v48, s17, v46\n"
"v_lshrrev_b32 v46, 3, v1\n"
"v_and_b32 v47, 7, v1\n"
"v_add_u32 v48, 0, v3\n"
"v_xor_b32 v48, v48, v47\n"
"v_lshlrev_b32 v48, 4, v48\n"
"v_lshl_add_u32 v49, v46, 10, v48\n"
"v_add_u32 v48, 4, v3\n"
"v_xor_b32 v48, v48, v47\n"
"v_lshlrev_b32 v48, 4, v48\n"
"v_lshl_add_u32 v50, v46, 10, v48\n"
"s_xor_b32 s16, s7, 0\n"
"s_sub_u32 s16, s16, 0\n"
"s_lshl_b32 s16, s16, 10\n"
"v_add_u32 v6, s16, v49\n"
"s_xor_b32 s16, s7, 8\n"
"s_sub_u32 s16, s16, 1\n"
"s_lshl_b32 s16, s16, 10\n"
"v_add_u32 v7, s16, v50\n"
"s_xor_b32 s16, s7, 16\n"
"s_sub_u32 s16, s16, 2\n"
"s_lshl_b32 s16, s16, 10\n"
"v_add_u32 v8, s16, v49\n"
"s_xor_b32 s16, s7, 24\n"
"s_sub_u32 s16, s16, 3\n"
"s_lshl_b32 s16, s16, 10\n"
"v_add_u32 v9, s16, v50\n"
"s_xor_b32 s16, s7, 32\n"
"s_sub_u32 s16, s16, 0\n"
"s_lshl_b32 s16, s16, 10\n"
"v_add_u32 v10, s16, v49\n"
"s_xor_b32 s16, s7, 40\n"
"s_sub_u32 s16, s16, 1\n"
"s_lshl_b32 s16, s16, 10\n"
"v_add_u32 v11, s16, v50\n"
"s_xor_b32 s16, s7, 48\n"
"s_sub_u32 s16, s16, 2\n"
"s_lshl_b32 s16, s16, 10\n"
"v_add_u32 v12, s16, v49\n"
"s_xor_b32 s16, s7, 56\n"
"s_sub_u32 s16, s16, 3\n"
"s_lshl_b32 s16, s16, 10\n"
"v_add_u32 v13, s16, v50\n"
"s_lshl_b32 s91, s5, 6\n"
"s_lshl_b32 s92, s9, 6\n"
"s_cmp_lt_u32 s13, 22\n"
"s_cselect_b32 s92, s92, 0x40000000\n"
"s_mov_b32 s90, 0\n"
"s_lshl_b32 s80, s90, 7\n"
"s_lshl_b32 s81, s90, 19\n"
"s_mul_i32 s83, s90, 176128\n"
"s_add_u32 s82, s83, s91\n"
"s_add_u32 s83, s83, s92\n"
"s_lshl_b32 s84, s4, 13\n"
"s_mul_i32 s85, s4, 4112\n"
"s_add_u32 s85, s85, 65536\n"
"s_mul_i32 s16, s4, 3088\n"
"s_sub_u32 s86, 16448, s16\n"
"s_lshl_b32 s16, s6, 8\n"
"s_lshl_b32 s17, s5, 3\n"
"s_add_u32 s16, s16, s17\n"
"s_lshl_b32 s17, s4, 1\n"
"s_add_u32 s43, s16, s17\n"
"v_mov_b32 v188, 24199168\n"
"s_lshl_b32 s17, s43, 10\n"
"s_lshl_b32 s16, s6, 2\n"
"s_add_u32 s16, s16, s4\n"
"s_lshl_b32 s16, s16, 16\n"
"s_lshl_b32 s42, s5, 8\n"
"s_waitcnt lgkmcnt(0)\n"
"s_add_u32 s56, s20, s17\n"
"s_addc_u32 s57, s21, 0\n"
"s_mov_b32 s58, 2048\n"
"s_mov_b32 s59, 0x20000\n"
"buffer_load_dwordx4 v[46:49], v4, s[56:59], 0 offen\n"
"buffer_load_dwordx4 v[50:53], v4, s[56:59], 0 offen offset:1024\n"
"s_mov_b32 s64, s40\n"
"s_mov_b32 s65, s41\n"
"s_mov_b32 s66, 50331648\n"
"s_mov_b32 s67, 0x20000\n"
"buffer_load_dword v188, v188, s[64:67], 0 offen sc1\n"
"s_add_u32 s48, s26, s42\n"
"s_addc_u32 s49, s27, 0\n"
"s_mov_b32 s50, 4186368\n"
"s_mov_b32 s51, 0x20000\n"
"s_add_u32 s44, s20, s16\n"
"s_addc_u32 s45, s21, 0\n"
"s_mov_b32 s46, 65536\n"
"s_mov_b32 s47, 0x20000\n"
"s_mov_b32 s52, s30\n"
"s_mov_b32 s53, s31\n"
"s_mov_b32 s54, 1409024\n"
"s_mov_b32 s55, 0x20000\n"
"s_mov_b32 m0, s84\n"
"s_nop 0\n"
"buffer_load_dwordx4 v6, s[44:47], s80 offen offset:0 lds\n"
"v_lshrrev_b32 v54, 1, v2\n"
"s_lshl_b32 s16, s4, 13\n"
"v_add_u32 v55, 0, v3\n"
"v_xor_b32 v55, v55, v54\n"
"v_lshlrev_b32 v55, 4, v55\n"
"v_lshl_add_u32 v55, v2, 7, v55\n"
"v_add_u32 v19, s16, v55\n"
"v_add_u32 v55, 4, v3\n"
"v_xor_b32 v55, v55, v54\n"
"v_lshlrev_b32 v55, 4, v55\n"
"buffer_load_dwordx4 v7, s[44:47], s80 offen offset:1024 lds\n"
"v_lshl_add_u32 v55, v2, 7, v55\n"
"v_add_u32 v20, s16, v55\n"
"s_mov_b32 s42, 65536\n"
"v_bfe_u32 v56, v1, 2, 2\n"
"v_and_b32 v57, 3, v1\n"
"v_lshl_add_u32 v54, v3, 3, v56\n"
"v_lshrrev_b32 v60, 1, v3\n"
"v_mul_u32_u24 v60, 4112, v60\n"
"v_and_b32 v59, 1, v3\n"
"v_mul_u32_u24 v61, 2056, v59\n"
"buffer_load_dwordx4 v8, s[44:47], s80 offen offset:2048 lds\n"
"v_lshl_add_u32 v61, v56, 8, v61\n"
"v_add3_u32 v60, v60, v61, s42\n"
"s_xor_b32 s16, s4, 0\n"
"v_xor_b32 v55, s16, v56\n"
"v_lshl_add_u32 v55, v55, 2, v57\n"
"v_lshl_add_u32 v21, v55, 4, v60\n"
"s_xor_b32 s16, s4, 1\n"
"v_xor_b32 v55, s16, v56\n"
"v_lshl_add_u32 v55, v55, 2, v57\n"
"v_lshl_add_u32 v22, v55, 4, v60\n"
"buffer_load_dwordx4 v9, s[44:47], s80 offen offset:3072 lds\n"
"s_xor_b32 s16, s4, 2\n"
"v_xor_b32 v55, s16, v56\n"
"v_lshl_add_u32 v55, v55, 2, v57\n"
"v_lshl_add_u32 v23, v55, 4, v60\n"
"s_xor_b32 s16, s4, 3\n"
"v_xor_b32 v55, s16, v56\n"
"v_lshl_add_u32 v55, v55, 2, v57\n"
"v_lshl_add_u32 v24, v55, 4, v60\n"
"v_lshlrev_b32 v58, 3, v57\n"
"v_lshl_add_u32 v58, v54, 6, v58\n"
"s_add_u32 m0, s84, 4096\n"
"s_xor_b32 s84, s84, 0x8000\n"
"buffer_load_dwordx4 v10, s[44:47], s80 offen offset:0 lds\n"
"v_xor_b32 v55, 0, v59\n"
"v_lshl_add_u32 v55, v55, 5, v58\n"
"v_add_u32 v25, 81984, v55\n"
"v_xor_b32 v55, 1, v59\n"
"v_lshl_add_u32 v55, v55, 5, v58\n"
"v_add_u32 v26, 81984, v55\n"
"s_and_b32 s16, s4, 1\n"
"v_xor_b32 v55, s16, v59\n"
"v_lshl_add_u32 v55, v55, 5, v58\n"
"s_lshr_b32 s16, s4, 1\n"
"s_lshl_b32 s16, s16, 12\n"
"buffer_load_dwordx4 v11, s[44:47], s80 offen offset:1024 lds\n"
"s_add_u32 s16, s16, 81984\n"
"v_add_u32 v27, s16, v55\n"
"s_lshl_b32 s17, s4, 12\n"
"v_lshl_add_u32 v54, v3, 8, s17\n"
"v_add_u32 v55, 0, v3\n"
"v_xor_b32 v55, v55, v2\n"
"v_lshl_add_u32 v28, v55, 4, v54\n"
"v_add_u32 v55, 4, v3\n"
"v_xor_b32 v55, v55, v2\n"
"v_lshl_add_u32 v29, v55, 4, v54\n"
"buffer_load_dwordx4 v12, s[44:47], s80 offen offset:2048 lds\n"
"v_add_u32 v55, 8, v3\n"
"v_xor_b32 v55, v55, v2\n"
"v_lshl_add_u32 v30, v55, 4, v54\n"
"v_add_u32 v55, 12, v3\n"
"v_xor_b32 v55, v55, v2\n"
"v_lshl_add_u32 v31, v55, 4, v54\n"
"v_add_u32 v55, 0, v3\n"
"v_lshlrev_b32 v55, 1, v55\n"
"v_xor_b32 v55, v55, v2\n"
"v_lshl_add_u32 v32, v55, 4, v54\n"
"buffer_load_dwordx4 v13, s[44:47], s80 offen offset:3072 lds\n"
"v_add_u32 v55, 4, v3\n"
"v_lshlrev_b32 v55, 1, v55\n"
"v_xor_b32 v55, v55, v2\n"
"v_lshl_add_u32 v33, v55, 4, v54\n"
"s_xor_b32 s16, s4, 0\n"
"s_lshl_b32 s16, s16, 2\n"
"v_add_u32 v55, s16, v3\n"
"v_xor_b32 v55, v55, v2\n"
"v_lshlrev_b32 v55, 4, v55\n"
"v_lshl_add_u32 v55, v2, 8, v55\n"
"s_mov_b32 m0, s85\n"
"s_add_u32 s90, s90, 1\n"
"buffer_load_dwordx4 v14, s[48:51], s81 offen lds\n"
"s_and_b32 s90, s90, 7\n"
"v_add_u32 v34, 131072, v55\n"
"s_xor_b32 s16, s4, 1\n"
"s_lshl_b32 s16, s16, 2\n"
"v_add_u32 v55, s16, v3\n"
"v_xor_b32 v55, v55, v2\n"
"v_lshlrev_b32 v55, 4, v55\n"
"v_lshl_add_u32 v55, v2, 8, v55\n"
"v_add_u32 v35, 131072, v55\n"
"s_xor_b32 s16, s4, 2\n"
"s_lshl_b32 s16, s16, 2\n"
"buffer_load_dwordx4 v15, s[48:51], s81 offen offset:1024 lds\n"
"s_lshl_b32 s80, s90, 7\n"
"v_add_u32 v55, s16, v3\n"
"v_xor_b32 v55, v55, v2\n"
"v_lshlrev_b32 v55, 4, v55\n"
"v_lshl_add_u32 v55, v2, 8, v55\n"
"v_add_u32 v36, 131072, v55\n"
"s_xor_b32 s16, s4, 3\n"
"s_lshl_b32 s16, s16, 2\n"
"v_add_u32 v55, s16, v3\n"
"v_xor_b32 v55, v55, v2\n"
"v_lshlrev_b32 v55, 4, v55\n"
"v_lshl_add_u32 v55, v2, 8, v55\n"
"buffer_load_dwordx4 v16, s[48:51], s81 offen offset:2056 lds\n"
"v_add_u32 v37, 131072, v55\n"
"v_bfe_u32 v56, v1, 2, 2\n"
"v_and_b32 v57, 3, v1\n"
"v_lshl_add_u32 v54, v3, 2, v56\n"
"v_and_b32 v59, 7, v54\n"
"v_lshlrev_b32 v58, 3, v57\n"
"v_lshl_add_u32 v58, v54, 8, v58\n"
"v_add_u32 v58, 147456, v58\n"
"v_xor_b32 v55, 0, v59\n"
"v_lshl_add_u32 v38, v55, 5, v58\n"
"buffer_load_dwordx4 v17, s[48:51], s81 offen offset:3080 lds\n"
"v_xor_b32 v55, 1, v59\n"
"v_lshl_add_u32 v39, v55, 5, v58\n"
"v_xor_b32 v55, 2, v59\n"
"v_lshl_add_u32 v40, v55, 5, v58\n"
"v_xor_b32 v55, 3, v59\n"
"v_lshl_add_u32 v41, v55, 5, v58\n"
"v_xor_b32 v55, 4, v59\n"
"v_lshl_add_u32 v42, v55, 5, v58\n"
"v_xor_b32 v55, 5, v59\n"
"v_lshl_add_u32 v43, v55, 5, v58\n"
"s_add_u32 m0, s85, s86\n"
"s_lshl_b32 s81, s90, 19\n"
"buffer_load_dwordx4 v18, s[52:55], s82 offen lds\n"
"s_xor_b32 s85, s85, 0x8000\n"
"v_xor_b32 v55, 6, v59\n"
"v_lshl_add_u32 v44, v55, 5, v58\n"
"v_xor_b32 v55, 7, v59\n"
"v_lshl_add_u32 v45, v55, 5, v58\n"
"s_mov_b32 s68, s40\n"
"s_mov_b32 s69, s41\n"
"s_mov_b32 s71, 0x20000\n"
"s_lshl_b32 s16, s5, 19\n"
"s_add_u32 s16, s16, 393216\n"
"s_add_u32 s56, s22, s16\n"
"s_addc_u32 s57, s23, 0\n"
"s_add_u32 m0, m0, 4096\n"
"s_mul_i32 s82, s90, 176128\n"
"buffer_load_dwordx4 v18, s[52:55], s83 offen lds\n"
"s_add_u32 s83, s82, s92\n"
"s_add_u32 s82, s82, s91\n"
"s_mov_b32 s58, 131072\n"
"s_mov_b32 s59, 0x20000\n"
"s_add_u32 s60, s24, s16\n"
"s_addc_u32 s61, s25, 0\n"
"s_mov_b32 s62, 131072\n"
"s_mov_b32 s63, 0x20000\n"
"s_lshl_b32 s16, s4, 12\n"
"s_add_u32 s88, s16, 131072\n"
"s_add_u32 s89, s16, 147456\n"
"s_mov_b32 s87, 0\n"
"s_mov_b32 m0, s84\n"
"s_nop 0\n"
"buffer_load_dwordx4 v6, s[44:47], s80 offen offset:0 lds\n"
"v_mov_b32 v64, 0\n"
"v_mov_b32 v65, 0\n"
"v_mov_b32 v66, 0\n"
"v_mov_b32 v67, 0\n"
"v_mov_b32 v68, 0\n"
"v_mov_b32 v69, 0\n"
"v_mov_b32 v70, 0\n"
"v_mov_b32 v71, 0\n"
"v_mov_b32 v72, 0\n"
"v_mov_b32 v73, 0\n"
"v_mov_b32 v74, 0\n"
"v_mov_b32 v75, 0\n"
"v_mov_b32 v76, 0\n"
"v_mov_b32 v77, 0\n"
"v_mov_b32 v78, 0\n"
"v_mov_b32 v79, 0\n"
"v_mov_b32 v80, 0\n"
"v_mov_b32 v81, 0\n"
"v_mov_b32 v82, 0\n"
"v_mov_b32 v83, 0\n"
"v_mov_b32 v84, 0\n"
"v_mov_b32 v85, 0\n"
"v_mov_b32 v86, 0\n"
"buffer_load_dwordx4 v7, s[44:47], s80 offen offset:1024 lds\n"
"v_mov_b32 v87, 0\n"
"v_mov_b32 v88, 0\n"
"v_mov_b32 v89, 0\n"
"v_mov_b32 v90, 0\n"
"v_mov_b32 v91, 0\n"
"v_mov_b32 v92, 0\n"
"v_mov_b32 v93, 0\n"
"v_mov_b32 v94, 0\n"
"v_mov_b32 v95, 0\n"
"v_mov_b32 v96, 0\n"
"v_mov_b32 v97, 0\n"
"v_mov_b32 v98, 0\n"
"v_mov_b32 v99, 0\n"
"v_mov_b32 v100, 0\n"
"v_mov_b32 v101, 0\n"
"v_mov_b32 v102, 0\n"
"v_mov_b32 v103, 0\n"
"v_mov_b32 v104, 0\n"
"v_mov_b32 v105, 0\n"
"v_mov_b32 v106, 0\n"
"v_mov_b32 v107, 0\n"
"v_mov_b32 v108, 0\n"
"v_mov_b32 v109, 0\n"
"buffer_load_dwordx4 v8, s[44:47], s80 offen offset:2048 lds\n"
"v_mov_b32 v110, 0\n"
"v_mov_b32 v111, 0\n"
"v_mov_b32 v112, 0\n"
"v_mov_b32 v113, 0\n"
"v_mov_b32 v114, 0\n"
"v_mov_b32 v115, 0\n"
"v_mov_b32 v116, 0\n"
"v_mov_b32 v117, 0\n"
"v_mov_b32 v118, 0\n"
"v_mov_b32 v119, 0\n"
"v_mov_b32 v120, 0\n"
"v_mov_b32 v121, 0\n"
"v_mov_b32 v122, 0\n"
"v_mov_b32 v123, 0\n"
"v_mov_b32 v124, 0\n"
"v_mov_b32 v125, 0\n"
"v_mov_b32 v126, 0\n"
"v_mov_b32 v127, 0\n"
"v_mov_b32 v128, 0\n"
"v_mov_b32 v129, 0\n"
"v_mov_b32 v130, 0\n"
"v_mov_b32 v131, 0\n"
"v_mov_b32 v132, 0\n"
"buffer_load_dwordx4 v9, s[44:47], s80 offen offset:3072 lds\n"
"v_mov_b32 v133, 0\n"
"v_mov_b32 v134, 0\n"
"v_mov_b32 v135, 0\n"
"v_mov_b32 v136, 0\n"
"v_mov_b32 v137, 0\n"
"v_mov_b32 v138, 0\n"
"v_mov_b32 v139, 0\n"
"v_mov_b32 v140, 0\n"
"v_mov_b32 v141, 0\n"
"v_mov_b32 v142, 0\n"
"v_mov_b32 v143, 0\n"
"v_mov_b32 v144, 0\n"
"v_mov_b32 v145, 0\n"
"v_mov_b32 v146, 0\n"
"v_mov_b32 v147, 0\n"
"v_mov_b32 v148, 0\n"
"v_mov_b32 v149, 0\n"
"v_mov_b32 v150, 0\n"
"v_mov_b32 v151, 0\n"
"v_mov_b32 v152, 0\n"
"v_mov_b32 v153, 0\n"
"v_mov_b32 v154, 0\n"
"v_mov_b32 v155, 0\n"
"s_add_u32 m0, s84, 4096\n"
"s_xor_b32 s84, s84, 0x8000\n"
"buffer_load_dwordx4 v10, s[44:47], s80 offen offset:0 lds\n"
"v_mov_b32 v156, 0\n"
"v_mov_b32 v157, 0\n"
"v_mov_b32 v158, 0\n"
"v_mov_b32 v159, 0\n"
"s_waitcnt vmcnt(19)\n"
"v_readfirstlane_b32 s8, v188\n"
"v_lshlrev_b32 v54, 16, v46\n"
"v_and_b32 v55, 0xffff0000, v46\n"
"v_lshlrev_b32 v56, 16, v47\n"
"v_and_b32 v57, 0xffff0000, v47\n"
"v_lshlrev_b32 v58, 16, v48\n"
"v_and_b32 v59, 0xffff0000, v48\n"
"v_lshlrev_b32 v60, 16, v49\n"
"v_and_b32 v61, 0xffff0000, v49\n"
"s_add_u32 s8, s8, 1\n"
"v_lshlrev_b32 v46, 16, v50\n"
"v_and_b32 v47, 0xffff0000, v50\n"
"v_lshlrev_b32 v48, 16, v51\n"
"v_and_b32 v49, 0xffff0000, v51\n"
"v_lshlrev_b32 v188, 16, v52\n"
"v_and_b32 v189, 0xffff0000, v52\n"
"v_lshlrev_b32 v190, 16, v53\n"
"v_and_b32 v191, 0xffff0000, v53\n"
"v_add_f32 v50, v54, v55\n"
"buffer_load_dwordx4 v11, s[44:47], s80 offen offset:1024 lds\n"
"v_add_f32 v51, v46, v47\n"
"v_add_f32 v50, v50, v56\n"
"v_add_f32 v51, v51, v48\n"
"v_add_f32 v50, v50, v57\n"
"v_add_f32 v51, v51, v49\n"
"v_add_f32 v50, v50, v58\n"
"v_add_f32 v51, v51, v188\n"
"v_add_f32 v50, v50, v59\n"
"v_add_f32 v51, v51, v189\n"
"v_add_f32 v50, v50, v60\n"
"v_add_f32 v51, v51, v190\n"
"v_add_f32 v50, v50, v61\n"
"v_add_f32 v51, v51, v191\n"
"s_nop 0\n"
"v_add_f32_dpp v50, v50, v50 quad_perm:[1,0,3,2] row_mask:0xf bank_mask:0xf\n"
"v_add_f32_dpp v51, v51, v51 quad_perm:[1,0,3,2] row_mask:0xf bank_mask:0xf\n"
"s_nop 0\n"
"v_add_f32_dpp v50, v50, v50 quad_perm:[2,3,0,1] row_mask:0xf bank_mask:0xf\n"
"v_add_f32_dpp v51, v51, v51 quad_perm:[2,3,0,1] row_mask:0xf bank_mask:0xf\n"
"s_nop 0\n"
"v_add_f32_dpp v50, v50, v50 row_half_mirror row_mask:0xf bank_mask:0xf\n"
"v_add_f32_dpp v51, v51, v51 row_half_mirror row_mask:0xf bank_mask:0xf\n"
"s_nop 0\n"
"buffer_load_dwordx4 v12, s[44:47], s80 offen offset:2048 lds\n"
"v_add_f32_dpp v50, v50, v50 row_mirror row_mask:0xf bank_mask:0xf\n"
"v_add_f32_dpp v51, v51, v51 row_mirror row_mask:0xf bank_mask:0xf\n"
"s_nop 0\n"
"v_mov_b32 v52, v50\n"
"v_mov_b32 v53, v51\n"
"s_nop 0\n"
"v_permlane16_swap_b32 v50, v52\n"
"v_permlane16_swap_b32 v51, v53\n"
"v_add_f32 v50, v50, v52\n"
"v_add_f32 v51, v51, v53\n"
"v_mov_b32 v52, v50\n"
"v_mov_b32 v53, v51\n"
"s_nop 0\n"
"v_permlane32_swap_b32 v50, v52\n"
"v_permlane32_swap_b32 v51, v53\n"
"v_add_f32 v50, v50, v52\n"
"v_add_f32 v51, v51, v53\n"
"v_mul_f32 v50, 0.001953125, v50\n"
"v_mul_f32 v51, 0.001953125, v51\n"
"v_sub_f32 v54, v54, v50\n"
"v_sub_f32 v46, v46, v51\n"
"v_sub_f32 v55, v55, v50\n"
"v_sub_f32 v47, v47, v51\n"
"v_sub_f32 v56, v56, v50\n"
"buffer_load_dwordx4 v13, s[44:47], s80 offen offset:3072 lds\n"
"v_sub_f32 v48, v48, v51\n"
"v_sub_f32 v57, v57, v50\n"
"v_sub_f32 v49, v49, v51\n"
"v_sub_f32 v58, v58, v50\n"
"v_sub_f32 v188, v188, v51\n"
"v_sub_f32 v59, v59, v50\n"
"v_sub_f32 v189, v189, v51\n"
"v_sub_f32 v60, v60, v50\n"
"v_sub_f32 v190, v190, v51\n"
"v_sub_f32 v61, v61, v50\n"
"v_sub_f32 v191, v191, v51\n"
"v_mul_f32 v54, v54, v54\n"
"v_mul_f32 v46, v46, v46\n"
"v_fmac_f32 v54, v55, v55\n"
"v_fmac_f32 v46, v47, v47\n"
"v_fmac_f32 v54, v56, v56\n"
"v_fmac_f32 v46, v48, v48\n"
"v_fmac_f32 v54, v57, v57\n"
"v_fmac_f32 v46, v49, v49\n"
"v_fmac_f32 v54, v58, v58\n"
"v_fmac_f32 v46, v188, v188\n"
"v_fmac_f32 v54, v59, v59\n"
"v_fmac_f32 v46, v189, v189\n"
"s_mov_b32 m0, s85\n"
"s_add_u32 s90, s90, 1\n"
"buffer_load_dwordx4 v14, s[48:51], s81 offen lds\n"
"s_and_b32 s90, s90, 7\n"
"v_fmac_f32 v54, v60, v60\n"
"v_fmac_f32 v46, v190, v190\n"
"v_fmac_f32 v54, v61, v61\n"
"v_fmac_f32 v46, v191, v191\n"
"s_nop 0\n"
"v_add_f32_dpp v54, v54, v54 quad_perm:[1,0,3,2] row_mask:0xf bank_mask:0xf\n"
"v_add_f32_dpp v46, v46, v46 quad_perm:[1,0,3,2] row_mask:0xf bank_mask:0xf\n"
"s_nop 0\n"
"v_add_f32_dpp v54, v54, v54 quad_perm:[2,3,0,1] row_mask:0xf bank_mask:0xf\n"
"v_add_f32_dpp v46, v46, v46 quad_perm:[2,3,0,1] row_mask:0xf bank_mask:0xf\n"
"s_nop 0\n"
"v_add_f32_dpp v54, v54, v54 row_half_mirror row_mask:0xf bank_mask:0xf\n"
"v_add_f32_dpp v46, v46, v46 row_half_mirror row_mask:0xf bank_mask:0xf\n"
"s_nop 0\n"
"v_add_f32_dpp v54, v54, v54 row_mirror row_mask:0xf bank_mask:0xf\n"
"v_add_f32_dpp v46, v46, v46 row_mirror row_mask:0xf bank_mask:0xf\n"
"s_nop 0\n"
"v_mov_b32 v52, v54\n"
"v_mov_b32 v53, v46\n"
"s_nop 0\n"
"v_permlane16_swap_b32 v54, v52\n"
"v_permlane16_swap_b32 v46, v53\n"
"v_add_f32 v54, v54, v52\n"
"buffer_load_dwordx4 v15, s[48:51], s81 offen offset:1024 lds\n"
"s_lshl_b32 s80, s90, 7\n"
"v_add_f32 v46, v46, v53\n"
"v_mov_b32 v52, v54\n"
"v_mov_b32 v53, v46\n"
"s_nop 0\n"
"v_permlane32_swap_b32 v54, v52\n"
"v_permlane32_swap_b32 v46, v53\n"
"v_add_f32 v54, v54, v52\n"
"v_add_f32 v46, v46, v53\n"
"v_cmp_eq_u32 vcc, 0, v1\n"
"v_mov_b32 v189, 0x3727c5ac\n"
"s_lshl_b32 s16, s43, 4\n"
"s_nop 0\n"
"v_cndmask_b32 v190, v46, v54, vcc\n"
"v_cndmask_b32 v188, v51, v50, vcc\n"
"v_fmac_f32 v189, 0.001953125, v190\n"
"v_mov_b32 v191, s8\n"
"v_rsq_f32 v190, v189\n"
"v_mov_b32 v189, s8\n"
"s_add_u32 s16, s16, 24117248\n"
"v_mov_b32 v160, 0\n"
"v_mov_b32 v161, 0\n"
"v_mov_b32 v162, 0\n"
"v_mov_b32 v163, 0\n"
"buffer_load_dwordx4 v16, s[48:51], s81 offen offset:2056 lds\n"
"v_mov_b32 v164, 0\n"
"v_mov_b32 v165, 0\n"
"v_mov_b32 v166, 0\n"
"v_mov_b32 v167, 0\n"
"v_mov_b32 v168, 0\n"
"v_mov_b32 v169, 0\n"
"v_mov_b32 v170, 0\n"
"v_mov_b32 v171, 0\n"
"v_mov_b32 v172, 0\n"
"v_mov_b32 v173, 0\n"
"v_mov_b32 v174, 0\n"
"v_mov_b32 v175, 0\n"
"v_mov_b32 v176, 0\n"
"v_mov_b32 v177, 0\n"
"v_mov_b32 v178, 0\n"
"v_mov_b32 v179, 0\n"
"v_mov_b32 v180, 0\n"
"v_mov_b32 v181, 0\n"
"v_mov_b32 v182, 0\n"
"v_mov_b32 v183, 0\n"
"v_mov_b32 v184, 0\n"
"v_mov_b32 v185, 0\n"
"v_mov_b32 v186, 0\n"
"buffer_load_dwordx4 v17, s[48:51], s81 offen offset:3080 lds\n"
"v_mov_b32 v187, 0\n"
"v_mov_b32 v192, 0\n"
"v_mov_b32 v193, 0\n"
"v_mov_b32 v194, 0\n"
"v_mov_b32 v195, 0\n"
"v_mov_b32 v196, 0\n"
"v_mov_b32 v197, 0\n"
"v_mov_b32 v198, 0\n"
"v_mov_b32 v199, 0\n"
"v_mov_b32 v200, 0\n"
"v_mov_b32 v201, 0\n"
"v_mov_b32 v202, 0\n"
"v_mov_b32 v203, 0\n"
"v_mov_b32 v204, 0\n"
"v_mov_b32 v205, 0\n"
"v_mov_b32 v206, 0\n"
"v_mov_b32 v207, 0\n"
"v_mov_b32 v208, 0\n"
"v_mov_b32 v209, 0\n"
"v_mov_b32 v210, 0\n"
"v_mov_b32 v211, 0\n"
"v_mov_b32 v212, 0\n"
"v_mov_b32 v213, 0\n"
"s_add_u32 m0, s85, s86\n"
"s_lshl_b32 s81, s90, 19\n"
"buffer_load_dwordx4 v18, s[52:55], s82 offen lds\n"
"s_xor_b32 s85, s85, 0x8000\n"
"v_mov_b32 v214, 0\n"
"v_mov_b32 v215, 0\n"
"v_mov_b32 v216, 0\n"
"v_mov_b32 v217, 0\n"
"v_mov_b32 v218, 0\n"
"v_mov_b32 v219, 0\n"
"v_mov_b32 v220, 0\n"
"v_mov_b32 v221, 0\n"
"v_mov_b32 v222, 0\n"
"v_mov_b32 v223, 0\n"
"v_mov_b32 v224, 0\n"
"v_mov_b32 v225, 0\n"
"v_mov_b32 v226, 0\n"
"v_mov_b32 v227, 0\n"
"v_mov_b32 v228, 0\n"
"v_mov_b32 v229, 0\n"
"v_mov_b32 v230, 0\n"
"v_mov_b32 v231, 0\n"
"v_mov_b32 v232, 0\n"
"v_mov_b32 v233, 0\n"
"v_mov_b32 v234, 0\n"
"v_mov_b32 v235, 0\n"
"v_mov_b32 v236, 0\n"
"s_add_u32 m0, m0, 4096\n"
"s_mul_i32 s82, s90, 176128\n"
"buffer_load_dwordx4 v18, s[52:55], s83 offen lds\n"
"s_add_u32 s83, s82, s92\n"
"s_add_u32 s82, s82, s91\n"
"v_mov_b32 v237, 0\n"
"v_mov_b32 v238, 0\n"
"v_mov_b32 v239, 0\n"
"v_mov_b32 v240, 0\n"
"v_mov_b32 v241, 0\n"
"v_mov_b32 v242, 0\n"
"v_mov_b32 v243, 0\n"
"v_mov_b32 v244, 0\n"
"v_mov_b32 v245, 0\n"
"v_mov_b32 v246, 0\n"
"v_mov_b32 v247, 0\n"
"v_mov_b32 v248, 0\n"
"v_mov_b32 v249, 0\n"
"v_mov_b32 v250, 0\n"
"v_mov_b32 v251, 0\n"
"v_mov_b32 v252, 0\n"
"v_mov_b32 v253, 0\n"
"v_mov_b32 v254, 0\n"
"v_mov_b32 v255, 0\n"
"v_mov_b32 v5, 0x3f803f80\n"
"v_accvgpr_write_b32 a136, v5\n"
"v_accvgpr_write_b32 a137, v5\n"
"v_accvgpr_write_b32 a138, v5\n"
"v_accvgpr_write_b32 a139, v5\n"
"s_mov_b64 exec, 3\n"
"buffer_store_dwordx4 v[188:191], v4, s[64:67], s16 offen sc1\n"
"s_mov_b64 exec, -1\n"
"v_lshlrev_b32 v5, 16, v1\n"
"s_mov_b64 exec, 3\n"
"buffer_load_dword v0, v5, s[56:59], 0 offen\n"
"buffer_load_dword v0, v5, s[60:63], 0 offen\n"
"s_mov_b64 exec, -1\n"
"s_waitcnt vmcnt(17)\n"
"s_barrier\n"
"ds_read_b64_tr_b16 a[0:1], v21 offset:0\n"
"ds_read_b64_tr_b16 a[2:3], v21 offset:1024\n"
"ds_read_b64_tr_b16 a[4:5], v21 offset:8\n"
"ds_read_b64_tr_b16 a[6:7], v21 offset:1032\n"
"ds_read_b64_tr_b16 a[48:49], v27 offset:0\n"
"ds_read_b64_tr_b16 a[50:51], v27 offset:256\n"
"ds_read_b128 a[104:107], v19 offset:0\n"
"ds_read_b128 a[108:111], v19 offset:2048\n"
"ds_read_b128 a[112:115], v19 offset:4096\n"
"ds_read_b128 a[116:119], v19 offset:6144\n"
"ds_read_b64_tr_b16 a[8:9], v22 offset:0\n"
"ds_read_b64_tr_b16 a[10:11], v22 offset:1024\n"
"ds_read_b64_tr_b16 a[12:13], v22 offset:8\n"
"ds_read_b64_tr_b16 a[14:15], v22 offset:1032\n"
"ds_read_b64_tr_b16 a[16:17], v23 offset:0\n"
"ds_read_b64_tr_b16 a[18:19], v23 offset:1024\n"
"ds_read_b64_tr_b16 a[20:21], v23 offset:8\n"
"ds_read_b64_tr_b16 a[22:23], v23 offset:1032\n"
"ds_read_b64_tr_b16 a[24:25], v24 offset:0\n"
"ds_read_b64_tr_b16 a[26:27], v24 offset:1024\n"
"ds_read_b64_tr_b16 a[28:29], v24 offset:8\n"
"ds_read_b64_tr_b16 a[30:31], v24 offset:1032\n"
"ds_read_b64_tr_b16 a[32:33], v25 offset:0\n"
"ds_read_b64_tr_b16 a[34:35], v25 offset:256\n"
"ds_read_b64_tr_b16 a[36:37], v26 offset:0\n"
"ds_read_b64_tr_b16 a[38:39], v26 offset:256\n"
"ds_read_b64_tr_b16 a[40:41], v25 offset:4096\n"
"ds_read_b64_tr_b16 a[42:43], v25 offset:4352\n"
"ds_read_b64_tr_b16 a[44:45], v26 offset:4096\n"
"ds_read_b64_tr_b16 a[46:47], v26 offset:4352\n"
"s_mov_b32 s13, 6\n"
"LPROJ_%=:\n"
"s_waitcnt lgkmcnt(15)\n"
"v_mfma_f32_16x16x32_bf16 v[176:179], a[0:3], a[136:139], v[176:179]\n"
"ds_read_b64_tr_b16 a[52:53], v21 offset:8224\n"
"v_mfma_f32_16x16x32_bf16 v[180:183], a[4:7], a[136:139], v[180:183]\n"
"ds_read_b64_tr_b16 a[54:55], v21 offset:9248\n"
"v_mfma_f32_16x16x32_bf16 v[184:187], a[48:51], a[136:139], v[184:187]\n"
"ds_read_b64_tr_b16 a[56:57], v21 offset:8232\n"
"v_mfma_f32_16x16x32_bf16 v[64:67], a[0:3], a[104:107], v[64:67]\n"
"ds_read_b64_tr_b16 a[58:59], v21 offset:9256\n"
"v_mfma_f32_16x16x32_bf16 v[80:83], a[4:7], a[104:107], v[80:83]\n"
"ds_read_b64_tr_b16 a[100:101], v27 offset:2048\n"
"v_mfma_f32_16x16x32_bf16 v[96:99], a[8:11], a[104:107], v[96:99]\n"
"ds_read_b64_tr_b16 a[102:103], v27 offset:2304\n"
"v_mfma_f32_16x16x32_bf16 v[112:115], a[12:15], a[104:107], v[112:115]\n"
"s_waitcnt lgkmcnt(8)\n"
"ds_read_b128 a[120:123], v20 offset:0\n"
"v_mfma_f32_16x16x32_bf16 v[192:195], a[16:19], a[104:107], v[192:195]\n"
"ds_read_b128 a[124:127], v20 offset:2048\n"
"v_mfma_f32_16x16x32_bf16 v[208:211], a[20:23], a[104:107], v[208:211]\n"
"ds_read_b128 a[128:131], v20 offset:4096\n"
"v_mfma_f32_16x16x32_bf16 v[224:227], a[24:27], a[104:107], v[224:227]\n"
"ds_read_b128 a[132:135], v20 offset:6144\n"
"v_mfma_f32_16x16x32_bf16 v[240:243], a[28:31], a[104:107], v[240:243]\n"
"ds_read_b64_tr_b16 a[60:61], v22 offset:8224\n"
"v_mfma_f32_16x16x32_bf16 v[128:131], a[32:35], a[104:107], v[128:131]\n"
"ds_read_b64_tr_b16 a[62:63], v22 offset:9248\n"
"v_mfma_f32_16x16x32_bf16 v[144:147], a[36:39], a[104:107], v[144:147]\n"
"ds_read_b64_tr_b16 a[64:65], v22 offset:8232\n"
"v_mfma_f32_16x16x32_bf16 v[160:163], a[40:43], a[104:107], v[160:163]\n"
"ds_read_b64_tr_b16 a[66:67], v22 offset:9256\n"
"s_waitcnt lgkmcnt(4)\n"
"v_mfma_f32_16x16x32_bf16 v[168:171], a[44:47], a[104:107], v[168:171]\n"
"s_mov_b32 m0, s84\n"
"ds_read_b64_tr_b16 a[68:69], v23 offset:8224\n"
"buffer_load_dwordx4 v6, s[44:47], s80 offen offset:0 lds\n"
"v_mfma_f32_16x16x32_bf16 v[68:71], a[0:3], a[108:111], v[68:71]\n"
"ds_read_b64_tr_b16 a[70:71], v23 offset:9248\n"
"v_mfma_f32_16x16x32_bf16 v[84:87], a[4:7], a[108:111], v[84:87]\n"
"ds_read_b64_tr_b16 a[72:73], v23 offset:8232\n"
"v_mfma_f32_16x16x32_bf16 v[100:103], a[8:11], a[108:111], v[100:103]\n"
"ds_read_b64_tr_b16 a[74:75], v23 offset:9256\n"
"v_mfma_f32_16x16x32_bf16 v[116:119], a[12:15], a[108:111], v[116:119]\n"
"ds_read_b64_tr_b16 a[76:77], v24 offset:8224\n"
"buffer_load_dwordx4 v7, s[44:47], s80 offen offset:1024 lds\n"
"v_mfma_f32_16x16x32_bf16 v[196:199], a[16:19], a[108:111], v[196:199]\n"
"ds_read_b64_tr_b16 a[78:79], v24 offset:9248\n"
"v_mfma_f32_16x16x32_bf16 v[212:215], a[20:23], a[108:111], v[212:215]\n"
"ds_read_b64_tr_b16 a[80:81], v24 offset:8232\n"
"v_mfma_f32_16x16x32_bf16 v[228:231], a[24:27], a[108:111], v[228:231]\n"
"ds_read_b64_tr_b16 a[82:83], v24 offset:9256\n"
"v_mfma_f32_16x16x32_bf16 v[244:247], a[28:31], a[108:111], v[244:247]\n"
"ds_read_b64_tr_b16 a[84:85], v25 offset:2048\n"
"buffer_load_dwordx4 v8, s[44:47], s80 offen offset:2048 lds\n"
"v_mfma_f32_16x16x32_bf16 v[132:135], a[32:35], a[108:111], v[132:135]\n"
"ds_read_b64_tr_b16 a[86:87], v25 offset:2304\n"
"v_mfma_f32_16x16x32_bf16 v[148:151], a[36:39], a[108:111], v[148:151]\n"
"ds_read_b64_tr_b16 a[88:89], v26 offset:2048\n"
"v_mfma_f32_16x16x32_bf16 v[164:167], a[40:43], a[108:111], v[164:167]\n"
"ds_read_b64_tr_b16 a[90:91], v26 offset:2304\n"
"v_mfma_f32_16x16x32_bf16 v[172:175], a[44:47], a[108:111], v[172:175]\n"
"ds_read_b64_tr_b16 a[92:93], v25 offset:6144\n"
"buffer_load_dwordx4 v9, s[44:47], s80 offen offset:3072 lds\n"
"v_mfma_f32_16x16x32_bf16 v[72:75], a[0:3], a[112:115], v[72:75]\n"
"ds_read_b64_tr_b16 a[94:95], v25 offset:6400\n"
"v_mfma_f32_16x16x32_bf16 v[88:91], a[4:7], a[112:115], v[88:91]\n"
"ds_read_b64_tr_b16 a[96:97], v26 offset:6144\n"
"v_mfma_f32_16x16x32_bf16 v[104:107], a[8:11], a[112:115], v[104:107]\n"
"ds_read_b64_tr_b16 a[98:99], v26 offset:6400\n"
"v_mfma_f32_16x16x32_bf16 v[120:123], a[12:15], a[112:115], v[120:123]\n"
"s_add_u32 m0, s84, 4096\n"
"s_xor_b32 s84, s84, 0x8000\n"
"buffer_load_dwordx4 v10, s[44:47], s80 offen offset:0 lds\n"
"v_mfma_f32_16x16x32_bf16 v[200:203], a[16:19], a[112:115], v[200:203]\n"
"v_mfma_f32_16x16x32_bf16 v[216:219], a[20:23], a[112:115], v[216:219]\n"
"v_mfma_f32_16x16x32_bf16 v[232:235], a[24:27], a[112:115], v[232:235]\n"
"v_xor_b32 v19, 0x8000, v19\n"
"v_mfma_f32_16x16x32_bf16 v[248:251], a[28:31], a[112:115], v[248:251]\n"
"buffer_load_dwordx4 v11, s[44:47], s80 offen offset:1024 lds\n"
"v_xor_b32 v20, 0x8000, v20\n"
"v_mfma_f32_16x16x32_bf16 v[136:139], a[32:35], a[112:115], v[136:139]\n"
"v_xor_b32 v21, 0x8000, v21\n"
"v_mfma_f32_16x16x32_bf16 v[152:155], a[36:39], a[112:115], v[152:155]\n"
"v_xor_b32 v22, 0x8000, v22\n"
"v_mfma_f32_16x16x32_bf16 v[76:79], a[0:3], a[116:119], v[76:79]\n"
"v_xor_b32 v23, 0x8000, v23\n"
"v_mfma_f32_16x16x32_bf16 v[92:95], a[4:7], a[116:119], v[92:95]\n"
"buffer_load_dwordx4 v12, s[44:47], s80 offen offset:2048 lds\n"
"v_xor_b32 v24, 0x8000, v24\n"
"v_mfma_f32_16x16x32_bf16 v[108:111], a[8:11], a[116:119], v[108:111]\n"
"v_xor_b32 v25, 0x8000, v25\n"
"v_mfma_f32_16x16x32_bf16 v[124:127], a[12:15], a[116:119], v[124:127]\n"
"v_xor_b32 v26, 0x8000, v26\n"
"v_mfma_f32_16x16x32_bf16 v[204:207], a[16:19], a[116:119], v[204:207]\n"
"v_xor_b32 v27, 0x8000, v27\n"
"v_mfma_f32_16x16x32_bf16 v[220:223], a[20:23], a[116:119], v[220:223]\n"
"buffer_load_dwordx4 v13, s[44:47], s80 offen offset:3072 lds\n"
"v_mfma_f32_16x16x32_bf16 v[236:239], a[24:27], a[116:119], v[236:239]\n"
"v_mfma_f32_16x16x32_bf16 v[252:255], a[28:31], a[116:119], v[252:255]\n"
"v_mfma_f32_16x16x32_bf16 v[140:143], a[32:35], a[116:119], v[140:143]\n"
"v_mfma_f32_16x16x32_bf16 v[156:159], a[36:39], a[116:119], v[156:159]\n"
"s_waitcnt vmcnt(8) lgkmcnt(0)\n"
"s_barrier\n"
"v_mfma_f32_16x16x32_bf16 v[176:179], a[52:55], a[136:139], v[176:179]\n"
"s_mov_b32 m0, s85\n"
"ds_read_b64_tr_b16 a[0:1], v21 offset:0\n"
"buffer_load_dwordx4 v14, s[48:51], s81 offen lds\n"
"v_mfma_f32_16x16x32_bf16 v[180:183], a[56:59], a[136:139], v[180:183]\n"
"s_add_u32 s90, s90, 1\n"
"s_and_b32 s90, s90, 7\n"
"ds_read_b64_tr_b16 a[2:3], v21 offset:1024\n"
"v_mfma_f32_16x16x32_bf16 v[184:187], a[100:103], a[136:139], v[184:187]\n"
"ds_read_b64_tr_b16 a[4:5], v21 offset:8\n"
"v_mfma_f32_16x16x32_bf16 v[64:67], a[52:55], a[120:123], v[64:67]\n"
"ds_read_b64_tr_b16 a[6:7], v21 offset:1032\n"
"v_mfma_f32_16x16x32_bf16 v[80:83], a[56:59], a[120:123], v[80:83]\n"
"s_lshl_b32 s80, s90, 7\n"
"ds_read_b64_tr_b16 a[48:49], v27 offset:0\n"
"buffer_load_dwordx4 v15, s[48:51], s81 offen offset:1024 lds\n"
"v_mfma_f32_16x16x32_bf16 v[96:99], a[60:63], a[120:123], v[96:99]\n"
"ds_read_b64_tr_b16 a[50:51], v27 offset:256\n"
"v_mfma_f32_16x16x32_bf16 v[112:115], a[64:67], a[120:123], v[112:115]\n"
"ds_read_b128 a[104:107], v19 offset:0\n"
"v_mfma_f32_16x16x32_bf16 v[192:195], a[68:71], a[120:123], v[192:195]\n"
"ds_read_b128 a[108:111], v19 offset:2048\n"
"v_mfma_f32_16x16x32_bf16 v[208:211], a[72:75], a[120:123], v[208:211]\n"
"ds_read_b128 a[112:115], v19 offset:4096\n"
"buffer_load_dwordx4 v16, s[48:51], s81 offen offset:2056 lds\n"
"v_mfma_f32_16x16x32_bf16 v[224:227], a[76:79], a[120:123], v[224:227]\n"
"ds_read_b128 a[116:119], v19 offset:6144\n"
"v_mfma_f32_16x16x32_bf16 v[240:243], a[80:83], a[120:123], v[240:243]\n"
"ds_read_b64_tr_b16 a[8:9], v22 offset:0\n"
"v_mfma_f32_16x16x32_bf16 v[128:131], a[84:87], a[120:123], v[128:131]\n"
"ds_read_b64_tr_b16 a[10:11], v22 offset:1024\n"
"v_mfma_f32_16x16x32_bf16 v[144:147], a[88:91], a[120:123], v[144:147]\n"
"ds_read_b64_tr_b16 a[12:13], v22 offset:8\n"
"buffer_load_dwordx4 v17, s[48:51], s81 offen offset:3080 lds\n"
"v_mfma_f32_16x16x32_bf16 v[160:163], a[92:95], a[120:123], v[160:163]\n"
"ds_read_b64_tr_b16 a[14:15], v22 offset:1032\n"
"v_mfma_f32_16x16x32_bf16 v[168:171], a[96:99], a[120:123], v[168:171]\n"
"ds_read_b64_tr_b16 a[16:17], v23 offset:0\n"
"v_mfma_f32_16x16x32_bf16 v[68:71], a[52:55], a[124:127], v[68:71]\n"
"s_lshl_b32 s81, s90, 19\n"
"ds_read_b64_tr_b16 a[18:19], v23 offset:1024\n"
"v_mfma_f32_16x16x32_bf16 v[84:87], a[56:59], a[124:127], v[84:87]\n"
"s_add_u32 m0, s85, s86\n"
"ds_read_b64_tr_b16 a[20:21], v23 offset:8\n"
"buffer_load_dwordx4 v18, s[52:55], s82 offen lds\n"
"v_mfma_f32_16x16x32_bf16 v[100:103], a[60:63], a[124:127], v[100:103]\n"
"s_xor_b32 s85, s85, 0x8000\n"
"ds_read_b64_tr_b16 a[22:23], v23 offset:1032\n"
"v_mfma_f32_16x16x32_bf16 v[116:119], a[64:67], a[124:127], v[116:119]\n"
"ds_read_b64_tr_b16 a[24:25], v24 offset:0\n"
"v_mfma_f32_16x16x32_bf16 v[196:199], a[68:71], a[124:127], v[196:199]\n"
"s_mul_i32 s82, s90, 176128\n"
"ds_read_b64_tr_b16 a[26:27], v24 offset:1024\n"
"v_mfma_f32_16x16x32_bf16 v[212:215], a[72:75], a[124:127], v[212:215]\n"
"s_add_u32 m0, m0, 4096\n"
"ds_read_b64_tr_b16 a[28:29], v24 offset:8\n"
"buffer_load_dwordx4 v18, s[52:55], s83 offen lds\n"
"v_mfma_f32_16x16x32_bf16 v[228:231], a[76:79], a[124:127], v[228:231]\n"
"s_add_u32 s83, s82, s92\n"
"s_add_u32 s82, s82, s91\n"
"ds_read_b64_tr_b16 a[30:31], v24 offset:1032\n"
"v_mfma_f32_16x16x32_bf16 v[244:247], a[80:83], a[124:127], v[244:247]\n"
"ds_read_b64_tr_b16 a[32:33], v25 offset:0\n"
"v_mfma_f32_16x16x32_bf16 v[132:135], a[84:87], a[124:127], v[132:135]\n"
"ds_read_b64_tr_b16 a[34:35], v25 offset:256\n"
"v_mfma_f32_16x16x32_bf16 v[148:151], a[88:91], a[124:127], v[148:151]\n"
"ds_read_b64_tr_b16 a[36:37], v26 offset:0\n"
"v_mfma_f32_16x16x32_bf16 v[164:167], a[92:95], a[124:127], v[164:167]\n"
"ds_read_b64_tr_b16 a[38:39], v26 offset:256\n"
"v_mfma_f32_16x16x32_bf16 v[172:175], a[96:99], a[124:127], v[172:175]\n"
"ds_read_b64_tr_b16 a[40:41], v25 offset:4096\n"
"v_mfma_f32_16x16x32_bf16 v[72:75], a[52:55], a[128:131], v[72:75]\n"
"ds_read_b64_tr_b16 a[42:43], v25 offset:4352\n"
"v_mfma_f32_16x16x32_bf16 v[88:91], a[56:59], a[128:131], v[88:91]\n"
"ds_read_b64_tr_b16 a[44:45], v26 offset:4096\n"
"v_mfma_f32_16x16x32_bf16 v[104:107], a[60:63], a[128:131], v[104:107]\n"
"ds_read_b64_tr_b16 a[46:47], v26 offset:4352\n"
"v_mfma_f32_16x16x32_bf16 v[120:123], a[64:67], a[128:131], v[120:123]\n"
"v_mfma_f32_16x16x32_bf16 v[200:203], a[68:71], a[128:131], v[200:203]\n"
"v_mfma_f32_16x16x32_bf16 v[216:219], a[72:75], a[128:131], v[216:219]\n"
"v_mfma_f32_16x16x32_bf16 v[232:235], a[76:79], a[128:131], v[232:235]\n"
"v_mfma_f32_16x16x32_bf16 v[248:251], a[80:83], a[128:131], v[248:251]\n"
"v_mfma_f32_16x16x32_bf16 v[136:139], a[84:87], a[128:131], v[136:139]\n"
"v_mfma_f32_16x16x32_bf16 v[152:155], a[88:91], a[128:131], v[152:155]\n"
"v_mfma_f32_16x16x32_bf16 v[76:79], a[52:55], a[132:135], v[76:79]\n"
"v_mfma_f32_16x16x32_bf16 v[92:95], a[56:59], a[132:135], v[92:95]\n"
"v_mfma_f32_16x16x32_bf16 v[108:111], a[60:63], a[132:135], v[108:111]\n"
"v_mfma_f32_16x16x32_bf16 v[124:127], a[64:67], a[132:135], v[124:127]\n"
"v_mfma_f32_16x16x32_bf16 v[204:207], a[68:71], a[132:135], v[204:207]\n"
"v_mfma_f32_16x16x32_bf16 v[220:223], a[72:75], a[132:135], v[220:223]\n"
"v_mfma_f32_16x16x32_bf16 v[236:239], a[76:79], a[132:135], v[236:239]\n"
"v_mfma_f32_16x16x32_bf16 v[252:255], a[80:83], a[132:135], v[252:255]\n"
"v_mfma_f32_16x16x32_bf16 v[140:143], a[84:87], a[132:135], v[140:143]\n"
"v_mfma_f32_16x16x32_bf16 v[156:159], a[88:91], a[132:135], v[156:159]\n"
"s_sub_u32 s13, s13, 1\n"
"s_cmp_lg_u32 s13, 0\n"
"s_cbranch_scc1 LPROJ_%=\n"
"s_waitcnt lgkmcnt(15)\n"
"v_mfma_f32_16x16x32_bf16 v[176:179], a[0:3], a[136:139], v[176:179]\n"
"ds_read_b64_tr_b16 a[52:53], v21 offset:8224\n"
"v_mfma_f32_16x16x32_bf16 v[180:183], a[4:7], a[136:139], v[180:183]\n"
"ds_read_b64_tr_b16 a[54:55], v21 offset:9248\n"
"v_mfma_f32_16x16x32_bf16 v[184:187], a[48:51], a[136:139], v[184:187]\n"
"ds_read_b64_tr_b16 a[56:57], v21 offset:8232\n"
"v_mfma_f32_16x16x32_bf16 v[64:67], a[0:3], a[104:107], v[64:67]\n"
"ds_read_b64_tr_b16 a[58:59], v21 offset:9256\n"
"v_mfma_f32_16x16x32_bf16 v[80:83], a[4:7], a[104:107], v[80:83]\n"
"ds_read_b64_tr_b16 a[100:101], v27 offset:2048\n"
"v_mfma_f32_16x16x32_bf16 v[96:99], a[8:11], a[104:107], v[96:99]\n"
"ds_read_b64_tr_b16 a[102:103], v27 offset:2304\n"
"v_mfma_f32_16x16x32_bf16 v[112:115], a[12:15], a[104:107], v[112:115]\n"
"s_waitcnt lgkmcnt(6)\n"
"ds_read_b128 a[120:123], v20 offset:0\n"
"v_mfma_f32_16x16x32_bf16 v[192:195], a[16:19], a[104:107], v[192:195]\n"
"ds_read_b128 a[124:127], v20 offset:2048\n"
"v_mfma_f32_16x16x32_bf16 v[208:211], a[20:23], a[104:107], v[208:211]\n"
"ds_read_b128 a[128:131], v20 offset:4096\n"
"v_mfma_f32_16x16x32_bf16 v[224:227], a[24:27], a[104:107], v[224:227]\n"
"s_mov_b32 m0, s88\n"
"ds_read_b128 a[132:135], v20 offset:6144\n"
"buffer_load_dwordx4 v28, s[56:59], s87 offen lds\n"
"v_mfma_f32_16x16x32_bf16 v[240:243], a[28:31], a[104:107], v[240:243]\n"
"ds_read_b64_tr_b16 a[60:61], v22 offset:8224\n"
"v_mfma_f32_16x16x32_bf16 v[128:131], a[32:35], a[104:107], v[128:131]\n"
"ds_read_b64_tr_b16 a[62:63], v22 offset:9248\n"
"v_mfma_f32_16x16x32_bf16 v[144:147], a[36:39], a[104:107], v[144:147]\n"
"ds_read_b64_tr_b16 a[64:65], v22 offset:8232\n"
"v_mfma_f32_16x16x32_bf16 v[160:163], a[40:43], a[104:107], v[160:163]\n"
"ds_read_b64_tr_b16 a[66:67], v22 offset:9256\n"
"buffer_load_dwordx4 v29, s[56:59], s87 offen offset:1024 lds\n"
"v_mfma_f32_16x16x32_bf16 v[168:171], a[44:47], a[104:107], v[168:171]\n"
"ds_read_b64_tr_b16 a[68:69], v23 offset:8224\n"
"v_mfma_f32_16x16x32_bf16 v[68:71], a[0:3], a[108:111], v[68:71]\n"
"ds_read_b64_tr_b16 a[70:71], v23 offset:9248\n"
"v_mfma_f32_16x16x32_bf16 v[84:87], a[4:7], a[108:111], v[84:87]\n"
"ds_read_b64_tr_b16 a[72:73], v23 offset:8232\n"
"v_mfma_f32_16x16x32_bf16 v[100:103], a[8:11], a[108:111], v[100:103]\n"
"ds_read_b64_tr_b16 a[74:75], v23 offset:9256\n"
"buffer_load_dwordx4 v30, s[56:59], s87 offen offset:2048 lds\n"
"v_mfma_f32_16x16x32_bf16 v[116:119], a[12:15], a[108:111], v[116:119]\n"
"ds_read_b64_tr_b16 a[76:77], v24 offset:8224\n"
"v_mfma_f32_16x16x32_bf16 v[196:199], a[16:19], a[108:111], v[196:199]\n"
"ds_read_b64_tr_b16 a[78:79], v24 offset:9248\n"
"v_mfma_f32_16x16x32_bf16 v[212:215], a[20:23], a[108:111], v[212:215]\n"
"ds_read_b64_tr_b16 a[80:81], v24 offset:8232\n"
"v_mfma_f32_16x16x32_bf16 v[228:231], a[24:27], a[108:111], v[228:231]\n"
"ds_read_b64_tr_b16 a[82:83], v24 offset:9256\n"
"buffer_load_dwordx4 v31, s[56:59], s87 offen offset:3072 lds\n"
"v_mfma_f32_16x16x32_bf16 v[244:247], a[28:31], a[108:111], v[244:247]\n"
"ds_read_b64_tr_b16 a[84:85], v25 offset:2048\n"
"v_mfma_f32_16x16x32_bf16 v[132:135], a[32:35], a[108:111], v[132:135]\n"
"ds_read_b64_tr_b16 a[86:87], v25 offset:2304\n"
"v_mfma_f32_16x16x32_bf16 v[148:151], a[36:39], a[108:111], v[148:151]\n"
"ds_read_b64_tr_b16 a[88:89], v26 offset:2048\n"
"v_mfma_f32_16x16x32_bf16 v[164:167], a[40:43], a[108:111], v[164:167]\n"
"s_mov_b32 m0, s89\n"
"ds_read_b64_tr_b16 a[90:91], v26 offset:2304\n"
"buffer_load_dwordx4 v32, s[60:63], s87 offen lds\n"
"v_mfma_f32_16x16x32_bf16 v[172:175], a[44:47], a[108:111], v[172:175]\n"
"ds_read_b64_tr_b16 a[92:93], v25 offset:6144\n"
"v_mfma_f32_16x16x32_bf16 v[72:75], a[0:3], a[112:115], v[72:75]\n"
"ds_read_b64_tr_b16 a[94:95], v25 offset:6400\n"
"v_mfma_f32_16x16x32_bf16 v[88:91], a[4:7], a[112:115], v[88:91]\n"
"ds_read_b64_tr_b16 a[96:97], v26 offset:6144\n"
"v_mfma_f32_16x16x32_bf16 v[104:107], a[8:11], a[112:115], v[104:107]\n"
"ds_read_b64_tr_b16 a[98:99], v26 offset:6400\n"
"buffer_load_dwordx4 v33, s[60:63], s87 offen offset:1024 lds\n"
"v_mfma_f32_16x16x32_bf16 v[120:123], a[12:15], a[112:115], v[120:123]\n"
"v_mfma_f32_16x16x32_bf16 v[200:203], a[16:19], a[112:115], v[200:203]\n"
"v_mfma_f32_16x16x32_bf16 v[216:219], a[20:23], a[112:115], v[216:219]\n"
"v_mfma_f32_16x16x32_bf16 v[232:235], a[24:27], a[112:115], v[232:235]\n"
"v_xor_b32 v19, 0x8000, v19\n"
"buffer_load_dwordx4 v32, s[60:63], s87 offen offset:2048 lds\n"
"v_mfma_f32_16x16x32_bf16 v[248:251], a[28:31], a[112:115], v[248:251]\n"
"v_xor_b32 v20, 0x8000, v20\n"
"v_mfma_f32_16x16x32_bf16 v[136:139], a[32:35], a[112:115], v[136:139]\n"
"v_xor_b32 v21, 0x8000, v21\n"
"v_mfma_f32_16x16x32_bf16 v[152:155], a[36:39], a[112:115], v[152:155]\n"
"v_xor_b32 v22, 0x8000, v22\n"
"v_mfma_f32_16x16x32_bf16 v[76:79], a[0:3], a[116:119], v[76:79]\n"
"v_xor_b32 v23, 0x8000, v23\n"
"buffer_load_dwordx4 v33, s[60:63], s87 offen offset:3072 lds\n"
"v_mfma_f32_16x16x32_bf16 v[92:95], a[4:7], a[116:119], v[92:95]\n"
"v_xor_b32 v24, 0x8000, v24\n"
"v_mfma_f32_16x16x32_bf16 v[108:111], a[8:11], a[116:119], v[108:111]\n"
"v_xor_b32 v25, 0x8000, v25\n"
"v_mfma_f32_16x16x32_bf16 v[124:127], a[12:15], a[116:119], v[124:127]\n"
"v_xor_b32 v26, 0x8000, v26\n"
"v_mfma_f32_16x16x32_bf16 v[204:207], a[16:19], a[116:119], v[204:207]\n"
"v_xor_b32 v27, 0x8000, v27\n"
"v_mfma_f32_16x16x32_bf16 v[220:223], a[20:23], a[116:119], v[220:223]\n"
"v_mfma_f32_16x16x32_bf16 v[236:239], a[24:27], a[116:119], v[236:239]\n"
"v_mfma_f32_16x16x32_bf16 v[252:255], a[28:31], a[116:119], v[252:255]\n"
"v_mfma_f32_16x16x32_bf16 v[140:143], a[32:35], a[116:119], v[140:143]\n"
"v_mfma_f32_16x16x32_bf16 v[156:159], a[36:39], a[116:119], v[156:159]\n"
"s_waitcnt vmcnt(8) lgkmcnt(0)\n"
"s_barrier\n"
"v_mfma_f32_16x16x32_bf16 v[176:179], a[52:55], a[136:139], v[176:179]\n"
"s_lshl_b32 s88, s4, 12\n"
"ds_read_b64_tr_b16 a[0:1], v21 offset:0\n"
"s_add_u32 s88, s88, 0\n"
"s_lshl_b32 s89, s4, 12\n"
"s_add_u32 s87, s87, 16384\n"
"s_and_b32 s87, s87, 0x1ffff\n"
"s_mov_b32 m0, s88\n"
"s_add_u32 s89, s89, 16384\n"
"buffer_load_dwordx4 v28, s[56:59], s87 offen lds\n"
"v_mfma_f32_16x16x32_bf16 v[180:183], a[56:59], a[136:139], v[180:183]\n"
"ds_read_b64_tr_b16 a[2:3], v21 offset:1024\n"
"v_mfma_f32_16x16x32_bf16 v[184:187], a[100:103], a[136:139], v[184:187]\n"
"ds_read_b64_tr_b16 a[4:5], v21 offset:8\n"
"v_mfma_f32_16x16x32_bf16 v[64:67], a[52:55], a[120:123], v[64:67]\n"
"ds_read_b64_tr_b16 a[6:7], v21 offset:1032\n"
"v_mfma_f32_16x16x32_bf16 v[80:83], a[56:59], a[120:123], v[80:83]\n"
"ds_read_b64_tr_b16 a[48:49], v27 offset:0\n"
"buffer_load_dwordx4 v29, s[56:59], s87 offen offset:1024 lds\n"
"v_mfma_f32_16x16x32_bf16 v[96:99], a[60:63], a[120:123], v[96:99]\n"
"ds_read_b64_tr_b16 a[50:51], v27 offset:256\n"
"v_mfma_f32_16x16x32_bf16 v[112:115], a[64:67], a[120:123], v[112:115]\n"
"ds_read_b128 a[104:107], v19 offset:0\n"
"v_mfma_f32_16x16x32_bf16 v[192:195], a[68:71], a[120:123], v[192:195]\n"
"ds_read_b128 a[108:111], v19 offset:2048\n"
"v_mfma_f32_16x16x32_bf16 v[208:211], a[72:75], a[120:123], v[208:211]\n"
"ds_read_b128 a[112:115], v19 offset:4096\n"
"buffer_load_dwordx4 v30, s[56:59], s87 offen offset:2048 lds\n"
"v_mfma_f32_16x16x32_bf16 v[224:227], a[76:79], a[120:123], v[224:227]\n"
"ds_read_b128 a[116:119], v19 offset:6144\n"
"v_mfma_f32_16x16x32_bf16 v[240:243], a[80:83], a[120:123], v[240:243]\n"
"ds_read_b64_tr_b16 a[8:9], v22 offset:0\n"
"v_mfma_f32_16x16x32_bf16 v[128:131], a[84:87], a[120:123], v[128:131]\n"
"ds_read_b64_tr_b16 a[10:11], v22 offset:1024\n"
"v_mfma_f32_16x16x32_bf16 v[144:147], a[88:91], a[120:123], v[144:147]\n"
"ds_read_b64_tr_b16 a[12:13], v22 offset:8\n"
"buffer_load_dwordx4 v31, s[56:59], s87 offen offset:3072 lds\n"
"v_mfma_f32_16x16x32_bf16 v[160:163], a[92:95], a[120:123], v[160:163]\n"
"ds_read_b64_tr_b16 a[14:15], v22 offset:1032\n"
"v_mfma_f32_16x16x32_bf16 v[168:171], a[96:99], a[120:123], v[168:171]\n"
"ds_read_b64_tr_b16 a[16:17], v23 offset:0\n"
"v_mfma_f32_16x16x32_bf16 v[68:71], a[52:55], a[124:127], v[68:71]\n"
"ds_read_b64_tr_b16 a[18:19], v23 offset:1024\n"
"v_mfma_f32_16x16x32_bf16 v[84:87], a[56:59], a[124:127], v[84:87]\n"
"s_mov_b32 m0, s89\n"
"ds_read_b64_tr_b16 a[20:21], v23 offset:8\n"
"buffer_load_dwordx4 v32, s[60:63], s87 offen lds\n"
"v_mfma_f32_16x16x32_bf16 v[100:103], a[60:63], a[124:127], v[100:103]\n"
"ds_read_b64_tr_b16 a[22:23], v23 offset:1032\n"
"v_mfma_f32_16x16x32_bf16 v[116:119], a[64:67], a[124:127], v[116:119]\n"
"ds_read_b64_tr_b16 a[24:25], v24 offset:0\n"
"v_mfma_f32_16x16x32_bf16 v[196:199], a[68:71], a[124:127], v[196:199]\n"
"ds_read_b64_tr_b16 a[26:27], v24 offset:1024\n"
"v_mfma_f32_16x16x32_bf16 v[212:215], a[72:75], a[124:127], v[212:215]\n"
"ds_read_b64_tr_b16 a[28:29], v24 offset:8\n"
"buffer_load_dwordx4 v33, s[60:63], s87 offen offset:1024 lds\n"
"v_mfma_f32_16x16x32_bf16 v[228:231], a[76:79], a[124:127], v[228:231]\n"
"ds_read_b64_tr_b16 a[30:31], v24 offset:1032\n"
"v_mfma_f32_16x16x32_bf16 v[244:247], a[80:83], a[124:127], v[244:247]\n"
"ds_read_b64_tr_b16 a[32:33], v25 offset:0\n"
"v_mfma_f32_16x16x32_bf16 v[132:135], a[84:87], a[124:127], v[132:135]\n"
"ds_read_b64_tr_b16 a[34:35], v25 offset:256\n"
"v_mfma_f32_16x16x32_bf16 v[148:151], a[88:91], a[124:127], v[148:151]\n"
"ds_read_b64_tr_b16 a[36:37], v26 offset:0\n"
"buffer_load_dwordx4 v32, s[60:63], s87 offen offset:2048 lds\n"
"v_mfma_f32_16x16x32_bf16 v[164:167], a[92:95], a[124:127], v[164:167]\n"
"ds_read_b64_tr_b16 a[38:39], v26 offset:256\n"
"v_mfma_f32_16x16x32_bf16 v[172:175], a[96:99], a[124:127], v[172:175]\n"
"ds_read_b64_tr_b16 a[40:41], v25 offset:4096\n"
"v_mfma_f32_16x16x32_bf16 v[72:75], a[52:55], a[128:131], v[72:75]\n"
"ds_read_b64_tr_b16 a[42:43], v25 offset:4352\n"
"v_mfma_f32_16x16x32_bf16 v[88:91], a[56:59], a[128:131], v[88:91]\n"
"ds_read_b64_tr_b16 a[44:45], v26 offset:4096\n"
"buffer_load_dwordx4 v33, s[60:63], s87 offen offset:3072 lds\n"
"v_mfma_f32_16x16x32_bf16 v[104:107], a[60:63], a[128:131], v[104:107]\n"
"ds_read_b64_tr_b16 a[46:47], v26 offset:4352\n"
"v_mfma_f32_16x16x32_bf16 v[120:123], a[64:67], a[128:131], v[120:123]\n"
"s_add_u32 s87, s87, 16384\n"
"s_and_b32 s87, s87, 0x1ffff\n"
"v_mfma_f32_16x16x32_bf16 v[200:203], a[68:71], a[128:131], v[200:203]\n"
"s_lshl_b32 s89, s4, 12\n"
"s_lshl_b32 s88, s4, 12\n"
"s_add_u32 s88, s88, 65536\n"
"v_mfma_f32_16x16x32_bf16 v[216:219], a[72:75], a[128:131], v[216:219]\n"
"s_mov_b32 m0, s88\n"
"s_add_u32 s89, s89, 32768\n"
"buffer_load_dwordx4 v28, s[56:59], s87 offen lds\n"
"v_mfma_f32_16x16x32_bf16 v[232:235], a[76:79], a[128:131], v[232:235]\n"
"v_mfma_f32_16x16x32_bf16 v[248:251], a[80:83], a[128:131], v[248:251]\n"
"v_mfma_f32_16x16x32_bf16 v[136:139], a[84:87], a[128:131], v[136:139]\n"
"v_mfma_f32_16x16x32_bf16 v[152:155], a[88:91], a[128:131], v[152:155]\n"
"buffer_load_dwordx4 v29, s[56:59], s87 offen offset:1024 lds\n"
"v_mfma_f32_16x16x32_bf16 v[76:79], a[52:55], a[132:135], v[76:79]\n"
"v_mfma_f32_16x16x32_bf16 v[92:95], a[56:59], a[132:135], v[92:95]\n"
"v_mfma_f32_16x16x32_bf16 v[108:111], a[60:63], a[132:135], v[108:111]\n"
"v_mfma_f32_16x16x32_bf16 v[124:127], a[64:67], a[132:135], v[124:127]\n"
"buffer_load_dwordx4 v30, s[56:59], s87 offen offset:2048 lds\n"
"v_mfma_f32_16x16x32_bf16 v[204:207], a[68:71], a[132:135], v[204:207]\n"
"v_mfma_f32_16x16x32_bf16 v[220:223], a[72:75], a[132:135], v[220:223]\n"
"v_mfma_f32_16x16x32_bf16 v[236:239], a[76:79], a[132:135], v[236:239]\n"
"v_mfma_f32_16x16x32_bf16 v[252:255], a[80:83], a[132:135], v[252:255]\n"
"buffer_load_dwordx4 v31, s[56:59], s87 offen offset:3072 lds\n"
"v_mfma_f32_16x16x32_bf16 v[140:143], a[84:87], a[132:135], v[140:143]\n"
"v_mfma_f32_16x16x32_bf16 v[156:159], a[88:91], a[132:135], v[156:159]\n"
"s_xor_b32 s16, s7, 0\n"
"s_waitcnt lgkmcnt(8)\n"
"v_mfma_f32_16x16x32_bf16 v[176:179], a[0:3], a[136:139], v[176:179]\n"
"s_lshl_b32 s43, s6, 2\n"
"ds_read_b64_tr_b16 a[52:53], v21 offset:8224\n"
"s_add_u32 s43, s43, s4\n"
"s_lshl_b32 s43, s43, 10\n"
"v_add_lshl_u32 v188, v2, s16, 4\n"
"v_mfma_f32_16x16x32_bf16 v[180:183], a[4:7], a[136:139], v[180:183]\n"
"s_add_u32 s43, s43, 24117248\n"
"ds_read_b64_tr_b16 a[54:55], v21 offset:9248\n"
"s_xor_b32 s16, s7, 16\n"
"v_add_lshl_u32 v189, v2, s16, 4\n"
"v_mfma_f32_16x16x32_bf16 v[184:187], a[48:51], a[136:139], v[184:187]\n"
"s_xor_b32 s16, s7, 32\n"
"ds_read_b64_tr_b16 a[56:57], v21 offset:8232\n"
"v_add_lshl_u32 v190, v2, s16, 4\n"
"v_mfma_f32_16x16x32_bf16 v[64:67], a[0:3], a[104:107], v[64:67]\n"
"s_xor_b32 s16, s7, 48\n"
"ds_read_b64_tr_b16 a[58:59], v21 offset:9256\n"
"v_add_lshl_u32 v191, v2, s16, 4\n"
"v_mfma_f32_16x16x32_bf16 v[80:83], a[4:7], a[104:107], v[80:83]\n"
"ds_read_b64_tr_b16 a[100:101], v27 offset:2048\n"
"v_mfma_f32_16x16x32_bf16 v[96:99], a[8:11], a[104:107], v[96:99]\n"
"ds_read_b64_tr_b16 a[102:103], v27 offset:2304\n"
"buffer_load_dwordx4 v[46:49], v188, s[64:67], s43 offen sc1\n"
"v_mfma_f32_16x16x32_bf16 v[112:115], a[12:15], a[104:107], v[112:115]\n"
"ds_read_b128 a[120:123], v20 offset:0\n"
"v_mfma_f32_16x16x32_bf16 v[192:195], a[16:19], a[104:107], v[192:195]\n"
"ds_read_b128 a[124:127], v20 offset:2048\n"
"buffer_load_dwordx4 v[50:53], v189, s[64:67], s43 offen sc1\n"
"v_mfma_f32_16x16x32_bf16 v[208:211], a[20:23], a[104:107], v[208:211]\n"
"ds_read_b128 a[128:131], v20 offset:4096\n"
"v_mfma_f32_16x16x32_bf16 v[224:227], a[24:27], a[104:107], v[224:227]\n"
"ds_read_b128 a[132:135], v20 offset:6144\n"
"buffer_load_dwordx4 v[54:57], v190, s[64:67], s43 offen sc1\n"
"v_mfma_f32_16x16x32_bf16 v[240:243], a[28:31], a[104:107], v[240:243]\n"
"s_lshl_b32 s82, s5, 6\n"
"s_waitcnt lgkmcnt(10)\n"
"ds_read_b64_tr_b16 a[60:61], v22 offset:8224\n"
"v_mfma_f32_16x16x32_bf16 v[128:131], a[32:35], a[104:107], v[128:131]\n"
"s_mov_b32 s45, s33\n"
"ds_read_b64_tr_b16 a[62:63], v22 offset:9248\n"
"buffer_load_dwordx4 v[58:61], v191, s[64:67], s43 offen sc1\n"
"v_mfma_f32_16x16x32_bf16 v[144:147], a[36:39], a[104:107], v[144:147]\n"
"s_mov_b32 s44, s32\n"
"ds_read_b64_tr_b16 a[64:65], v22 offset:8232\n"
"v_lshlrev_b32 v18, 3, v3\n"
"v_mfma_f32_16x16x32_bf16 v[160:163], a[40:43], a[104:107], v[160:163]\n"
"s_mov_b32 s47, 0x20000\n"
"s_mov_b32 s46, 2752\n"
"ds_read_b64_tr_b16 a[66:67], v22 offset:9256\n"
"v_mfma_f32_16x16x32_bf16 v[168:171], a[44:47], a[104:107], v[168:171]\n"
"s_lshl_b32 s83, s9, 6\n"
"ds_read_b64_tr_b16 a[68:69], v23 offset:8224\n"
"buffer_load_dwordx2 v[6:7], v18, s[44:47], s82 offen offset:0\n"
"v_mfma_f32_16x16x32_bf16 v[68:71], a[0:3], a[108:111], v[68:71]\n"
"ds_read_b64_tr_b16 a[70:71], v23 offset:9248\n"
"v_mfma_f32_16x16x32_bf16 v[84:87], a[4:7], a[108:111], v[84:87]\n"
"ds_read_b64_tr_b16 a[72:73], v23 offset:8232\n"
"buffer_load_dwordx2 v[8:9], v18, s[44:47], s82 offen offset:32\n"
"v_mfma_f32_16x16x32_bf16 v[100:103], a[8:11], a[108:111], v[100:103]\n"
"ds_read_b64_tr_b16 a[74:75], v23 offset:9256\n"
"v_mfma_f32_16x16x32_bf16 v[116:119], a[12:15], a[108:111], v[116:119]\n"
"ds_read_b64_tr_b16 a[76:77], v24 offset:8224\n"
"buffer_load_dwordx2 v[10:11], v18, s[44:47], s83 offen offset:0\n"
"v_mfma_f32_16x16x32_bf16 v[196:199], a[16:19], a[108:111], v[196:199]\n"
"ds_read_b64_tr_b16 a[78:79], v24 offset:9248\n"
"v_mfma_f32_16x16x32_bf16 v[212:215], a[20:23], a[108:111], v[212:215]\n"
"ds_read_b64_tr_b16 a[80:81], v24 offset:8232\n"
"buffer_load_dwordx2 v[12:13], v18, s[44:47], s83 offen offset:32\n"
"v_mfma_f32_16x16x32_bf16 v[228:231], a[24:27], a[108:111], v[228:231]\n"
"ds_read_b64_tr_b16 a[82:83], v24 offset:9256\n"
"v_mfma_f32_16x16x32_bf16 v[244:247], a[28:31], a[108:111], v[244:247]\n"
"ds_read_b64_tr_b16 a[84:85], v25 offset:2048\n"
"v_mfma_f32_16x16x32_bf16 v[132:135], a[32:35], a[108:111], v[132:135]\n"
"ds_read_b64_tr_b16 a[86:87], v25 offset:2304\n"
"v_mfma_f32_16x16x32_bf16 v[148:151], a[36:39], a[108:111], v[148:151]\n"
"ds_read_b64_tr_b16 a[88:89], v26 offset:2048\n"
"v_mfma_f32_16x16x32_bf16 v[164:167], a[40:43], a[108:111], v[164:167]\n"
"ds_read_b64_tr_b16 a[90:91], v26 offset:2304\n"
"v_mfma_f32_16x16x32_bf16 v[172:175], a[44:47], a[108:111], v[172:175]\n"
"ds_read_b64_tr_b16 a[92:93], v25 offset:6144\n"
"v_mfma_f32_16x16x32_bf16 v[72:75], a[0:3], a[112:115], v[72:75]\n"
"ds_read_b64_tr_b16 a[94:95], v25 offset:6400\n"
"v_mfma_f32_16x16x32_bf16 v[88:91], a[4:7], a[112:115], v[88:91]\n"
"ds_read_b64_tr_b16 a[96:97], v26 offset:6144\n"
"v_mfma_f32_16x16x32_bf16 v[104:107], a[8:11], a[112:115], v[104:107]\n"
"ds_read_b64_tr_b16 a[98:99], v26 offset:6400\n"
"v_mfma_f32_16x16x32_bf16 v[120:123], a[12:15], a[112:115], v[120:123]\n"
"v_mfma_f32_16x16x32_bf16 v[200:203], a[16:19], a[112:115], v[200:203]\n"
"v_mfma_f32_16x16x32_bf16 v[216:219], a[20:23], a[112:115], v[216:219]\n"
"v_mfma_f32_16x16x32_bf16 v[232:235], a[24:27], a[112:115], v[232:235]\n"
"s_lshl_b32 s16, s4, 7\n"
"s_add_u32 s16, s16, 90368\n"
"v_lshl_add_u32 v5, v3, 4, s16\n"
"v_mfma_f32_16x16x32_bf16 v[248:251], a[28:31], a[112:115], v[248:251]\n"
"s_lshl_b32 s16, s4, 6\n"
"s_add_u32 s16, s16, 90880\n"
"v_lshl_add_u32 v27, v3, 4, s16\n"
"v_mfma_f32_16x16x32_bf16 v[136:139], a[32:35], a[112:115], v[136:139]\n"
"s_xor_b32 s16, s4, 0\n"
"v_lshlrev_b32 v14, 4, v3\n"
"v_mfma_f32_16x16x32_bf16 v[152:155], a[36:39], a[112:115], v[152:155]\n"
"s_lshl_b32 s16, s16, 7\n"
"s_add_u32 s16, s16, 90368\n"
"v_add_u32 v21, s16, v14\n"
"v_mfma_f32_16x16x32_bf16 v[76:79], a[0:3], a[116:119], v[76:79]\n"
"s_xor_b32 s16, s4, 1\n"
"s_lshl_b32 s16, s16, 7\n"
"s_add_u32 s16, s16, 90368\n"
"v_add_u32 v22, s16, v14\n"
"v_mfma_f32_16x16x32_bf16 v[92:95], a[4:7], a[116:119], v[92:95]\n"
"s_xor_b32 s16, s4, 2\n"
"s_lshl_b32 s16, s16, 7\n"
"s_add_u32 s16, s16, 90368\n"
"v_add_u32 v23, s16, v14\n"
"v_mfma_f32_16x16x32_bf16 v[108:111], a[8:11], a[116:119], v[108:111]\n"
"s_xor_b32 s16, s4, 3\n"
"s_lshl_b32 s16, s16, 7\n"
"s_add_u32 s16, s16, 90368\n"
"v_add_u32 v24, s16, v14\n"
"v_mfma_f32_16x16x32_bf16 v[124:127], a[12:15], a[116:119], v[124:127]\n"
"v_add_u32 v14, 90880, v14\n"
"v_mfma_f32_16x16x32_bf16 v[204:207], a[16:19], a[116:119], v[204:207]\n"
"v_mfma_f32_16x16x32_bf16 v[220:223], a[20:23], a[116:119], v[220:223]\n"
"v_mfma_f32_16x16x32_bf16 v[236:239], a[24:27], a[116:119], v[236:239]\n"
"v_mfma_f32_16x16x32_bf16 v[252:255], a[28:31], a[116:119], v[252:255]\n"
"v_mfma_f32_16x16x32_bf16 v[140:143], a[32:35], a[116:119], v[140:143]\n"
"v_mfma_f32_16x16x32_bf16 v[156:159], a[36:39], a[116:119], v[156:159]\n"
"s_waitcnt vmcnt(20) lgkmcnt(0)\n"
"s_barrier\n"
"v_mfma_f32_16x16x32_bf16 v[176:179], a[52:55], a[136:139], v[176:179]\n"
"ds_read_b128 a[0:3], v34 offset:0\n"
"v_mfma_f32_16x16x32_bf16 v[180:183], a[56:59], a[136:139], v[180:183]\n"
"s_waitcnt vmcnt(4)\n"
"ds_read_b128 a[4:7], v35 offset:0\n"
"v_cmp_ne_u32 s[18:19], s8, v47\n"
"v_cmp_ne_u32 vcc, s8, v49\n"
"s_nop 0\n"
"s_or_b64 s[18:19], s[18:19], vcc\n"
"v_cmp_ne_u32 vcc, s8, v51\n"
"s_nop 0\n"
"s_or_b64 s[18:19], s[18:19], vcc\n"
"v_cmp_ne_u32 vcc, s8, v53\n"
"s_nop 0\n"
"s_or_b64 s[18:19], s[18:19], vcc\n"
"v_cmp_ne_u32 vcc, s8, v55\n"
"s_nop 0\n"
"s_or_b64 s[18:19], s[18:19], vcc\n"
"v_cmp_ne_u32 vcc, s8, v57\n"
"s_nop 0\n"
"s_or_b64 s[18:19], s[18:19], vcc\n"
"v_cmp_ne_u32 vcc, s8, v59\n"
"s_nop 0\n"
"s_or_b64 s[18:19], s[18:19], vcc\n"
"v_cmp_ne_u32 vcc, s8, v61\n"
"s_nop 0\n"
"s_or_b64 s[18:19], s[18:19], vcc\n"
"s_nop 0\n"
"s_cmp_lg_u64 s[18:19], 0\n"
"s_cbranch_scc1 LSTR_%=\n"
"LSTK_%=:\n"
"v_mfma_f32_16x16x32_bf16 v[184:187], a[100:103], a[136:139], v[184:187]\n"
"ds_read_b128 a[8:11], v36 offset:0\n"
"v_mul_f32 v47, 0x3e0293ee, v48\n"
"v_mfma_f32_16x16x32_bf16 v[64:67], a[52:55], a[120:123], v[64:67]\n"
"ds_read_b128 a[12:15], v37 offset:0\n"
"v_mul_f32 v46, v46, v48\n"
"v_mfma_f32_16x16x32_bf16 v[68:71], a[52:55], a[124:127], v[68:71]\n"
"ds_read_b128 a[16:19], v34 offset:4096\n"
"v_mul_f32 v51, 0x3e0293ee, v52\n"
"v_mfma_f32_16x16x32_bf16 v[72:75], a[52:55], a[128:131], v[72:75]\n"
"ds_read_b128 a[20:23], v35 offset:4096\n"
"v_mul_f32 v50, v50, v52\n"
"v_mfma_f32_16x16x32_bf16 v[76:79], a[52:55], a[132:135], v[76:79]\n"
"ds_read_b128 a[24:27], v36 offset:4096\n"
"v_mul_f32 v55, 0x3e0293ee, v56\n"
"v_mfma_f32_16x16x32_bf16 v[80:83], a[56:59], a[120:123], v[80:83]\n"
"s_mov_b32 m0, s89\n"
"ds_read_b128 a[28:31], v37 offset:4096\n"
"v_mul_f32 v54, v54, v56\n"
"v_mfma_f32_16x16x32_bf16 v[84:87], a[56:59], a[124:127], v[84:87]\n"
"buffer_load_dwordx4 v32, s[60:63], s87 offen lds\n"
"v_mul_f32 v59, 0x3e0293ee, v60\n"
"v_mfma_f32_16x16x32_bf16 v[88:91], a[56:59], a[128:131], v[88:91]\n"
"v_mul_f32 v49, 0xbe0293ee, v46\n"
"v_mul_f32 v58, v58, v60\n"
"v_mfma_f32_16x16x32_bf16 v[92:95], a[56:59], a[132:135], v[92:95]\n"
"buffer_load_dwordx4 v33, s[60:63], s87 offen offset:1024 lds\n"
"v_mfma_f32_16x16x32_bf16 v[96:99], a[60:63], a[120:123], v[96:99]\n"
"s_mov_b32 exec_lo, 0x10001\n"
"s_mov_b32 exec_hi, 0x10001\n"
"ds_write_b128 v5, v[176:179]\n"
"ds_write_b128 v5, v[180:183] offset:64\n"
"ds_write_b128 v27, v[184:187]\n"
"s_mov_b64 exec, -1\n"
"v_mfma_f32_16x16x32_bf16 v[100:103], a[60:63], a[124:127], v[100:103]\n"
"buffer_load_dwordx4 v32, s[60:63], s87 offen offset:2048 lds\n"
"v_mul_f32 v53, 0xbe0293ee, v50\n"
"v_mfma_f32_16x16x32_bf16 v[104:107], a[60:63], a[128:131], v[104:107]\n"
"s_lshl_b32 s89, s4, 12\n"
"s_add_u32 s89, s89, 49152\n"
"v_mul_f32 v57, 0xbe0293ee, v54\n"
"v_mfma_f32_16x16x32_bf16 v[108:111], a[60:63], a[132:135], v[108:111]\n"
"s_lshl_b32 s88, s4, 12\n"
"buffer_load_dwordx4 v33, s[60:63], s87 offen offset:3072 lds\n"
"v_mul_f32 v61, 0xbe0293ee, v58\n"
"v_mfma_f32_16x16x32_bf16 v[112:115], a[64:67], a[120:123], v[112:115]\n"
"s_add_u32 s87, s87, 16384\n"
"v_xor_b32 v46, 0x80000000, v46\n"
"v_xor_b32 v50, 0x80000000, v50\n"
"v_mfma_f32_16x16x32_bf16 v[116:119], a[64:67], a[124:127], v[116:119]\n"
"s_add_u32 s88, s88, 131072\n"
"v_xor_b32 v54, 0x80000000, v54\n"
"v_xor_b32 v58, 0x80000000, v58\n"
"v_mfma_f32_16x16x32_bf16 v[120:123], a[64:67], a[128:131], v[120:123]\n"
"s_and_b32 s87, s87, 0x1ffff\n"
"s_waitcnt lgkmcnt(0)\n"
"s_barrier\n"
"ds_read_b128 v[176:179], v21 offset:0\n"
"ds_read_b128 v[180:183], v21 offset:64\n"
"v_mfma_f32_16x16x32_bf16 v[124:127], a[64:67], a[132:135], v[124:127]\n"
"ds_read_b128 v[184:187], v22 offset:0\n"
"ds_read_b128 v[188:191], v22 offset:64\n"
"v_mfma_f32_16x16x32_bf16 v[192:195], a[68:71], a[120:123], v[192:195]\n"
"v_mul_f32 v64, v64, v47\n"
"v_mul_f32 v65, v65, v47\n"
"v_mfma_f32_16x16x32_bf16 v[196:199], a[68:71], a[124:127], v[196:199]\n"
"v_mul_f32 v66, v66, v47\n"
"v_mul_f32 v67, v67, v47\n"
"v_mfma_f32_16x16x32_bf16 v[200:203], a[68:71], a[128:131], v[200:203]\n"
"v_mul_f32 v80, v80, v47\n"
"v_mul_f32 v81, v81, v47\n"
"v_mfma_f32_16x16x32_bf16 v[204:207], a[68:71], a[132:135], v[204:207]\n"
"v_mul_f32 v82, v82, v47\n"
"v_mul_f32 v83, v83, v47\n"
"v_mfma_f32_16x16x32_bf16 v[208:211], a[72:75], a[120:123], v[208:211]\n"
"s_waitcnt lgkmcnt(2)\n"
"v_fmac_f32 v64, v176, v49\n"
"v_fmac_f32 v65, v177, v49\n"
"v_mfma_f32_16x16x32_bf16 v[212:215], a[72:75], a[124:127], v[212:215]\n"
"v_fmac_f32 v66, v178, v49\n"
"v_fmac_f32 v67, v179, v49\n"
"v_mfma_f32_16x16x32_bf16 v[216:219], a[72:75], a[128:131], v[216:219]\n"
"v_fmac_f32 v80, v180, v49\n"
"v_fmac_f32 v81, v181, v49\n"
"v_mfma_f32_16x16x32_bf16 v[220:223], a[72:75], a[132:135], v[220:223]\n"
"v_fmac_f32 v82, v182, v49\n"
"v_fmac_f32 v83, v183, v49\n"
"v_mfma_f32_16x16x32_bf16 v[224:227], a[76:79], a[120:123], v[224:227]\n"
"v_cvt_pk_bf16_f32 v64, v64, v65\n"
"v_cvt_pk_bf16_f32 v66, v66, v67\n"
"v_mfma_f32_16x16x32_bf16 v[228:231], a[76:79], a[124:127], v[228:231]\n"
"v_cvt_pk_bf16_f32 v80, v80, v81\n"
"v_cvt_pk_bf16_f32 v82, v82, v83\n"
"v_mfma_f32_16x16x32_bf16 v[232:235], a[76:79], a[128:131], v[232:235]\n"
"v_accvgpr_write_b32 a192, v64\n"
"v_accvgpr_write_b32 a193, v66\n"
"v_mfma_f32_16x16x32_bf16 v[236:239], a[76:79], a[132:135], v[236:239]\n"
"v_accvgpr_write_b32 a194, v80\n"
"v_accvgpr_write_b32 a195, v82\n"
"v_mfma_f32_16x16x32_bf16 v[240:243], a[80:83], a[120:123], v[240:243]\n"
"v_mul_f32 v68, v68, v51\n"
"v_mul_f32 v69, v69, v51\n"
"v_mfma_f32_16x16x32_bf16 v[244:247], a[80:83], a[124:127], v[244:247]\n"
"v_mul_f32 v70, v70, v51\n"
"v_mul_f32 v71, v71, v51\n"
"v_mfma_f32_16x16x32_bf16 v[248:251], a[80:83], a[128:131], v[248:251]\n"
"v_mul_f32 v84, v84, v51\n"
"v_mul_f32 v85, v85, v51\n"
"v_mfma_f32_16x16x32_bf16 v[252:255], a[80:83], a[132:135], v[252:255]\n"
"v_mul_f32 v86, v86, v51\n"
"v_mul_f32 v87, v87, v51\n"
"v_mfma_f32_16x16x32_bf16 v[128:131], a[84:87], a[120:123], v[128:131]\n"
"v_fmac_f32 v68, v176, v53\n"
"v_fmac_f32 v69, v177, v53\n"
"v_mfma_f32_16x16x32_bf16 v[132:135], a[84:87], a[124:127], v[132:135]\n"
"v_fmac_f32 v70, v178, v53\n"
"v_fmac_f32 v71, v179, v53\n"
"v_mfma_f32_16x16x32_bf16 v[136:139], a[84:87], a[128:131], v[136:139]\n"
"v_fmac_f32 v84, v180, v53\n"
"v_fmac_f32 v85, v181, v53\n"
"v_mfma_f32_16x16x32_bf16 v[140:143], a[84:87], a[132:135], v[140:143]\n"
"v_fmac_f32 v86, v182, v53\n"
"v_fmac_f32 v87, v183, v53\n"
"v_mfma_f32_16x16x32_bf16 v[144:147], a[88:91], a[120:123], v[144:147]\n"
"v_cvt_pk_bf16_f32 v68, v68, v69\n"
"v_cvt_pk_bf16_f32 v70, v70, v71\n"
"v_mfma_f32_16x16x32_bf16 v[148:151], a[88:91], a[124:127], v[148:151]\n"
"v_cvt_pk_bf16_f32 v84, v84, v85\n"
"v_cvt_pk_bf16_f32 v86, v86, v87\n"
"v_mfma_f32_16x16x32_bf16 v[152:155], a[88:91], a[128:131], v[152:155]\n"
"v_accvgpr_write_b32 a208, v68\n"
"v_accvgpr_write_b32 a209, v70\n"
"v_mfma_f32_16x16x32_bf16 v[156:159], a[88:91], a[132:135], v[156:159]\n"
"v_accvgpr_write_b32 a210, v84\n"
"v_accvgpr_write_b32 a211, v86\n"
"v_mfma_f32_16x16x32_bf16 v[160:163], a[92:95], a[120:123], v[160:163]\n"
"v_mfma_f32_16x16x32_bf16 v[164:167], a[92:95], a[124:127], v[164:167]\n"
"v_mfma_f32_16x16x32_bf16 v[168:171], a[96:99], a[120:123], v[168:171]\n"
"v_mfma_f32_16x16x32_bf16 v[172:175], a[96:99], a[124:127], v[172:175]\n"
"s_waitcnt lgkmcnt(0) vmcnt(5)\n"
"v_pk_mul_f32 v[72:73], v[72:73], v[54:55] op_sel:[0,1] op_sel_hi:[1,1]\n"
"v_pk_mul_f32 v[74:75], v[74:75], v[54:55] op_sel:[0,1] op_sel_hi:[1,1]\n"
"v_pk_mul_f32 v[88:89], v[88:89], v[54:55] op_sel:[0,1] op_sel_hi:[1,1]\n"
"v_pk_mul_f32 v[90:91], v[90:91], v[54:55] op_sel:[0,1] op_sel_hi:[1,1]\n"
"v_pk_fma_f32 v[72:73], v[176:177], v[56:57], v[72:73] op_sel:[0,1,0] op_sel_hi:[1,1,1]\n"
"v_pk_fma_f32 v[74:75], v[178:179], v[56:57], v[74:75] op_sel:[0,1,0] op_sel_hi:[1,1,1]\n"
"v_pk_fma_f32 v[88:89], v[180:181], v[56:57], v[88:89] op_sel:[0,1,0] op_sel_hi:[1,1,1]\n"
"v_pk_fma_f32 v[90:91], v[182:183], v[56:57], v[90:91] op_sel:[0,1,0] op_sel_hi:[1,1,1]\n"
"v_cvt_pk_bf16_f32 v72, v72, v73\n"
"v_cvt_pk_bf16_f32 v74, v74, v75\n"
"v_cvt_pk_bf16_f32 v88, v88, v89\n"
"v_cvt_pk_bf16_f32 v90, v90, v91\n"
"v_accvgpr_write_b32 a224, v72\n"
"v_accvgpr_write_b32 a225, v74\n"
"v_accvgpr_write_b32 a226, v88\n"
"v_accvgpr_write_b32 a227, v90\n"
"v_pk_mul_f32 v[76:77], v[76:77], v[58:59] op_sel:[0,1] op_sel_hi:[1,1]\n"
"v_pk_mul_f32 v[78:79], v[78:79], v[58:59] op_sel:[0,1] op_sel_hi:[1,1]\n"
"v_pk_mul_f32 v[92:93], v[92:93], v[58:59] op_sel:[0,1] op_sel_hi:[1,1]\n"
"v_pk_mul_f32 v[94:95], v[94:95], v[58:59] op_sel:[0,1] op_sel_hi:[1,1]\n"
"v_pk_fma_f32 v[76:77], v[176:177], v[60:61], v[76:77] op_sel:[0,1,0] op_sel_hi:[1,1,1]\n"
"v_pk_fma_f32 v[78:79], v[178:179], v[60:61], v[78:79] op_sel:[0,1,0] op_sel_hi:[1,1,1]\n"
"v_pk_fma_f32 v[92:93], v[180:181], v[60:61], v[92:93] op_sel:[0,1,0] op_sel_hi:[1,1,1]\n"
"v_pk_fma_f32 v[94:95], v[182:183], v[60:61], v[94:95] op_sel:[0,1,0] op_sel_hi:[1,1,1]\n"
"v_cvt_pk_bf16_f32 v76, v76, v77\n"
"v_cvt_pk_bf16_f32 v78, v78, v79\n"
"v_cvt_pk_bf16_f32 v92, v92, v93\n"
"v_cvt_pk_bf16_f32 v94, v94, v95\n"
"v_accvgpr_write_b32 a240, v76\n"
"v_accvgpr_write_b32 a241, v78\n"
"v_accvgpr_write_b32 a242, v92\n"
"v_accvgpr_write_b32 a243, v94\n"
"ds_read_b128 v[176:179], v23 offset:0\n"
"ds_read_b128 v[180:183], v23 offset:64\n"
"v_pk_mul_f32 v[96:97], v[96:97], v[46:47] op_sel:[0,1] op_sel_hi:[1,1]\n"
"v_pk_mul_f32 v[98:99], v[98:99], v[46:47] op_sel:[0,1] op_sel_hi:[1,1]\n"
"v_pk_mul_f32 v[112:113], v[112:113], v[46:47] op_sel:[0,1] op_sel_hi:[1,1]\n"
"v_pk_mul_f32 v[114:115], v[114:115], v[46:47] op_sel:[0,1] op_sel_hi:[1,1]\n"
"v_pk_fma_f32 v[96:97], v[184:185], v[48:49], v[96:97] op_sel:[0,1,0] op_sel_hi:[1,1,1]\n"
"v_pk_fma_f32 v[98:99], v[186:187], v[48:49], v[98:99] op_sel:[0,1,0] op_sel_hi:[1,1,1]\n"
"v_pk_fma_f32 v[112:113], v[188:189], v[48:49], v[112:113] op_sel:[0,1,0] op_sel_hi:[1,1,1]\n"
"v_pk_fma_f32 v[114:115], v[190:191], v[48:49], v[114:115] op_sel:[0,1,0] op_sel_hi:[1,1,1]\n"
"v_cvt_pk_bf16_f32 v96, v96, v97\n"
"v_cvt_pk_bf16_f32 v98, v98, v99\n"
"v_cvt_pk_bf16_f32 v112, v112, v113\n"
"v_cvt_pk_bf16_f32 v114, v114, v115\n"
"v_accvgpr_write_b32 a196, v96\n"
"v_accvgpr_write_b32 a197, v98\n"
"v_accvgpr_write_b32 a198, v112\n"
"v_accvgpr_write_b32 a199, v114\n"
"v_pk_mul_f32 v[100:101], v[100:101], v[50:51] op_sel:[0,1] op_sel_hi:[1,1]\n"
"v_pk_mul_f32 v[102:103], v[102:103], v[50:51] op_sel:[0,1] op_sel_hi:[1,1]\n"
"v_pk_mul_f32 v[116:117], v[116:117], v[50:51] op_sel:[0,1] op_sel_hi:[1,1]\n"
"v_pk_mul_f32 v[118:119], v[118:119], v[50:51] op_sel:[0,1] op_sel_hi:[1,1]\n"
"v_pk_fma_f32 v[100:101], v[184:185], v[52:53], v[100:101] op_sel:[0,1,0] op_sel_hi:[1,1,1]\n"
"v_pk_fma_f32 v[102:103], v[186:187], v[52:53], v[102:103] op_sel:[0,1,0] op_sel_hi:[1,1,1]\n"
"v_pk_fma_f32 v[116:117], v[188:189], v[52:53], v[116:117] op_sel:[0,1,0] op_sel_hi:[1,1,1]\n"
"v_pk_fma_f32 v[118:119], v[190:191], v[52:53], v[118:119] op_sel:[0,1,0] op_sel_hi:[1,1,1]\n"
"v_cvt_pk_bf16_f32 v100, v100, v101\n"
"v_cvt_pk_bf16_f32 v102, v102, v103\n"
"v_cvt_pk_bf16_f32 v116, v116, v117\n"
"v_cvt_pk_bf16_f32 v118, v118, v119\n"
"v_accvgpr_write_b32 a212, v100\n"
"v_accvgpr_write_b32 a213, v102\n"
"v_accvgpr_write_b32 a214, v116\n"
"v_accvgpr_write_b32 a215, v118\n"
"v_pk_mul_f32 v[104:105], v[104:105], v[54:55] op_sel:[0,1] op_sel_hi:[1,1]\n"
"v_pk_mul_f32 v[106:107], v[106:107], v[54:55] op_sel:[0,1] op_sel_hi:[1,1]\n"
"v_pk_mul_f32 v[120:121], v[120:121], v[54:55] op_sel:[0,1] op_sel_hi:[1,1]\n"
"v_pk_mul_f32 v[122:123], v[122:123], v[54:55] op_sel:[0,1] op_sel_hi:[1,1]\n"
"v_pk_fma_f32 v[104:105], v[184:185], v[56:57], v[104:105] op_sel:[0,1,0] op_sel_hi:[1,1,1]\n"
"v_pk_fma_f32 v[106:107], v[186:187], v[56:57], v[106:107] op_sel:[0,1,0] op_sel_hi:[1,1,1]\n"
"v_pk_fma_f32 v[120:121], v[188:189], v[56:57], v[120:121] op_sel:[0,1,0] op_sel_hi:[1,1,1]\n"
"v_pk_fma_f32 v[122:123], v[190:191], v[56:57], v[122:123] op_sel:[0,1,0] op_sel_hi:[1,1,1]\n"
"v_cvt_pk_bf16_f32 v104, v104, v105\n"
"v_cvt_pk_bf16_f32 v106, v106, v107\n"
"v_cvt_pk_bf16_f32 v120, v120, v121\n"
"v_cvt_pk_bf16_f32 v122, v122, v123\n"
"v_accvgpr_write_b32 a228, v104\n"
"v_accvgpr_write_b32 a229, v106\n"
"v_accvgpr_write_b32 a230, v120\n"
"v_accvgpr_write_b32 a231, v122\n"
"v_pk_mul_f32 v[108:109], v[108:109], v[58:59] op_sel:[0,1] op_sel_hi:[1,1]\n"
"v_pk_mul_f32 v[110:111], v[110:111], v[58:59] op_sel:[0,1] op_sel_hi:[1,1]\n"
"v_pk_mul_f32 v[124:125], v[124:125], v[58:59] op_sel:[0,1] op_sel_hi:[1,1]\n"
"v_pk_mul_f32 v[126:127], v[126:127], v[58:59] op_sel:[0,1] op_sel_hi:[1,1]\n"
"v_pk_fma_f32 v[108:109], v[184:185], v[60:61], v[108:109] op_sel:[0,1,0] op_sel_hi:[1,1,1]\n"
"v_pk_fma_f32 v[110:111], v[186:187], v[60:61], v[110:111] op_sel:[0,1,0] op_sel_hi:[1,1,1]\n"
"v_pk_fma_f32 v[124:125], v[188:189], v[60:61], v[124:125] op_sel:[0,1,0] op_sel_hi:[1,1,1]\n"
"v_pk_fma_f32 v[126:127], v[190:191], v[60:61], v[126:127] op_sel:[0,1,0] op_sel_hi:[1,1,1]\n"
"v_cvt_pk_bf16_f32 v108, v108, v109\n"
"v_cvt_pk_bf16_f32 v110, v110, v111\n"
"v_cvt_pk_bf16_f32 v124, v124, v125\n"
"v_cvt_pk_bf16_f32 v126, v126, v127\n"
"v_accvgpr_write_b32 a244, v108\n"
"v_accvgpr_write_b32 a245, v110\n"
"v_accvgpr_write_b32 a246, v124\n"
"v_accvgpr_write_b32 a247, v126\n"
"ds_read_b128 v[184:187], v24 offset:0\n"
"ds_read_b128 v[188:191], v24 offset:64\n"
"v_pk_mul_f32 v[192:193], v[192:193], v[46:47] op_sel:[0,1] op_sel_hi:[1,1]\n"
"v_pk_mul_f32 v[194:195], v[194:195], v[46:47] op_sel:[0,1] op_sel_hi:[1,1]\n"
"v_pk_mul_f32 v[208:209], v[208:209], v[46:47] op_sel:[0,1] op_sel_hi:[1,1]\n"
"v_pk_mul_f32 v[210:211], v[210:211], v[46:47] op_sel:[0,1] op_sel_hi:[1,1]\n"
"s_waitcnt lgkmcnt(3)\n"
"v_pk_fma_f32 v[192:193], v[176:177], v[48:49], v[192:193] op_sel:[0,1,0] op_sel_hi:[1,1,1]\n"
"v_pk_fma_f32 v[194:195], v[178:179], v[48:49], v[194:195] op_sel:[0,1,0] op_sel_hi:[1,1,1]\n"
"s_waitcnt lgkmcnt(2)\n"
"v_pk_fma_f32 v[208:209], v[180:181], v[48:49], v[208:209] op_sel:[0,1,0] op_sel_hi:[1,1,1]\n"
"v_pk_fma_f32 v[210:211], v[182:183], v[48:49], v[210:211] op_sel:[0,1,0] op_sel_hi:[1,1,1]\n"
"v_cvt_pk_bf16_f32 v192, v192, v193\n"
"v_cvt_pk_bf16_f32 v194, v194, v195\n"
"v_cvt_pk_bf16_f32 v208, v208, v209\n"
"v_cvt_pk_bf16_f32 v210, v210, v211\n"
"v_accvgpr_write_b32 a200, v192\n"
"v_accvgpr_write_b32 a201, v194\n"
"v_accvgpr_write_b32 a202, v208\n"
"v_accvgpr_write_b32 a203, v210\n"
"v_pk_mul_f32 v[196:197], v[196:197], v[50:51] op_sel:[0,1] op_sel_hi:[1,1]\n"
"v_pk_mul_f32 v[198:199], v[198:199], v[50:51] op_sel:[0,1] op_sel_hi:[1,1]\n"
"v_pk_mul_f32 v[212:213], v[212:213], v[50:51] op_sel:[0,1] op_sel_hi:[1,1]\n"
"v_pk_mul_f32 v[214:215], v[214:215], v[50:51] op_sel:[0,1] op_sel_hi:[1,1]\n"
"v_pk_fma_f32 v[196:197], v[176:177], v[52:53], v[196:197] op_sel:[0,1,0] op_sel_hi:[1,1,1]\n"
"v_pk_fma_f32 v[198:199], v[178:179], v[52:53], v[198:199] op_sel:[0,1,0] op_sel_hi:[1,1,1]\n"
"v_pk_fma_f32 v[212:213], v[180:181], v[52:53], v[212:213] op_sel:[0,1,0] op_sel_hi:[1,1,1]\n"
"v_pk_fma_f32 v[214:215], v[182:183], v[52:53], v[214:215] op_sel:[0,1,0] op_sel_hi:[1,1,1]\n"
"v_cvt_pk_bf16_f32 v196, v196, v197\n"
"v_cvt_pk_bf16_f32 v198, v198, v199\n"
"v_cvt_pk_bf16_f32 v212, v212, v213\n"
"v_cvt_pk_bf16_f32 v214, v214, v215\n"
"v_accvgpr_write_b32 a216, v196\n"
"v_accvgpr_write_b32 a217, v198\n"
"v_accvgpr_write_b32 a218, v212\n"
"v_accvgpr_write_b32 a219, v214\n"
"v_pk_mul_f32 v[200:201], v[200:201], v[54:55] op_sel:[0,1] op_sel_hi:[1,1]\n"
"v_pk_mul_f32 v[202:203], v[202:203], v[54:55] op_sel:[0,1] op_sel_hi:[1,1]\n"
"v_pk_mul_f32 v[216:217], v[216:217], v[54:55] op_sel:[0,1] op_sel_hi:[1,1]\n"
"v_pk_mul_f32 v[218:219], v[218:219], v[54:55] op_sel:[0,1] op_sel_hi:[1,1]\n"
"v_pk_fma_f32 v[200:201], v[176:177], v[56:57], v[200:201] op_sel:[0,1,0] op_sel_hi:[1,1,1]\n"
"v_pk_fma_f32 v[202:203], v[178:179], v[56:57], v[202:203] op_sel:[0,1,0] op_sel_hi:[1,1,1]\n"
"v_pk_fma_f32 v[216:217], v[180:181], v[56:57], v[216:217] op_sel:[0,1,0] op_sel_hi:[1,1,1]\n"
"v_pk_fma_f32 v[218:219], v[182:183], v[56:57], v[218:219] op_sel:[0,1,0] op_sel_hi:[1,1,1]\n"
"v_cvt_pk_bf16_f32 v200, v200, v201\n"
"v_cvt_pk_bf16_f32 v202, v202, v203\n"
"v_cvt_pk_bf16_f32 v216, v216, v217\n"
"v_cvt_pk_bf16_f32 v218, v218, v219\n"
"v_accvgpr_write_b32 a232, v200\n"
"v_accvgpr_write_b32 a233, v202\n"
"v_accvgpr_write_b32 a234, v216\n"
"v_accvgpr_write_b32 a235, v218\n"
"v_pk_mul_f32 v[204:205], v[204:205], v[58:59] op_sel:[0,1] op_sel_hi:[1,1]\n"
"v_pk_mul_f32 v[206:207], v[206:207], v[58:59] op_sel:[0,1] op_sel_hi:[1,1]\n"
"v_pk_mul_f32 v[220:221], v[220:221], v[58:59] op_sel:[0,1] op_sel_hi:[1,1]\n"
"v_pk_mul_f32 v[222:223], v[222:223], v[58:59] op_sel:[0,1] op_sel_hi:[1,1]\n"
"v_pk_fma_f32 v[204:205], v[176:177], v[60:61], v[204:205] op_sel:[0,1,0] op_sel_hi:[1,1,1]\n"
"v_pk_fma_f32 v[206:207], v[178:179], v[60:61], v[206:207] op_sel:[0,1,0] op_sel_hi:[1,1,1]\n"
"v_pk_fma_f32 v[220:221], v[180:181], v[60:61], v[220:221] op_sel:[0,1,0] op_sel_hi:[1,1,1]\n"
"v_pk_fma_f32 v[222:223], v[182:183], v[60:61], v[222:223] op_sel:[0,1,0] op_sel_hi:[1,1,1]\n"
"v_cvt_pk_bf16_f32 v204, v204, v205\n"
"v_cvt_pk_bf16_f32 v206, v206, v207\n"
"v_cvt_pk_bf16_f32 v220, v220, v221\n"
"v_cvt_pk_bf16_f32 v222, v222, v223\n"
"v_accvgpr_write_b32 a248, v204\n"
"v_accvgpr_write_b32 a249, v206\n"
"v_accvgpr_write_b32 a250, v220\n"
"v_accvgpr_write_b32 a251, v222\n"
"v_pk_mul_f32 v[224:225], v[224:225], v[46:47] op_sel:[0,1] op_sel_hi:[1,1]\n"
"v_pk_mul_f32 v[226:227], v[226:227], v[46:47] op_sel:[0,1] op_sel_hi:[1,1]\n"
"v_pk_mul_f32 v[240:241], v[240:241], v[46:47] op_sel:[0,1] op_sel_hi:[1,1]\n"
"v_pk_mul_f32 v[242:243], v[242:243], v[46:47] op_sel:[0,1] op_sel_hi:[1,1]\n"
"s_waitcnt lgkmcnt(1)\n"
"v_pk_fma_f32 v[224:225], v[184:185], v[48:49], v[224:225] op_sel:[0,1,0] op_sel_hi:[1,1,1]\n"
"v_pk_fma_f32 v[226:227], v[186:187], v[48:49], v[226:227] op_sel:[0,1,0] op_sel_hi:[1,1,1]\n"
"s_waitcnt lgkmcnt(0)\n"
"v_pk_fma_f32 v[240:241], v[188:189], v[48:49], v[240:241] op_sel:[0,1,0] op_sel_hi:[1,1,1]\n"
"v_pk_fma_f32 v[242:243], v[190:191], v[48:49], v[242:243] op_sel:[0,1,0] op_sel_hi:[1,1,1]\n"
"v_cvt_pk_bf16_f32 v224, v224, v225\n"
"v_cvt_pk_bf16_f32 v226, v226, v227\n"
"v_cvt_pk_bf16_f32 v240, v240, v241\n"
"v_cvt_pk_bf16_f32 v242, v242, v243\n"
"v_accvgpr_write_b32 a204, v224\n"
"v_accvgpr_write_b32 a205, v226\n"
"v_accvgpr_write_b32 a206, v240\n"
"v_accvgpr_write_b32 a207, v242\n"
"v_pk_mul_f32 v[228:229], v[228:229], v[50:51] op_sel:[0,1] op_sel_hi:[1,1]\n"
"v_pk_mul_f32 v[230:231], v[230:231], v[50:51] op_sel:[0,1] op_sel_hi:[1,1]\n"
"v_pk_mul_f32 v[244:245], v[244:245], v[50:51] op_sel:[0,1] op_sel_hi:[1,1]\n"
"v_pk_mul_f32 v[246:247], v[246:247], v[50:51] op_sel:[0,1] op_sel_hi:[1,1]\n"
"v_pk_fma_f32 v[228:229], v[184:185], v[52:53], v[228:229] op_sel:[0,1,0] op_sel_hi:[1,1,1]\n"
"v_pk_fma_f32 v[230:231], v[186:187], v[52:53], v[230:231] op_sel:[0,1,0] op_sel_hi:[1,1,1]\n"
"v_pk_fma_f32 v[244:245], v[188:189], v[52:53], v[244:245] op_sel:[0,1,0] op_sel_hi:[1,1,1]\n"
"v_pk_fma_f32 v[246:247], v[190:191], v[52:53], v[246:247] op_sel:[0,1,0] op_sel_hi:[1,1,1]\n"
"v_cvt_pk_bf16_f32 v228, v228, v229\n"
"v_cvt_pk_bf16_f32 v230, v230, v231\n"
"v_cvt_pk_bf16_f32 v244, v244, v245\n"
"v_cvt_pk_bf16_f32 v246, v246, v247\n"
"v_accvgpr_write_b32 a220, v228\n"
"v_accvgpr_write_b32 a221, v230\n"
"v_accvgpr_write_b32 a222, v244\n"
"v_accvgpr_write_b32 a223, v246\n"
"v_pk_mul_f32 v[232:233], v[232:233], v[54:55] op_sel:[0,1] op_sel_hi:[1,1]\n"
"v_pk_mul_f32 v[234:235], v[234:235], v[54:55] op_sel:[0,1] op_sel_hi:[1,1]\n"
"v_pk_mul_f32 v[248:249], v[248:249], v[54:55] op_sel:[0,1] op_sel_hi:[1,1]\n"
"v_pk_mul_f32 v[250:251], v[250:251], v[54:55] op_sel:[0,1] op_sel_hi:[1,1]\n"
"v_pk_fma_f32 v[232:233], v[184:185], v[56:57], v[232:233] op_sel:[0,1,0] op_sel_hi:[1,1,1]\n"
"v_pk_fma_f32 v[234:235], v[186:187], v[56:57], v[234:235] op_sel:[0,1,0] op_sel_hi:[1,1,1]\n"
"v_pk_fma_f32 v[248:249], v[188:189], v[56:57], v[248:249] op_sel:[0,1,0] op_sel_hi:[1,1,1]\n"
"v_pk_fma_f32 v[250:251], v[190:191], v[56:57], v[250:251] op_sel:[0,1,0] op_sel_hi:[1,1,1]\n"
"v_cvt_pk_bf16_f32 v232, v232, v233\n"
"v_cvt_pk_bf16_f32 v234, v234, v235\n"
"v_cvt_pk_bf16_f32 v248, v248, v249\n"
"v_cvt_pk_bf16_f32 v250, v250, v251\n"
"v_accvgpr_write_b32 a236, v232\n"
"v_accvgpr_write_b32 a237, v234\n"
"v_accvgpr_write_b32 a238, v248\n"
"v_accvgpr_write_b32 a239, v250\n"
"v_pk_mul_f32 v[236:237], v[236:237], v[58:59] op_sel:[0,1] op_sel_hi:[1,1]\n"
"v_pk_mul_f32 v[238:239], v[238:239], v[58:59] op_sel:[0,1] op_sel_hi:[1,1]\n"
"v_pk_mul_f32 v[252:253], v[252:253], v[58:59] op_sel:[0,1] op_sel_hi:[1,1]\n"
"v_pk_mul_f32 v[254:255], v[254:255], v[58:59] op_sel:[0,1] op_sel_hi:[1,1]\n"
"v_pk_fma_f32 v[236:237], v[184:185], v[60:61], v[236:237] op_sel:[0,1,0] op_sel_hi:[1,1,1]\n"
"v_pk_fma_f32 v[238:239], v[186:187], v[60:61], v[238:239] op_sel:[0,1,0] op_sel_hi:[1,1,1]\n"
"v_pk_fma_f32 v[252:253], v[188:189], v[60:61], v[252:253] op_sel:[0,1,0] op_sel_hi:[1,1,1]\n"
"v_pk_fma_f32 v[254:255], v[190:191], v[60:61], v[254:255] op_sel:[0,1,0] op_sel_hi:[1,1,1]\n"
"v_cvt_pk_bf16_f32 v236, v236, v237\n"
"v_cvt_pk_bf16_f32 v238, v238, v239\n"
"v_cvt_pk_bf16_f32 v252, v252, v253\n"
"v_cvt_pk_bf16_f32 v254, v254, v255\n"
"v_accvgpr_write_b32 a252, v236\n"
"v_accvgpr_write_b32 a253, v238\n"
"v_accvgpr_write_b32 a254, v252\n"
"v_accvgpr_write_b32 a255, v254\n"
"s_nop 0\n"
"v_mfma_f32_16x16x32_bf16 v[64:67], a[0:3], a[192:195], 0\n"
"ds_read_b128 v[176:179], v14 offset:0\n"
"v_mfma_f32_16x16x32_bf16 v[64:67], a[4:7], a[196:199], v[64:67]\n"
"ds_read_b128 v[180:183], v14 offset:64\n"
"v_mfma_f32_16x16x32_bf16 v[64:67], a[8:11], a[200:203], v[64:67]\n"
"ds_read_b128 v[184:187], v14 offset:128\n"
"v_mfma_f32_16x16x32_bf16 v[64:67], a[12:15], a[204:207], v[64:67]\n"
"ds_read_b128 v[188:191], v14 offset:192\n"
"v_mfma_f32_16x16x32_bf16 v[68:71], a[0:3], a[208:211], 0\n"
"v_lshlrev_b32 v96, 16, v6\n"
"v_and_b32 v97, 0xffff0000, v6\n"
"v_mfma_f32_16x16x32_bf16 v[68:71], a[4:7], a[212:215], v[68:71]\n"
"v_lshlrev_b32 v98, 16, v7\n"
"v_and_b32 v99, 0xffff0000, v7\n"
"v_mfma_f32_16x16x32_bf16 v[68:71], a[8:11], a[216:219], v[68:71]\n"
"v_lshlrev_b32 v100, 16, v8\n"
"v_and_b32 v101, 0xffff0000, v8\n"
"v_mfma_f32_16x16x32_bf16 v[68:71], a[12:15], a[220:223], v[68:71]\n"
"v_lshlrev_b32 v102, 16, v9\n"
"v_and_b32 v103, 0xffff0000, v9\n"
"v_mfma_f32_16x16x32_bf16 v[72:75], a[0:3], a[224:227], 0\n"
"v_lshlrev_b32 v104, 16, v10\n"
"v_and_b32 v105, 0xffff0000, v10\n"
"v_mfma_f32_16x16x32_bf16 v[72:75], a[4:7], a[228:231], v[72:75]\n"
"v_lshlrev_b32 v106, 16, v11\n"
"v_and_b32 v107, 0xffff0000, v11\n"
"v_mfma_f32_16x16x32_bf16 v[72:75], a[8:11], a[232:235], v[72:75]\n"
"s_waitcnt vmcnt(4)\n"
"v_lshlrev_b32 v108, 16, v12\n"
"v_and_b32 v109, 0xffff0000, v12\n"
"v_mfma_f32_16x16x32_bf16 v[72:75], a[12:15], a[236:239], v[72:75]\n"
"s_lshl_b32 s17, s4, 2\n"
"v_lshlrev_b32 v110, 16, v13\n"
"v_and_b32 v111, 0xffff0000, v13\n"
"v_mfma_f32_16x16x32_bf16 v[76:79], a[0:3], a[240:243], 0\n"
"s_lshl_b32 s16, s6, 4\n"
"ds_read_b64_tr_b16 a[64:65], v38 offset:0\n"
"ds_read_b64_tr_b16 a[66:67], v38 offset:4096\n"
"v_mfma_f32_16x16x32_bf16 v[76:79], a[4:7], a[244:247], v[76:79]\n"
"s_mov_b32 s95, 16384\n"
"ds_read_b64_tr_b16 a[68:69], v39 offset:0\n"
"ds_read_b64_tr_b16 a[70:71], v39 offset:4096\n"
"v_mfma_f32_16x16x32_bf16 v[76:79], a[8:11], a[248:251], v[76:79]\n"
"s_mov_b32 s91, 65536\n"
"ds_read_b64_tr_b16 a[72:73], v40 offset:0\n"
"ds_read_b64_tr_b16 a[74:75], v40 offset:4096\n"
"v_mfma_f32_16x16x32_bf16 v[76:79], a[12:15], a[252:255], v[76:79]\n"
"s_mov_b32 s90, 4294836224\n"
"ds_read_b64_tr_b16 a[76:77], v41 offset:0\n"
"ds_read_b64_tr_b16 a[78:79], v41 offset:4096\n"
"v_max3_f32 v64, v64, v65, v66\n"
"v_max_f32 v64, v64, v67\n"
"v_max3_f32 v68, v68, v69, v70\n"
"v_max_f32 v68, v68, v71\n"
"v_max3_f32 v72, v72, v73, v74\n"
"v_max_f32 v72, v72, v75\n"
"s_nop 0\n"
"v_max3_f32 v76, v76, v77, v78\n"
"v_max_f32 v76, v76, v79\n"
"v_mov_b32 v112, v64\n"
"v_mov_b32 v113, v68\n"
"v_mov_b32 v114, v72\n"
"v_mov_b32 v115, v76\n"
"s_nop 0\n"
"v_permlane16_swap_b32 v64, v112\n"
"v_permlane16_swap_b32 v68, v113\n"
"v_permlane16_swap_b32 v72, v114\n"
"v_permlane16_swap_b32 v76, v115\n"
"v_max_f32 v64, v64, v112\n"
"v_max_f32 v68, v68, v113\n"
"v_max_f32 v72, v72, v114\n"
"v_max_f32 v76, v76, v115\n"
"v_mov_b32 v112, v64\n"
"v_mov_b32 v113, v68\n"
"v_mov_b32 v114, v72\n"
"v_mov_b32 v115, v76\n"
"s_nop 0\n"
"v_permlane32_swap_b32 v64, v112\n"
"v_permlane32_swap_b32 v68, v113\n"
"v_permlane32_swap_b32 v72, v114\n"
"v_permlane32_swap_b32 v76, v115\n"
"v_max_f32 v64, v64, v112\n"
"v_max_f32 v68, v68, v113\n"
"v_max_f32 v72, v72, v114\n"
"v_max_f32 v76, v76, v115\n"
"v_xor_b32 v216, 0x80000000, v64\n"
"v_xor_b32 v217, 0x80000000, v64\n"
"v_xor_b32 v218, 0x80000000, v64\n"
"v_xor_b32 v219, 0x80000000, v64\n"
"v_xor_b32 v220, 0x80000000, v68\n"
"v_xor_b32 v221, 0x80000000, v68\n"
"v_xor_b32 v222, 0x80000000, v68\n"
"v_xor_b32 v223, 0x80000000, v68\n"
"v_xor_b32 v224, 0x80000000, v72\n"
"v_xor_b32 v225, 0x80000000, v72\n"
"v_xor_b32 v226, 0x80000000, v72\n"
"v_xor_b32 v227, 0x80000000, v72\n"
"v_xor_b32 v228, 0x80000000, v76\n"
"v_xor_b32 v229, 0x80000000, v76\n"
"v_xor_b32 v230, 0x80000000, v76\n"
"v_xor_b32 v231, 0x80000000, v76\n"
"v_mfma_f32_16x16x32_bf16 v[192:195], a[0:3], a[192:195], v[216:219]\n"
"s_mov_b32 s94, 16384\n"
"s_mov_b32 s93, 4294836224\n"
"s_mov_b32 s92, 65536\n"
"v_mfma_f32_16x16x32_bf16 v[192:195], a[4:7], a[196:199], v[192:195]\n"
"s_add_u32 s81, s16, s17\n"
"s_mov_b32 s80, 98304\n"
"ds_read_b128 a[32:35], v34 offset:8192\n"
"v_mfma_f32_16x16x32_bf16 v[192:195], a[8:11], a[200:203], v[192:195]\n"
"s_lshr_b32 s82, s7, 4\n"
"v_mfma_f32_16x16x32_bf16 v[192:195], a[12:15], a[204:207], v[192:195]\n"
"ds_read_b64_tr_b16 a[80:81], v42 offset:0\n"
"v_mfma_f32_16x16x32_bf16 v[196:199], a[16:19], a[192:195], v[216:219]\n"
"s_mov_b32 s47, 0x20000\n"
"v_mfma_f32_16x16x32_bf16 v[196:199], a[20:23], a[196:199], v[196:199]\n"
"s_mov_b32 s46, 1409024\n"
"s_mov_b32 s45, s35\n"
"ds_read_b64_tr_b16 a[82:83], v42 offset:4096\n"
"v_mfma_f32_16x16x32_bf16 v[196:199], a[24:27], a[200:203], v[196:199]\n"
"s_mov_b32 s44, s34\n"
"s_mul_i32 s42, s12, 360448\n"
"s_mov_b32 s83, 24182784\n"
"v_mfma_f32_16x16x32_bf16 v[196:199], a[28:31], a[204:207], v[196:199]\n"
"s_xor_b32 s16, s82, 0\n"
"s_add_u32 s16, s16, s81\n"
"s_waitcnt lgkmcnt(11)\n"
"v_pk_fma_f32 v[112:113], v[176:177], v[46:47], v[96:97] op_sel_hi:[1,0,1]\n"
"v_pk_fma_f32 v[114:115], v[178:179], v[46:47], v[98:99] op_sel_hi:[1,0,1]\n"
"v_pk_fma_f32 v[112:113], v[128:129], v[48:49], v[112:113] op_sel_hi:[1,0,1]\n"
"v_pk_fma_f32 v[114:115], v[130:131], v[48:49], v[114:115] op_sel_hi:[1,0,1]\n"
"v_pk_fma_f32 v[116:117], v[180:181], v[46:47], v[100:101] op_sel_hi:[1,0,1]\n"
"v_pk_fma_f32 v[118:119], v[182:183], v[46:47], v[102:103] op_sel_hi:[1,0,1]\n"
"v_pk_fma_f32 v[116:117], v[144:145], v[48:49], v[116:117] op_sel_hi:[1,0,1]\n"
"v_pk_fma_f32 v[118:119], v[146:147], v[48:49], v[118:119] op_sel_hi:[1,0,1]\n"
"v_max_f32 v112, 0, v112\n"
"v_max_f32 v113, 0, v113\n"
"v_max_f32 v114, 0, v114\n"
"v_max_f32 v115, 0, v115\n"
"v_max_f32 v116, 0, v116\n"
"v_max_f32 v117, 0, v117\n"
"v_max_f32 v118, 0, v118\n"
"v_max_f32 v119, 0, v119\n"
"s_mul_i32 s16, s16, 44\n"
"s_add_u32 s16, s16, s5\n"
"s_lshl_b32 s16, s16, 10\n"
"s_add_u32 s22, s16, 17825792\n"
"v_pk_mul_f32 v[112:113], v[112:113], v[112:113]\n"
"v_pk_mul_f32 v[114:115], v[114:115], v[114:115]\n"
"v_pk_mul_f32 v[116:117], v[116:117], v[116:117]\n"
"v_pk_mul_f32 v[118:119], v[118:119], v[118:119]\n"
"v_cvt_pk_bf16_f32 v128, v112, v113\n"
"v_cvt_pk_bf16_f32 v129, v114, v115\n"
"v_cvt_pk_bf16_f32 v130, v116, v117\n"
"v_cvt_pk_bf16_f32 v131, v118, v119\n"
"buffer_store_dwordx4 v[128:131], v4, s[64:67], s22 offen sc1\n"
"v_pk_fma_f32 v[112:113], v[176:177], v[50:51], v[96:97] op_sel_hi:[1,0,1]\n"
"v_pk_fma_f32 v[114:115], v[178:179], v[50:51], v[98:99] op_sel_hi:[1,0,1]\n"
"v_pk_fma_f32 v[112:113], v[132:133], v[52:53], v[112:113] op_sel_hi:[1,0,1]\n"
"v_pk_fma_f32 v[114:115], v[134:135], v[52:53], v[114:115] op_sel_hi:[1,0,1]\n"
"v_pk_fma_f32 v[116:117], v[180:181], v[50:51], v[100:101] op_sel_hi:[1,0,1]\n"
"v_pk_fma_f32 v[118:119], v[182:183], v[50:51], v[102:103] op_sel_hi:[1,0,1]\n"
"v_pk_fma_f32 v[116:117], v[148:149], v[52:53], v[116:117] op_sel_hi:[1,0,1]\n"
"v_pk_fma_f32 v[118:119], v[150:151], v[52:53], v[118:119] op_sel_hi:[1,0,1]\n"
"v_max_f32 v112, 0, v112\n"
"v_max_f32 v113, 0, v113\n"
"v_max_f32 v114, 0, v114\n"
"v_max_f32 v115, 0, v115\n"
"v_max_f32 v116, 0, v116\n"
"v_max_f32 v117, 0, v117\n"
"v_max_f32 v118, 0, v118\n"
"v_max_f32 v119, 0, v119\n"
"s_xor_b32 s16, s82, 1\n"
"s_add_u32 s16, s16, s81\n"
"s_mul_i32 s16, s16, 44\n"
"s_add_u32 s16, s16, s5\n"
"s_lshl_b32 s16, s16, 10\n"
"s_add_u32 s23, s16, 17825792\n"
"v_pk_mul_f32 v[112:113], v[112:113], v[112:113]\n"
"v_pk_mul_f32 v[114:115], v[114:115], v[114:115]\n"
"v_pk_mul_f32 v[116:117], v[116:117], v[116:117]\n"
"v_pk_mul_f32 v[118:119], v[118:119], v[118:119]\n"
"v_cvt_pk_bf16_f32 v132, v112, v113\n"
"v_cvt_pk_bf16_f32 v133, v114, v115\n"
"v_cvt_pk_bf16_f32 v134, v116, v117\n"
"v_cvt_pk_bf16_f32 v135, v118, v119\n"
"v_pk_fma_f32 v[112:113], v[176:177], v[54:55], v[96:97] op_sel_hi:[1,0,1]\n"
"v_pk_fma_f32 v[114:115], v[178:179], v[54:55], v[98:99] op_sel_hi:[1,0,1]\n"
"v_pk_fma_f32 v[112:113], v[136:137], v[56:57], v[112:113] op_sel_hi:[1,0,1]\n"
"v_pk_fma_f32 v[114:115], v[138:139], v[56:57], v[114:115] op_sel_hi:[1,0,1]\n"
"v_pk_fma_f32 v[116:117], v[180:181], v[54:55], v[100:101] op_sel_hi:[1,0,1]\n"
"v_pk_fma_f32 v[118:119], v[182:183], v[54:55], v[102:103] op_sel_hi:[1,0,1]\n"
"v_pk_fma_f32 v[116:117], v[152:153], v[56:57], v[116:117] op_sel_hi:[1,0,1]\n"
"v_pk_fma_f32 v[118:119], v[154:155], v[56:57], v[118:119] op_sel_hi:[1,0,1]\n"
"v_max_f32 v112, 0, v112\n"
"v_max_f32 v113, 0, v113\n"
"v_max_f32 v114, 0, v114\n"
"v_max_f32 v115, 0, v115\n"
"v_max_f32 v116, 0, v116\n"
"v_max_f32 v117, 0, v117\n"
"v_max_f32 v118, 0, v118\n"
"v_max_f32 v119, 0, v119\n"
"s_xor_b32 s16, s82, 2\n"
"s_add_u32 s16, s16, s81\n"
"s_mul_i32 s16, s16, 44\n"
"s_add_u32 s16, s16, s5\n"
"s_lshl_b32 s16, s16, 10\n"
"s_add_u32 s24, s16, 17825792\n"
"v_pk_mul_f32 v[112:113], v[112:113], v[112:113]\n"
"v_pk_mul_f32 v[114:115], v[114:115], v[114:115]\n"
"v_pk_mul_f32 v[116:117], v[116:117], v[116:117]\n"
"v_pk_mul_f32 v[118:119], v[118:119], v[118:119]\n"
"v_cvt_pk_bf16_f32 v136, v112, v113\n"
"v_cvt_pk_bf16_f32 v137, v114, v115\n"
"v_cvt_pk_bf16_f32 v138, v116, v117\n"
"v_cvt_pk_bf16_f32 v139, v118, v119\n"
"v_pk_fma_f32 v[112:113], v[176:177], v[58:59], v[96:97] op_sel_hi:[1,0,1]\n"
"v_pk_fma_f32 v[114:115], v[178:179], v[58:59], v[98:99] op_sel_hi:[1,0,1]\n"
"v_pk_fma_f32 v[112:113], v[140:141], v[60:61], v[112:113] op_sel_hi:[1,0,1]\n"
"v_pk_fma_f32 v[114:115], v[142:143], v[60:61], v[114:115] op_sel_hi:[1,0,1]\n"
"v_pk_fma_f32 v[116:117], v[180:181], v[58:59], v[100:101] op_sel_hi:[1,0,1]\n"
"v_pk_fma_f32 v[118:119], v[182:183], v[58:59], v[102:103] op_sel_hi:[1,0,1]\n"
"v_pk_fma_f32 v[116:117], v[156:157], v[60:61], v[116:117] op_sel_hi:[1,0,1]\n"
"v_pk_fma_f32 v[118:119], v[158:159], v[60:61], v[118:119] op_sel_hi:[1,0,1]\n"
"v_max_f32 v112, 0, v112\n"
"v_max_f32 v113, 0, v113\n"
"v_max_f32 v114, 0, v114\n"
"v_max_f32 v115, 0, v115\n"
"v_max_f32 v116, 0, v116\n"
"v_max_f32 v117, 0, v117\n"
"v_max_f32 v118, 0, v118\n"
"v_max_f32 v119, 0, v119\n"
"s_xor_b32 s16, s82, 3\n"
"s_add_u32 s16, s16, s81\n"
"s_mul_i32 s16, s16, 44\n"
"s_add_u32 s16, s16, s5\n"
"s_lshl_b32 s16, s16, 10\n"
"s_add_u32 s25, s16, 17825792\n"
"v_pk_mul_f32 v[112:113], v[112:113], v[112:113]\n"
"v_pk_mul_f32 v[114:115], v[114:115], v[114:115]\n"
"v_pk_mul_f32 v[116:117], v[116:117], v[116:117]\n"
"v_pk_mul_f32 v[118:119], v[118:119], v[118:119]\n"
"v_cvt_pk_bf16_f32 v140, v112, v113\n"
"v_cvt_pk_bf16_f32 v141, v114, v115\n"
"v_cvt_pk_bf16_f32 v142, v116, v117\n"
"v_cvt_pk_bf16_f32 v143, v118, v119\n"
"v_pk_fma_f32 v[112:113], v[184:185], v[46:47], v[104:105] op_sel_hi:[1,0,1]\n"
"v_pk_fma_f32 v[114:115], v[186:187], v[46:47], v[106:107] op_sel_hi:[1,0,1]\n"
"v_pk_fma_f32 v[112:113], v[160:161], v[48:49], v[112:113] op_sel_hi:[1,0,1]\n"
"v_pk_fma_f32 v[114:115], v[162:163], v[48:49], v[114:115] op_sel_hi:[1,0,1]\n"
"v_pk_fma_f32 v[116:117], v[188:189], v[46:47], v[108:109] op_sel_hi:[1,0,1]\n"
"v_pk_fma_f32 v[118:119], v[190:191], v[46:47], v[110:111] op_sel_hi:[1,0,1]\n"
"v_pk_fma_f32 v[116:117], v[168:169], v[48:49], v[116:117] op_sel_hi:[1,0,1]\n"
"v_pk_fma_f32 v[118:119], v[170:171], v[48:49], v[118:119] op_sel_hi:[1,0,1]\n"
"v_max_f32 v112, 0, v112\n"
"v_max_f32 v113, 0, v113\n"
"v_max_f32 v114, 0, v114\n"
"v_max_f32 v115, 0, v115\n"
"v_max_f32 v116, 0, v116\n"
"v_max_f32 v117, 0, v117\n"
"v_max_f32 v118, 0, v118\n"
"v_max_f32 v119, 0, v119\n"
"s_xor_b32 s16, s82, 0\n"
"s_add_u32 s16, s16, s81\n"
"s_mul_i32 s16, s16, 44\n"
"s_add_u32 s16, s16, s9\n"
"s_lshl_b32 s16, s16, 10\n"
"s_add_u32 s26, s16, 17825792\n"
"v_pk_mul_f32 v[112:113], v[112:113], v[112:113]\n"
"v_pk_mul_f32 v[114:115], v[114:115], v[114:115]\n"
"v_pk_mul_f32 v[116:117], v[116:117], v[116:117]\n"
"v_pk_mul_f32 v[118:119], v[118:119], v[118:119]\n"
"v_cvt_pk_bf16_f32 v160, v112, v113\n"
"v_cvt_pk_bf16_f32 v161, v114, v115\n"
"v_cvt_pk_bf16_f32 v162, v116, v117\n"
"v_cvt_pk_bf16_f32 v163, v118, v119\n"
"buffer_store_dwordx4 v[160:163], v4, s[68:71], s26 offen sc1\n"
"v_pk_fma_f32 v[112:113], v[184:185], v[50:51], v[104:105] op_sel_hi:[1,0,1]\n"
"v_pk_fma_f32 v[114:115], v[186:187], v[50:51], v[106:107] op_sel_hi:[1,0,1]\n"
"v_pk_fma_f32 v[112:113], v[164:165], v[52:53], v[112:113] op_sel_hi:[1,0,1]\n"
"v_pk_fma_f32 v[114:115], v[166:167], v[52:53], v[114:115] op_sel_hi:[1,0,1]\n"
"v_pk_fma_f32 v[116:117], v[188:189], v[50:51], v[108:109] op_sel_hi:[1,0,1]\n"
"v_pk_fma_f32 v[118:119], v[190:191], v[50:51], v[110:111] op_sel_hi:[1,0,1]\n"
"v_pk_fma_f32 v[116:117], v[172:173], v[52:53], v[116:117] op_sel_hi:[1,0,1]\n"
"v_pk_fma_f32 v[118:119], v[174:175], v[52:53], v[118:119] op_sel_hi:[1,0,1]\n"
"v_max_f32 v112, 0, v112\n"
"v_max_f32 v113, 0, v113\n"
"v_max_f32 v114, 0, v114\n"
"v_max_f32 v115, 0, v115\n"
"v_max_f32 v116, 0, v116\n"
"v_max_f32 v117, 0, v117\n"
"v_max_f32 v118, 0, v118\n"
"v_max_f32 v119, 0, v119\n"
"s_xor_b32 s16, s82, 1\n"
"s_add_u32 s16, s16, s81\n"
"s_mul_i32 s16, s16, 44\n"
"s_add_u32 s16, s16, s9\n"
"s_lshl_b32 s16, s16, 10\n"
"s_add_u32 s27, s16, 17825792\n"
"v_pk_mul_f32 v[112:113], v[112:113], v[112:113]\n"
"v_pk_mul_f32 v[114:115], v[114:115], v[114:115]\n"
"v_pk_mul_f32 v[116:117], v[116:117], v[116:117]\n"
"v_pk_mul_f32 v[118:119], v[118:119], v[118:119]\n"
"v_cvt_pk_bf16_f32 v164, v112, v113\n"
"v_cvt_pk_bf16_f32 v165, v114, v115\n"
"v_cvt_pk_bf16_f32 v166, v116, v117\n"
"v_cvt_pk_bf16_f32 v167, v118, v119\n"
"buffer_store_dwordx4 v[164:167], v4, s[68:71], s27 offen sc1\n"
"s_lshl_b32 s16, s81, 3\n"
"s_add_u32 s16, s16, s5\n"
"s_lshl_b32 s16, s16, 2\n"
"v_mov_b32 v6, s16\n"
"v_mov_b32 v7, s8\n"
"s_lshl_b32 s16, s10, 8\n"
"s_add_u32 s16, s16, 24182784\n"
"v_lshl_add_u32 v8, v1, 2, s16\n"
"s_lshr_b32 s16, %1, 3\n"
"s_lshl_b32 s16, s16, 2\n"
"s_add_u32 s16, s16, s4\n"
"v_lshlrev_b32 v27, 7, v1\n"
"s_mul_i32 s17, s16, 2816\n"
"s_add_u32 s17, s17, s42\n"
"s_mov_b64 exec, 4194303\n"
"buffer_load_dword v0, v27, s[44:47], s17 offen\n"
"s_mov_b64 exec, -1\n"
"v_cmp_gt_u32 vcc, 16, v1\n"
"s_mov_b32 s44, s28\n"
"s_mov_b32 s45, s29\n"
"s_mov_b32 s46, 4194304\n"
"s_mov_b32 s47, 0x20000\n"
"v_mov_b32 v5, 0x40000000\n"
"v_cndmask_b32 v27, v5, v27, vcc\n"
"s_lshl_b32 s55, s16, 13\n"
"s_lshl_b32 s42, s12, 20\n"
"s_add_u32 s55, s55, s42\n"
"ds_read_b128 a[36:39], v35 offset:8192\n"
"ds_read_b64_tr_b16 a[84:85], v43 offset:0\n"
"ds_read_b64_tr_b16 a[86:87], v43 offset:4096\n"
"v_mfma_f32_16x16x32_bf16 v[200:203], a[0:3], a[208:211], v[220:223]\n"
"s_mov_b32 s74, 1408256\n"
"v_exp_f32 v192, v192\n"
"v_mfma_f32_16x16x32_bf16 v[200:203], a[4:7], a[212:215], v[200:203]\n"
"s_lshl_b32 s16, s11, 8\n"
"ds_read_b128 a[40:43], v36 offset:8192\n"
"s_add_u32 s72, s34, s16\n"
"s_addc_u32 s73, s35, 0\n"
"s_lshl_b32 s17, s12, 20\n"
"s_add_u32 s16, s16, s17\n"
"s_add_u32 s76, s28, s16\n"
"s_addc_u32 s77, s29, 0\n"
"s_mov_b32 s16, 0\n"
"s_lshl_b32 s86, s16, 15\n"
"s_lshl_b32 s49, s16, 10\n"
"s_and_b32 s16, s5, 7\n"
"s_lshl_b32 s30, s16, 17\n"
"s_lshl_b32 s31, s16, 12\n"
"s_cmp_eq_u32 s16, 0\n"
"s_cselect_b32 s49, 4096, 0\n"
"s_mul_i32 s48, s10, 360448\n"
"s_mul_i32 s16, s12, 11264\n"
"s_add_u32 s48, s48, s16\n"
"s_mul_i32 s17, s10, 1089536\n"
"s_lshl_b32 s16, s12, 15\n"
"s_add_u32 s16, s17, s16\n"
"s_add_u32 s68, s40, s16\n"
"s_addc_u32 s69, s41, 0\n"
"s_lshl_b32 s17, s4, 13\n"
"v_add_u32 v24, 0, v3\n"
"v_mfma_f32_16x16x32_bf16 v[200:203], a[8:11], a[216:219], v[200:203]\n"
"s_mov_b32 s75, 0x20000\n"
"v_exp_f32 v193, v193\n"
"v_mfma_f32_16x16x32_bf16 v[200:203], a[12:15], a[220:223], v[200:203]\n"
"s_mov_b32 s78, 1047808\n"
"ds_read_b64_tr_b16 a[88:89], v44 offset:0\n"
"v_lshlrev_b32 v25, 1, v24\n"
"v_mfma_f32_16x16x32_bf16 v[204:207], a[16:19], a[208:211], v[220:223]\n"
"s_mov_b32 s79, 0x20000\n"
"v_exp_f32 v194, v194\n"
"v_mfma_f32_16x16x32_bf16 v[204:207], a[20:23], a[212:215], v[204:207]\n"
"s_lshl_b32 s84, s4, 11\n"
"ds_read_b64_tr_b16 a[90:91], v44 offset:4096\n"
"v_cvt_pk_bf16_f32 v208, v192, v193\n"
"v_mfma_f32_16x16x32_bf16 v[204:207], a[24:27], a[216:219], v[204:207]\n"
"s_mul_i32 s85, s12, 360448\n"
"v_exp_f32 v195, v195\n"
"v_mfma_f32_16x16x32_bf16 v[204:207], a[28:31], a[220:223], v[204:207]\n"
"s_mov_b32 s22, s30\n"
"buffer_store_dwordx4 v[132:135], v4, s[64:67], s23 offen sc1\n"
"s_nop 0\n"
"v_exp_f32 v196, v196\n"
"s_nop 0\n"
"ds_read_b128 a[44:47], v37 offset:8192\n"
"v_cvt_pk_bf16_f32 v209, v194, v195\n"
"s_nop 0\n"
"v_exp_f32 v197, v197\n"
"s_nop 0\n"
"ds_read_b64_tr_b16 a[92:93], v45 offset:0\n"
"v_xor_b32 v25, v25, v2\n"
"s_nop 0\n"
"v_exp_f32 v198, v198\n"
"s_nop 0\n"
"ds_read_b64_tr_b16 a[94:95], v45 offset:4096\n"
"v_cvt_pk_bf16_f32 v210, v196, v197\n"
"s_nop 0\n"
"v_exp_f32 v199, v199\n"
"s_nop 0\n"
"v_lshlrev_b32 v25, 4, v25\n"
"v_lshl_add_u32 v25, v24, 10, v25\n"
"s_sub_u32 s16, s17, 0\n"
"s_nop 0\n"
"v_add_u32 v10, s16, v25\n"
"v_cvt_pk_bf16_f32 v211, v198, v199\n"
"v_mfma_f32_16x16x32_bf16 v[192:195], a[0:3], a[224:227], v[224:227]\n"
"s_lshl_b32 s86, s49, 5\n"
"v_exp_f32 v200, v200\n"
"v_mfma_f32_16x16x32_bf16 v[192:195], a[4:7], a[228:231], v[192:195]\n"
"s_add_u32 s48, s48, 17825792\n"
"ds_read_b128 a[48:51], v34 offset:12288\n"
"v_add_u32 v24, 4, v3\n"
"v_mfma_f32_16x16x32_bf16 v[192:195], a[8:11], a[232:235], v[192:195]\n"
"s_mov_b32 s70, 1089536\n"
"v_exp_f32 v201, v201\n"
"v_mfma_f32_16x16x32_bf16 v[192:195], a[12:15], a[236:239], v[192:195]\n"
"s_mov_b32 s71, 0x20000\n"
"ds_read_b64_tr_b16 a[96:97], v38 offset:8192\n"
"v_lshlrev_b32 v25, 1, v24\n"
"v_mfma_f32_16x16x32_bf16 v[196:199], a[16:19], a[224:227], v[224:227]\n"
"v_exp_f32 v202, v202\n"
"v_mfma_f32_16x16x32_bf16 v[196:199], a[20:23], a[228:231], v[196:199]\n"
"ds_read_b64_tr_b16 a[98:99], v38 offset:12288\n"
"v_cvt_pk_bf16_f32 v212, v200, v201\n"
"v_mfma_f32_16x16x32_bf16 v[196:199], a[24:27], a[232:235], v[196:199]\n"
"v_exp_f32 v203, v203\n"
"v_mfma_f32_16x16x32_bf16 v[196:199], a[28:31], a[236:239], v[196:199]\n"
"s_waitcnt lgkmcnt(3)\n"
"buffer_store_dwordx4 v[136:139], v4, s[64:67], s24 offen sc1\n"
"v_add_u32 v38, s93, v38\n"
"v_mfma_f32_16x16x32_bf16 v[64:67], a[64:67], v[208:211], 0\n"
"v_exp_f32 v204, v204\n"
"v_mfma_f32_16x16x32_bf16 v[68:71], a[68:71], v[208:211], 0\n"
"ds_read_b128 a[52:55], v35 offset:12288\n"
"v_cvt_pk_bf16_f32 v213, v202, v203\n"
"v_mfma_f32_16x16x32_bf16 v[72:75], a[72:75], v[208:211], 0\n"
"v_exp_f32 v205, v205\n"
"v_mfma_f32_16x16x32_bf16 v[76:79], a[76:79], v[208:211], 0\n"
"ds_read_b64_tr_b16 a[100:101], v39 offset:8192\n"
"v_xor_b32 v25, v25, v2\n"
"v_mfma_f32_16x16x32_bf16 v[80:83], a[80:83], v[208:211], 0\n"
"v_exp_f32 v206, v206\n"
"v_mfma_f32_16x16x32_bf16 v[84:87], a[84:87], v[208:211], 0\n"
"ds_read_b64_tr_b16 a[102:103], v39 offset:12288\n"
"v_cvt_pk_bf16_f32 v214, v204, v205\n"
"v_mfma_f32_16x16x32_bf16 v[88:91], a[88:91], v[208:211], 0\n"
"v_exp_f32 v207, v207\n"
"v_mfma_f32_16x16x32_bf16 v[92:95], a[92:95], v[208:211], 0\n"
"v_add_u32 v34, s90, v34\n"
"v_add_u32 v35, s90, v35\n"
"v_mfma_f32_16x16x32_bf16 v[232:235], a[136:139], v[208:211], 0\n"
"v_cvt_pk_bf16_f32 v215, v206, v207\n"
"v_add_u32 v39, s93, v39\n"
"v_mfma_f32_16x16x32_bf16 v[200:203], a[0:3], a[240:243], v[228:231]\n"
"v_exp_f32 v192, v192\n"
"v_mfma_f32_16x16x32_bf16 v[200:203], a[4:7], a[244:247], v[200:203]\n"
"ds_read_b128 a[56:59], v36 offset:12288\n"
"v_lshlrev_b32 v25, 4, v25\n"
"v_mfma_f32_16x16x32_bf16 v[200:203], a[8:11], a[248:251], v[200:203]\n"
"v_exp_f32 v193, v193\n"
"v_mfma_f32_16x16x32_bf16 v[200:203], a[12:15], a[252:255], v[200:203]\n"
"s_sub_u32 s16, s17, 1024\n"
"ds_read_b64_tr_b16 a[104:105], v40 offset:8192\n"
"v_lshl_add_u32 v25, v24, 10, v25\n"
"v_mfma_f32_16x16x32_bf16 v[204:207], a[16:19], a[240:243], v[228:231]\n"
"v_exp_f32 v194, v194\n"
"v_mfma_f32_16x16x32_bf16 v[204:207], a[20:23], a[244:247], v[204:207]\n"
"ds_read_b64_tr_b16 a[106:107], v40 offset:12288\n"
"v_cvt_pk_bf16_f32 v208, v192, v193\n"
"v_mfma_f32_16x16x32_bf16 v[204:207], a[24:27], a[248:251], v[204:207]\n"
"v_exp_f32 v195, v195\n"
"v_mfma_f32_16x16x32_bf16 v[204:207], a[28:31], a[252:255], v[204:207]\n"
"buffer_store_dwordx4 v[140:143], v4, s[64:67], s25 offen sc1\n"
"v_add_u32 v40, s93, v40\n"
"v_mfma_f32_16x16x32_bf16 v[96:99], a[64:67], v[212:215], 0\n"
"v_exp_f32 v196, v196\n"
"v_mfma_f32_16x16x32_bf16 v[100:103], a[68:71], v[212:215], 0\n"
"ds_read_b128 a[60:63], v37 offset:12288\n"
"v_cvt_pk_bf16_f32 v209, v194, v195\n"
"v_mfma_f32_16x16x32_bf16 v[104:107], a[72:75], v[212:215], 0\n"
"v_exp_f32 v197, v197\n"
"v_mfma_f32_16x16x32_bf16 v[108:111], a[76:79], v[212:215], 0\n"
"ds_read_b64_tr_b16 a[108:109], v41 offset:8192\n"
"v_add_u32 v11, s16, v25\n"
"v_mfma_f32_16x16x32_bf16 v[112:115], a[80:83], v[212:215], 0\n"
"s_mul_i32 s16, s4, 90112\n"
"v_exp_f32 v198, v198\n"
"v_mfma_f32_16x16x32_bf16 v[116:119], a[84:87], v[212:215], 0\n"
"ds_read_b64_tr_b16 a[110:111], v41 offset:12288\n"
"v_cvt_pk_bf16_f32 v210, v196, v197\n"
"v_mfma_f32_16x16x32_bf16 v[120:123], a[88:91], v[212:215], 0\n"
"v_exp_f32 v199, v199\n"
"v_mfma_f32_16x16x32_bf16 v[124:127], a[92:95], v[212:215], 0\n"
"s_mov_b32 s43, s90\n"
"v_add_u32 v36, s90, v36\n"
"v_add_u32 v37, s90, v37\n"
"s_mov_b32 s90, s91\n"
"s_mov_b32 s91, s92\n"
"v_mfma_f32_16x16x32_bf16 v[236:239], a[136:139], v[212:215], 0\n"
"s_mov_b32 s92, s43\n"
"v_cvt_pk_bf16_f32 v211, v198, v199\n"
"v_add_u32 v41, s93, v41\n"
"s_waitcnt lgkmcnt(2)\n"
"s_barrier\n"
"v_mfma_f32_16x16x32_bf16 v[192:195], a[32:35], a[192:195], v[216:219]\n"
"v_exp_f32 v200, v200\n"
"v_mfma_f32_16x16x32_bf16 v[192:195], a[36:39], a[196:199], v[192:195]\n"
"ds_read_b128 a[0:3], v34 offset:0\n"
"v_add_u32 v12, s16, v4\n"
"v_mfma_f32_16x16x32_bf16 v[192:195], a[40:43], a[200:203], v[192:195]\n"
"v_exp_f32 v201, v201\n"
"v_mfma_f32_16x16x32_bf16 v[192:195], a[44:47], a[204:207], v[192:195]\n"
"s_mul_i32 s16, s4, 272384\n"
"ds_read_b64_tr_b16 a[112:113], v42 offset:8192\n"
"v_add_u32 v13, 44032, v12\n"
"v_mfma_f32_16x16x32_bf16 v[196:199], a[48:51], a[192:195], v[216:219]\n"
"v_exp_f32 v202, v202\n"
"v_mfma_f32_16x16x32_bf16 v[196:199], a[52:55], a[196:199], v[196:199]\n"
"ds_read_b64_tr_b16 a[114:115], v42 offset:12288\n"
"v_cvt_pk_bf16_f32 v212, v200, v201\n"
"v_mfma_f32_16x16x32_bf16 v[196:199], a[56:59], a[200:203], v[196:199]\n"
"s_mov_b32 m0, s88\n"
"v_exp_f32 v203, v203\n"
"v_mfma_f32_16x16x32_bf16 v[196:199], a[60:63], a[204:207], v[196:199]\n"
"buffer_load_dwordx4 v28, s[56:59], s87 offen lds\n"
"v_add_u32 v42, s93, v42\n"
"v_mfma_f32_16x16x32_bf16 v[128:131], a[64:67], v[208:211], 0\n"
"v_exp_f32 v204, v204\n"
"v_mfma_f32_16x16x32_bf16 v[132:135], a[68:71], v[208:211], 0\n"
"ds_read_b128 a[4:7], v35 offset:0\n"
"v_cvt_pk_bf16_f32 v213, v202, v203\n"
"v_mfma_f32_16x16x32_bf16 v[136:139], a[72:75], v[208:211], 0\n"
"v_exp_f32 v205, v205\n"
"v_mfma_f32_16x16x32_bf16 v[140:143], a[76:79], v[208:211], 0\n"
"ds_read_b64_tr_b16 a[116:117], v43 offset:8192\n"
"v_add_u32 v14, s16, v4\n"
"v_mfma_f32_16x16x32_bf16 v[144:147], a[80:83], v[208:211], 0\n"
"v_exp_f32 v206, v206\n"
"v_mfma_f32_16x16x32_bf16 v[148:151], a[84:87], v[208:211], 0\n"
"ds_read_b64_tr_b16 a[118:119], v43 offset:12288\n"
"v_cvt_pk_bf16_f32 v214, v204, v205\n"
"v_mfma_f32_16x16x32_bf16 v[152:155], a[88:91], v[208:211], 0\n"
"v_exp_f32 v207, v207\n"
"v_mfma_f32_16x16x32_bf16 v[156:159], a[92:95], v[208:211], 0\n"
"s_and_b32 s42, s4, 1\n"
"v_add_u32 v15, 135168, v14\n"
"v_bfe_u32 v56, v1, 2, 2\n"
"v_mfma_f32_16x16x32_bf16 v[240:243], a[136:139], v[208:211], 0\n"
"s_lshl_b32 s42, s42, 2\n"
"v_cvt_pk_bf16_f32 v215, v206, v207\n"
"v_add_u32 v43, s93, v43\n"
"v_mfma_f32_16x16x32_bf16 v[200:203], a[32:35], a[208:211], v[220:223]\n"
"v_exp_f32 v192, v192\n"
"v_mfma_f32_16x16x32_bf16 v[200:203], a[36:39], a[212:215], v[200:203]\n"
"ds_read_b128 a[8:11], v36 offset:0\n"
"v_and_b32 v57, 3, v1\n"
"v_mfma_f32_16x16x32_bf16 v[200:203], a[40:43], a[216:219], v[200:203]\n"
"v_exp_f32 v193, v193\n"
"v_mfma_f32_16x16x32_bf16 v[200:203], a[44:47], a[220:223], v[200:203]\n"
"ds_read_b64_tr_b16 a[120:121], v44 offset:8192\n"
"v_lshl_add_u32 v54, v3, 2, v56\n"
"v_mfma_f32_16x16x32_bf16 v[204:207], a[48:51], a[208:211], v[220:223]\n"
"v_exp_f32 v194, v194\n"
"v_mfma_f32_16x16x32_bf16 v[204:207], a[52:55], a[212:215], v[204:207]\n"
"ds_read_b64_tr_b16 a[122:123], v44 offset:12288\n"
"v_cvt_pk_bf16_f32 v208, v192, v193\n"
"v_mfma_f32_16x16x32_bf16 v[204:207], a[56:59], a[216:219], v[204:207]\n"
"v_exp_f32 v195, v195\n"
"v_mfma_f32_16x16x32_bf16 v[204:207], a[60:63], a[220:223], v[204:207]\n"
"buffer_load_dwordx4 v29, s[56:59], s87 offen offset:1024 lds\n"
"v_add_u32 v44, s93, v44\n"
"v_mfma_f32_16x16x32_bf16 v[160:163], a[64:67], v[212:215], 0\n"
"v_exp_f32 v196, v196\n"
"v_mfma_f32_16x16x32_bf16 v[164:167], a[68:71], v[212:215], 0\n"
"ds_read_b128 a[12:15], v37 offset:0\n"
"v_cvt_pk_bf16_f32 v209, v194, v195\n"
"v_mfma_f32_16x16x32_bf16 v[168:171], a[72:75], v[212:215], 0\n"
"v_exp_f32 v197, v197\n"
"v_mfma_f32_16x16x32_bf16 v[172:175], a[76:79], v[212:215], 0\n"
"s_mov_b32 s13, 5\n"
"ds_read_b64_tr_b16 a[124:125], v45 offset:8192\n"
"v_and_b32 v59, 7, v54\n"
"v_mfma_f32_16x16x32_bf16 v[176:179], a[80:83], v[212:215], 0\n"
"s_mov_b32 s43, s93\n"
"v_exp_f32 v198, v198\n"
"v_mfma_f32_16x16x32_bf16 v[180:183], a[84:87], v[212:215], 0\n"
"s_lshl_b32 s50, s10, 2\n"
"ds_read_b64_tr_b16 a[126:127], v45 offset:12288\n"
"v_cvt_pk_bf16_f32 v210, v196, v197\n"
"v_mfma_f32_16x16x32_bf16 v[184:187], a[88:91], v[212:215], 0\n"
"s_xor_b32 s16, s12, 0\n"
"v_exp_f32 v199, v199\n"
"v_mfma_f32_16x16x32_bf16 v[188:191], a[92:95], v[212:215], 0\n"
"s_lshr_b32 s17, s4, 1\n"
"v_lshlrev_b32 v58, 3, v57\n"
"v_lshl_add_u32 v58, v54, 8, v58\n"
"v_add_u32 v58, 0, v58\n"
"v_xor_b32 v59, s42, v59\n"
"v_xor_b32 v55, 0, v59\n"
"v_lshl_add_u32 v16, v55, 5, v58\n"
"v_xor_b32 v55, 1, v59\n"
"v_lshl_add_u32 v17, v55, 5, v58\n"
"v_xor_b32 v55, 2, v59\n"
"v_lshl_add_u32 v18, v55, 5, v58\n"
"v_xor_b32 v55, 3, v59\n"
"v_lshl_add_u32 v19, v55, 5, v58\n"
"s_lshl_b32 s17, s17, 2\n"
"s_add_u32 s16, s16, s17\n"
"s_lshl_b32 s16, s16, 10\n"
"v_add_u32 v20, s16, v4\n"
"s_xor_b32 s16, s12, 1\n"
"s_add_u32 s16, s16, s17\n"
"s_lshl_b32 s16, s16, 10\n"
"v_add_u32 v21, s16, v4\n"
"s_xor_b32 s16, s12, 2\n"
"s_add_u32 s16, s16, s17\n"
"s_lshl_b32 s16, s16, 10\n"
"v_add_u32 v22, s16, v4\n"
"s_xor_b32 s16, s12, 3\n"
"s_add_u32 s16, s16, s17\n"
"s_lshl_b32 s16, s16, 10\n"
"v_add_u32 v23, s16, v4\n"
"s_add_u32 s50, s50, s11\n"
"s_lshl_b32 s50, s50, 2\n"
"v_mov_b32 v46, -1\n"
"v_mov_b32 v47, -1\n"
"v_mov_b32 v48, -1\n"
"v_mov_b32 v49, -1\n"
"s_mov_b32 s16, s12\n"
"s_add_u32 s16, s16, s50\n"
"s_lshl_b32 s16, s16, 2\n"
"s_add_u32 s16, s16, s4\n"
"s_mul_i32 s16, s16, 3\n"
"s_add_u32 s16, s16, 0\n"
"s_lshl_b32 s16, s16, 11\n"
"s_add_u32 s16, s16, 37748736\n"
"s_mov_b32 s53, s16\n"
"s_add_u32 s54, s16, 4096\n"
"s_sub_u32 s33, s54, 12582912\n"
"v_mfma_f32_16x16x32_bf16 v[244:247], a[136:139], v[212:215], 0\n"
"s_sub_u32 s32, s53, 12582912\n"
"v_cvt_pk_bf16_f32 v211, v198, v199\n"
"v_add_u32 v45, s93, v45\n"
"s_mov_b32 s93, s94\n"
"s_mov_b32 s94, s95\n"
"s_mov_b32 s95, s80\n"
"s_mov_b32 s80, s43\n"
"LATT_%=:\n"
"v_mfma_f32_16x16x32_bf16 v[192:195], a[32:35], a[224:227], v[224:227]\n"
"v_exp_f32 v200, v200\n"
"v_mfma_f32_16x16x32_bf16 v[192:195], a[36:39], a[228:231], v[192:195]\n"
"ds_read_b128 a[16:19], v34 offset:4096\n"
"v_mfma_f32_16x16x32_bf16 v[192:195], a[40:43], a[232:235], v[192:195]\n"
"v_exp_f32 v201, v201\n"
"v_mfma_f32_16x16x32_bf16 v[192:195], a[44:47], a[236:239], v[192:195]\n"
"ds_read_b64_tr_b16 a[64:65], v38 offset:0\n"
"buffer_load_dword v0, v27, s[44:47], s55 offen\n"
"v_mfma_f32_16x16x32_bf16 v[196:199], a[48:51], a[224:227], v[224:227]\n"
"s_add_u32 s55, s55, 2048\n"
"v_exp_f32 v202, v202\n"
"v_mfma_f32_16x16x32_bf16 v[196:199], a[52:55], a[228:231], v[196:199]\n"
"ds_read_b64_tr_b16 a[66:67], v38 offset:4096\n"
"v_cvt_pk_bf16_f32 v212, v200, v201\n"
"v_mfma_f32_16x16x32_bf16 v[196:199], a[56:59], a[232:235], v[196:199]\n"
"v_exp_f32 v203, v203\n"
"v_mfma_f32_16x16x32_bf16 v[196:199], a[60:63], a[236:239], v[196:199]\n"
"buffer_load_dwordx4 v30, s[56:59], s87 offen offset:2048 lds\n"
"v_mfma_f32_16x16x32_bf16 v[64:67], a[96:99], v[208:211], v[64:67]\n"
"v_exp_f32 v204, v204\n"
"v_mfma_f32_16x16x32_bf16 v[68:71], a[100:103], v[208:211], v[68:71]\n"
"ds_read_b128 a[20:23], v35 offset:4096\n"
"v_cvt_pk_bf16_f32 v213, v202, v203\n"
"v_mfma_f32_16x16x32_bf16 v[72:75], a[104:107], v[208:211], v[72:75]\n"
"s_waitcnt lgkmcnt(4)\n"
"v_exp_f32 v205, v205\n"
"v_mfma_f32_16x16x32_bf16 v[76:79], a[108:111], v[208:211], v[76:79]\n"
"ds_read_b64_tr_b16 a[68:69], v39 offset:0\n"
"v_mfma_f32_16x16x32_bf16 v[80:83], a[112:115], v[208:211], v[80:83]\n"
"v_exp_f32 v206, v206\n"
"v_mfma_f32_16x16x32_bf16 v[84:87], a[116:119], v[208:211], v[84:87]\n"
"ds_read_b64_tr_b16 a[70:71], v39 offset:4096\n"
"v_cvt_pk_bf16_f32 v214, v204, v205\n"
"v_mfma_f32_16x16x32_bf16 v[88:91], a[120:123], v[208:211], v[88:91]\n"
"v_exp_f32 v207, v207\n"
"v_mfma_f32_16x16x32_bf16 v[92:95], a[124:127], v[208:211], v[92:95]\n"
"v_mfma_f32_16x16x32_bf16 v[232:235], a[136:139], v[208:211], v[232:235]\n"
"v_cvt_pk_bf16_f32 v215, v206, v207\n"
"v_mfma_f32_16x16x32_bf16 v[200:203], a[32:35], a[240:243], v[228:231]\n"
"v_exp_f32 v192, v192\n"
"v_mfma_f32_16x16x32_bf16 v[200:203], a[36:39], a[244:247], v[200:203]\n"
"ds_read_b128 a[24:27], v36 offset:4096\n"
"v_mfma_f32_16x16x32_bf16 v[200:203], a[40:43], a[248:251], v[200:203]\n"
"v_exp_f32 v193, v193\n"
"v_mfma_f32_16x16x32_bf16 v[200:203], a[44:47], a[252:255], v[200:203]\n"
"ds_read_b64_tr_b16 a[72:73], v40 offset:0\n"
"v_mfma_f32_16x16x32_bf16 v[204:207], a[48:51], a[240:243], v[228:231]\n"
"v_exp_f32 v194, v194\n"
"v_mfma_f32_16x16x32_bf16 v[204:207], a[52:55], a[244:247], v[204:207]\n"
"ds_read_b64_tr_b16 a[74:75], v40 offset:4096\n"
"v_cvt_pk_bf16_f32 v208, v192, v193\n"
"v_mfma_f32_16x16x32_bf16 v[204:207], a[56:59], a[248:251], v[204:207]\n"
"v_exp_f32 v195, v195\n"
"v_mfma_f32_16x16x32_bf16 v[204:207], a[60:63], a[252:255], v[204:207]\n"
"buffer_load_dwordx4 v31, s[56:59], s87 offen offset:3072 lds\n"
"s_add_u32 s88, s88, s92\n"
"v_mfma_f32_16x16x32_bf16 v[96:99], a[96:99], v[212:215], v[96:99]\n"
"v_exp_f32 v196, v196\n"
"v_mfma_f32_16x16x32_bf16 v[100:103], a[100:103], v[212:215], v[100:103]\n"
"ds_read_b128 a[28:31], v37 offset:4096\n"
"v_cvt_pk_bf16_f32 v209, v194, v195\n"
"v_mfma_f32_16x16x32_bf16 v[104:107], a[104:107], v[212:215], v[104:107]\n"
"v_exp_f32 v197, v197\n"
"v_mfma_f32_16x16x32_bf16 v[108:111], a[108:111], v[212:215], v[108:111]\n"
"ds_read_b64_tr_b16 a[76:77], v41 offset:0\n"
"v_mfma_f32_16x16x32_bf16 v[112:115], a[112:115], v[212:215], v[112:115]\n"
"v_exp_f32 v198, v198\n"
"v_mfma_f32_16x16x32_bf16 v[116:119], a[116:119], v[212:215], v[116:119]\n"
"ds_read_b64_tr_b16 a[78:79], v41 offset:4096\n"
"v_cvt_pk_bf16_f32 v210, v196, v197\n"
"v_mfma_f32_16x16x32_bf16 v[120:123], a[120:123], v[212:215], v[120:123]\n"
"v_exp_f32 v199, v199\n"
"v_mfma_f32_16x16x32_bf16 v[124:127], a[124:127], v[212:215], v[124:127]\n"
"v_mfma_f32_16x16x32_bf16 v[236:239], a[136:139], v[212:215], v[236:239]\n"
"v_cvt_pk_bf16_f32 v211, v198, v199\n"
"v_mfma_f32_16x16x32_bf16 v[192:195], a[0:3], a[192:195], v[216:219]\n"
"v_exp_f32 v200, v200\n"
"v_mfma_f32_16x16x32_bf16 v[192:195], a[4:7], a[196:199], v[192:195]\n"
"ds_read_b128 a[32:35], v34 offset:8192\n"
"v_mfma_f32_16x16x32_bf16 v[192:195], a[8:11], a[200:203], v[192:195]\n"
"v_exp_f32 v201, v201\n"
"v_mfma_f32_16x16x32_bf16 v[192:195], a[12:15], a[204:207], v[192:195]\n"
"s_waitcnt lgkmcnt(3)\n"
"ds_read_b64_tr_b16 a[80:81], v42 offset:0\n"
"v_mfma_f32_16x16x32_bf16 v[196:199], a[16:19], a[192:195], v[216:219]\n"
"v_exp_f32 v202, v202\n"
"v_mfma_f32_16x16x32_bf16 v[196:199], a[20:23], a[196:199], v[196:199]\n"
"ds_read_b64_tr_b16 a[82:83], v42 offset:4096\n"
"v_cvt_pk_bf16_f32 v212, v200, v201\n"
"v_mfma_f32_16x16x32_bf16 v[196:199], a[24:27], a[200:203], v[196:199]\n"
"s_mov_b32 m0, s89\n"
"v_exp_f32 v203, v203\n"
"v_mfma_f32_16x16x32_bf16 v[196:199], a[28:31], a[204:207], v[196:199]\n"
"buffer_load_dwordx4 v32, s[60:63], s87 offen lds\n"
"v_mfma_f32_16x16x32_bf16 v[128:131], a[96:99], v[208:211], v[128:131]\n"
"v_exp_f32 v204, v204\n"
"v_mfma_f32_16x16x32_bf16 v[132:135], a[100:103], v[208:211], v[132:135]\n"
"ds_read_b128 a[36:39], v35 offset:8192\n"
"v_cvt_pk_bf16_f32 v213, v202, v203\n"
"v_mfma_f32_16x16x32_bf16 v[136:139], a[104:107], v[208:211], v[136:139]\n"
"v_exp_f32 v205, v205\n"
"v_mfma_f32_16x16x32_bf16 v[140:143], a[108:111], v[208:211], v[140:143]\n"
"ds_read_b64_tr_b16 a[84:85], v43 offset:0\n"
"v_mfma_f32_16x16x32_bf16 v[144:147], a[112:115], v[208:211], v[144:147]\n"
"v_exp_f32 v206, v206\n"
"v_mfma_f32_16x16x32_bf16 v[148:151], a[116:119], v[208:211], v[148:151]\n"
"ds_read_b64_tr_b16 a[86:87], v43 offset:4096\n"
"v_cvt_pk_bf16_f32 v214, v204, v205\n"
"v_mfma_f32_16x16x32_bf16 v[152:155], a[120:123], v[208:211], v[152:155]\n"
"v_exp_f32 v207, v207\n"
"v_mfma_f32_16x16x32_bf16 v[156:159], a[124:127], v[208:211], v[156:159]\n"
"v_mfma_f32_16x16x32_bf16 v[240:243], a[136:139], v[208:211], v[240:243]\n"
"v_cvt_pk_bf16_f32 v215, v206, v207\n"
"v_mfma_f32_16x16x32_bf16 v[200:203], a[0:3], a[208:211], v[220:223]\n"
"v_exp_f32 v192, v192\n"
"v_mfma_f32_16x16x32_bf16 v[200:203], a[4:7], a[212:215], v[200:203]\n"
"ds_read_b128 a[40:43], v36 offset:8192\n"
"v_mfma_f32_16x16x32_bf16 v[200:203], a[8:11], a[216:219], v[200:203]\n"
"v_exp_f32 v193, v193\n"
"v_mfma_f32_16x16x32_bf16 v[200:203], a[12:15], a[220:223], v[200:203]\n"
"ds_read_b64_tr_b16 a[88:89], v44 offset:0\n"
"v_mfma_f32_16x16x32_bf16 v[204:207], a[16:19], a[208:211], v[220:223]\n"
"v_exp_f32 v194, v194\n"
"v_mfma_f32_16x16x32_bf16 v[204:207], a[20:23], a[212:215], v[204:207]\n"
"ds_read_b64_tr_b16 a[90:91], v44 offset:4096\n"
"v_cvt_pk_bf16_f32 v208, v192, v193\n"
"v_mfma_f32_16x16x32_bf16 v[204:207], a[24:27], a[216:219], v[204:207]\n"
"v_exp_f32 v195, v195\n"
"v_mfma_f32_16x16x32_bf16 v[204:207], a[28:31], a[220:223], v[204:207]\n"
"buffer_load_dwordx4 v33, s[60:63], s87 offen offset:1024 lds\n"
"v_mfma_f32_16x16x32_bf16 v[160:163], a[96:99], v[212:215], v[160:163]\n"
"v_exp_f32 v196, v196\n"
"v_mfma_f32_16x16x32_bf16 v[164:167], a[100:103], v[212:215], v[164:167]\n"
"ds_read_b128 a[44:47], v37 offset:8192\n"
"v_cvt_pk_bf16_f32 v209, v194, v195\n"
"v_mfma_f32_16x16x32_bf16 v[168:171], a[104:107], v[212:215], v[168:171]\n"
"v_exp_f32 v197, v197\n"
"v_mfma_f32_16x16x32_bf16 v[172:175], a[108:111], v[212:215], v[172:175]\n"
"ds_read_b64_tr_b16 a[92:93], v45 offset:0\n"
"v_mfma_f32_16x16x32_bf16 v[176:179], a[112:115], v[212:215], v[176:179]\n"
"v_exp_f32 v198, v198\n"
"v_mfma_f32_16x16x32_bf16 v[180:183], a[116:119], v[212:215], v[180:183]\n"
"ds_read_b64_tr_b16 a[94:95], v45 offset:4096\n"
"v_cvt_pk_bf16_f32 v210, v196, v197\n"
"v_mfma_f32_16x16x32_bf16 v[184:187], a[120:123], v[212:215], v[184:187]\n"
"v_exp_f32 v199, v199\n"
"v_mfma_f32_16x16x32_bf16 v[188:191], a[124:127], v[212:215], v[188:191]\n"
"v_mfma_f32_16x16x32_bf16 v[244:247], a[136:139], v[212:215], v[244:247]\n"
"v_cvt_pk_bf16_f32 v211, v198, v199\n"
"v_mfma_f32_16x16x32_bf16 v[192:195], a[0:3], a[224:227], v[224:227]\n"
"v_exp_f32 v200, v200\n"
"v_mfma_f32_16x16x32_bf16 v[192:195], a[4:7], a[228:231], v[192:195]\n"
"ds_read_b128 a[48:51], v34 offset:12288\n"
"v_mfma_f32_16x16x32_bf16 v[192:195], a[8:11], a[232:235], v[192:195]\n"
"v_exp_f32 v201, v201\n"
"v_mfma_f32_16x16x32_bf16 v[192:195], a[12:15], a[236:239], v[192:195]\n"
"ds_read_b64_tr_b16 a[96:97], v38 offset:8192\n"
"v_mfma_f32_16x16x32_bf16 v[196:199], a[16:19], a[224:227], v[224:227]\n"
"v_exp_f32 v202, v202\n"
"v_mfma_f32_16x16x32_bf16 v[196:199], a[20:23], a[228:231], v[196:199]\n"
"ds_read_b64_tr_b16 a[98:99], v38 offset:12288\n"
"v_cvt_pk_bf16_f32 v212, v200, v201\n"
"v_mfma_f32_16x16x32_bf16 v[196:199], a[24:27], a[232:235], v[196:199]\n"
"v_exp_f32 v203, v203\n"
"v_mfma_f32_16x16x32_bf16 v[196:199], a[28:31], a[236:239], v[196:199]\n"
"buffer_load_dwordx4 v32, s[60:63], s87 offen offset:2048 lds\n"
"v_add_u32 v38, s93, v38\n"
"v_mfma_f32_16x16x32_bf16 v[64:67], a[64:67], v[208:211], v[64:67]\n"
"v_exp_f32 v204, v204\n"
"v_mfma_f32_16x16x32_bf16 v[68:71], a[68:71], v[208:211], v[68:71]\n"
"ds_read_b128 a[52:55], v35 offset:12288\n"
"v_cvt_pk_bf16_f32 v213, v202, v203\n"
"v_mfma_f32_16x16x32_bf16 v[72:75], a[72:75], v[208:211], v[72:75]\n"
"s_waitcnt lgkmcnt(4)\n"
"v_exp_f32 v205, v205\n"
"v_mfma_f32_16x16x32_bf16 v[76:79], a[76:79], v[208:211], v[76:79]\n"
"ds_read_b64_tr_b16 a[100:101], v39 offset:8192\n"
"v_mfma_f32_16x16x32_bf16 v[80:83], a[80:83], v[208:211], v[80:83]\n"
"v_exp_f32 v206, v206\n"
"v_mfma_f32_16x16x32_bf16 v[84:87], a[84:87], v[208:211], v[84:87]\n"
"ds_read_b64_tr_b16 a[102:103], v39 offset:12288\n"
"v_cvt_pk_bf16_f32 v214, v204, v205\n"
"v_mfma_f32_16x16x32_bf16 v[88:91], a[88:91], v[208:211], v[88:91]\n"
"v_exp_f32 v207, v207\n"
"v_mfma_f32_16x16x32_bf16 v[92:95], a[92:95], v[208:211], v[92:95]\n"
"v_add_u32 v34, s90, v34\n"
"v_add_u32 v35, s90, v35\n"
"v_mfma_f32_16x16x32_bf16 v[232:235], a[136:139], v[208:211], v[232:235]\n"
"v_cvt_pk_bf16_f32 v215, v206, v207\n"
"v_add_u32 v39, s93, v39\n"
"v_mfma_f32_16x16x32_bf16 v[200:203], a[0:3], a[240:243], v[228:231]\n"
"v_exp_f32 v192, v192\n"
"v_mfma_f32_16x16x32_bf16 v[200:203], a[4:7], a[244:247], v[200:203]\n"
"ds_read_b128 a[56:59], v36 offset:12288\n"
"v_mfma_f32_16x16x32_bf16 v[200:203], a[8:11], a[248:251], v[200:203]\n"
"v_exp_f32 v193, v193\n"
"v_mfma_f32_16x16x32_bf16 v[200:203], a[12:15], a[252:255], v[200:203]\n"
"ds_read_b64_tr_b16 a[104:105], v40 offset:8192\n"
"v_mfma_f32_16x16x32_bf16 v[204:207], a[16:19], a[240:243], v[228:231]\n"
"v_exp_f32 v194, v194\n"
"v_mfma_f32_16x16x32_bf16 v[204:207], a[20:23], a[244:247], v[204:207]\n"
"ds_read_b64_tr_b16 a[106:107], v40 offset:12288\n"
"v_cvt_pk_bf16_f32 v208, v192, v193\n"
"v_mfma_f32_16x16x32_bf16 v[204:207], a[24:27], a[248:251], v[204:207]\n"
"v_exp_f32 v195, v195\n"
"v_mfma_f32_16x16x32_bf16 v[204:207], a[28:31], a[252:255], v[204:207]\n"
"s_add_u32 s89, s89, s95\n"
"buffer_load_dwordx4 v33, s[60:63], s87 offen offset:3072 lds\n"
"s_add_u32 s87, s87, 16384\n"
"v_add_u32 v40, s93, v40\n"
"v_mfma_f32_16x16x32_bf16 v[96:99], a[64:67], v[212:215], v[96:99]\n"
"s_and_b32 s87, s87, 0x1ffff\n"
"v_exp_f32 v196, v196\n"
"v_mfma_f32_16x16x32_bf16 v[100:103], a[68:71], v[212:215], v[100:103]\n"
"ds_read_b128 a[60:63], v37 offset:12288\n"
"v_cvt_pk_bf16_f32 v209, v194, v195\n"
"v_mfma_f32_16x16x32_bf16 v[104:107], a[72:75], v[212:215], v[104:107]\n"
"v_exp_f32 v197, v197\n"
"v_mfma_f32_16x16x32_bf16 v[108:111], a[76:79], v[212:215], v[108:111]\n"
"ds_read_b64_tr_b16 a[108:109], v41 offset:8192\n"
"v_mfma_f32_16x16x32_bf16 v[112:115], a[80:83], v[212:215], v[112:115]\n"
"v_exp_f32 v198, v198\n"
"v_mfma_f32_16x16x32_bf16 v[116:119], a[84:87], v[212:215], v[116:119]\n"
"ds_read_b64_tr_b16 a[110:111], v41 offset:12288\n"
"v_cvt_pk_bf16_f32 v210, v196, v197\n"
"v_mfma_f32_16x16x32_bf16 v[120:123], a[88:91], v[212:215], v[120:123]\n"
"v_exp_f32 v199, v199\n"
"v_mfma_f32_16x16x32_bf16 v[124:127], a[92:95], v[212:215], v[124:127]\n"
"s_mov_b32 s43, s90\n"
"v_add_u32 v36, s90, v36\n"
"v_add_u32 v37, s90, v37\n"
"s_mov_b32 s90, s91\n"
"s_mov_b32 s91, s92\n"
"v_mfma_f32_16x16x32_bf16 v[236:239], a[136:139], v[212:215], v[236:239]\n"
"s_mov_b32 s92, s43\n"
"v_cvt_pk_bf16_f32 v211, v198, v199\n"
"v_add_u32 v41, s93, v41\n"
"s_waitcnt vmcnt(9) lgkmcnt(2)\n"
"s_barrier\n"
"v_mfma_f32_16x16x32_bf16 v[192:195], a[32:35], a[192:195], v[216:219]\n"
"v_exp_f32 v200, v200\n"
"v_mfma_f32_16x16x32_bf16 v[192:195], a[36:39], a[196:199], v[192:195]\n"
"ds_read_b128 a[0:3], v34 offset:0\n"
"v_mfma_f32_16x16x32_bf16 v[192:195], a[40:43], a[200:203], v[192:195]\n"
"v_exp_f32 v201, v201\n"
"v_mfma_f32_16x16x32_bf16 v[192:195], a[44:47], a[204:207], v[192:195]\n"
"ds_read_b64_tr_b16 a[112:113], v42 offset:8192\n"
"v_mfma_f32_16x16x32_bf16 v[196:199], a[48:51], a[192:195], v[216:219]\n"
"v_exp_f32 v202, v202\n"
"v_mfma_f32_16x16x32_bf16 v[196:199], a[52:55], a[196:199], v[196:199]\n"
"ds_read_b64_tr_b16 a[114:115], v42 offset:12288\n"
"v_cvt_pk_bf16_f32 v212, v200, v201\n"
"v_mfma_f32_16x16x32_bf16 v[196:199], a[56:59], a[200:203], v[196:199]\n"
"s_mov_b32 m0, s88\n"
"v_exp_f32 v203, v203\n"
"v_mfma_f32_16x16x32_bf16 v[196:199], a[60:63], a[204:207], v[196:199]\n"
"buffer_load_dwordx4 v28, s[56:59], s87 offen lds\n"
"v_add_u32 v42, s93, v42\n"
"v_mfma_f32_16x16x32_bf16 v[128:131], a[64:67], v[208:211], v[128:131]\n"
"v_exp_f32 v204, v204\n"
"v_mfma_f32_16x16x32_bf16 v[132:135], a[68:71], v[208:211], v[132:135]\n"
"ds_read_b128 a[4:7], v35 offset:0\n"
"v_cvt_pk_bf16_f32 v213, v202, v203\n"
"v_mfma_f32_16x16x32_bf16 v[136:139], a[72:75], v[208:211], v[136:139]\n"
"v_exp_f32 v205, v205\n"
"v_mfma_f32_16x16x32_bf16 v[140:143], a[76:79], v[208:211], v[140:143]\n"
"ds_read_b64_tr_b16 a[116:117], v43 offset:8192\n"
"v_mfma_f32_16x16x32_bf16 v[144:147], a[80:83], v[208:211], v[144:147]\n"
"v_exp_f32 v206, v206\n"
"v_mfma_f32_16x16x32_bf16 v[148:151], a[84:87], v[208:211], v[148:151]\n"
"ds_read_b64_tr_b16 a[118:119], v43 offset:12288\n"
"v_cvt_pk_bf16_f32 v214, v204, v205\n"
"v_mfma_f32_16x16x32_bf16 v[152:155], a[88:91], v[208:211], v[152:155]\n"
"v_exp_f32 v207, v207\n"
"v_mfma_f32_16x16x32_bf16 v[156:159], a[92:95], v[208:211], v[156:159]\n"
"s_cmp_eq_u32 s13, 4\n"
"s_cbranch_scc1 LHS_%=\n"
"LHSK_%=:\n"
"v_mfma_f32_16x16x32_bf16 v[240:243], a[136:139], v[208:211], v[240:243]\n"
"v_cvt_pk_bf16_f32 v215, v206, v207\n"
"v_add_u32 v43, s93, v43\n"
"v_mfma_f32_16x16x32_bf16 v[200:203], a[32:35], a[208:211], v[220:223]\n"
"v_exp_f32 v192, v192\n"
"v_mfma_f32_16x16x32_bf16 v[200:203], a[36:39], a[212:215], v[200:203]\n"
"ds_read_b128 a[8:11], v36 offset:0\n"
"v_mfma_f32_16x16x32_bf16 v[200:203], a[40:43], a[216:219], v[200:203]\n"
"v_exp_f32 v193, v193\n"
"v_mfma_f32_16x16x32_bf16 v[200:203], a[44:47], a[220:223], v[200:203]\n"
"ds_read_b64_tr_b16 a[120:121], v44 offset:8192\n"
"v_mfma_f32_16x16x32_bf16 v[204:207], a[48:51], a[208:211], v[220:223]\n"
"v_exp_f32 v194, v194\n"
"v_mfma_f32_16x16x32_bf16 v[204:207], a[52:55], a[212:215], v[204:207]\n"
"ds_read_b64_tr_b16 a[122:123], v44 offset:12288\n"
"v_cvt_pk_bf16_f32 v208, v192, v193\n"
"v_mfma_f32_16x16x32_bf16 v[204:207], a[56:59], a[216:219], v[204:207]\n"
"v_exp_f32 v195, v195\n"
"v_mfma_f32_16x16x32_bf16 v[204:207], a[60:63], a[220:223], v[204:207]\n"
"buffer_load_dwordx4 v29, s[56:59], s87 offen offset:1024 lds\n"
"v_add_u32 v44, s93, v44\n"
"v_mfma_f32_16x16x32_bf16 v[160:163], a[64:67], v[212:215], v[160:163]\n"
"v_exp_f32 v196, v196\n"
"v_mfma_f32_16x16x32_bf16 v[164:167], a[68:71], v[212:215], v[164:167]\n"
"ds_read_b128 a[12:15], v37 offset:0\n"
"v_cvt_pk_bf16_f32 v209, v194, v195\n"
"v_mfma_f32_16x16x32_bf16 v[168:171], a[72:75], v[212:215], v[168:171]\n"
"v_exp_f32 v197, v197\n"
"v_mfma_f32_16x16x32_bf16 v[172:175], a[76:79], v[212:215], v[172:175]\n"
"ds_read_b64_tr_b16 a[124:125], v45 offset:8192\n"
"v_mfma_f32_16x16x32_bf16 v[176:179], a[80:83], v[212:215], v[176:179]\n"
"v_exp_f32 v198, v198\n"
"v_mfma_f32_16x16x32_bf16 v[180:183], a[84:87], v[212:215], v[180:183]\n"
"ds_read_b64_tr_b16 a[126:127], v45 offset:12288\n"
"v_cvt_pk_bf16_f32 v210, v196, v197\n"
"v_mfma_f32_16x16x32_bf16 v[184:187], a[88:91], v[212:215], v[184:187]\n"
"v_exp_f32 v199, v199\n"
"v_mfma_f32_16x16x32_bf16 v[188:191], a[92:95], v[212:215], v[188:191]\n"
"s_sub_u32 s13, s13, 1\n"
"s_cmp_lg_u32 s13, 0\n"
"v_mfma_f32_16x16x32_bf16 v[244:247], a[136:139], v[212:215], v[244:247]\n"
"s_mov_b32 s43, s93\n"
"v_cvt_pk_bf16_f32 v211, v198, v199\n"
"v_add_u32 v45, s93, v45\n"
"s_mov_b32 s93, s94\n"
"s_mov_b32 s94, s95\n"
"s_mov_b32 s95, s80\n"
"s_mov_b32 s80, s43\n"
"s_cbranch_scc1 LATT_%=\n"
"buffer_load_dword v9, v8, s[64:67], 0 offen sc1\n"
"v_mfma_f32_16x16x32_bf16 v[192:195], a[32:35], a[224:227], v[224:227]\n"
"v_exp_f32 v200, v200\n"
"v_mfma_f32_16x16x32_bf16 v[192:195], a[36:39], a[228:231], v[192:195]\n"
"ds_read_b128 a[16:19], v34 offset:4096\n"
"v_mfma_f32_16x16x32_bf16 v[192:195], a[40:43], a[232:235], v[192:195]\n"
"v_exp_f32 v201, v201\n"
"v_mfma_f32_16x16x32_bf16 v[192:195], a[44:47], a[236:239], v[192:195]\n"
"ds_read_b64_tr_b16 a[64:65], v38 offset:0\n"
"v_mfma_f32_16x16x32_bf16 v[196:199], a[48:51], a[224:227], v[224:227]\n"
"v_exp_f32 v202, v202\n"
"v_mfma_f32_16x16x32_bf16 v[196:199], a[52:55], a[228:231], v[196:199]\n"
"ds_read_b64_tr_b16 a[66:67], v38 offset:4096\n"
"v_cvt_pk_bf16_f32 v212, v200, v201\n"
"v_mfma_f32_16x16x32_bf16 v[196:199], a[56:59], a[232:235], v[196:199]\n"
"s_add_u32 m0, s84, 81920\n"
"v_exp_f32 v203, v203\n"
"v_mfma_f32_16x16x32_bf16 v[196:199], a[60:63], a[236:239], v[196:199]\n"
"buffer_load_dwordx4 v10, s[72:75], s85 offen lds\n"
"v_mfma_f32_16x16x32_bf16 v[64:67], a[96:99], v[208:211], v[64:67]\n"
"v_exp_f32 v204, v204\n"
"v_mfma_f32_16x16x32_bf16 v[68:71], a[100:103], v[208:211], v[68:71]\n"
"ds_read_b128 a[20:23], v35 offset:4096\n"
"v_cvt_pk_bf16_f32 v213, v202, v203\n"
"v_mfma_f32_16x16x32_bf16 v[72:75], a[104:107], v[208:211], v[72:75]\n"
"s_waitcnt lgkmcnt(4)\n"
"v_exp_f32 v205, v205\n"
"v_mfma_f32_16x16x32_bf16 v[76:79], a[108:111], v[208:211], v[76:79]\n"
"ds_read_b64_tr_b16 a[68:69], v39 offset:0\n"
"v_mfma_f32_16x16x32_bf16 v[80:83], a[112:115], v[208:211], v[80:83]\n"
"v_exp_f32 v206, v206\n"
"v_mfma_f32_16x16x32_bf16 v[84:87], a[116:119], v[208:211], v[84:87]\n"
"ds_read_b64_tr_b16 a[70:71], v39 offset:4096\n"
"v_cvt_pk_bf16_f32 v214, v204, v205\n"
"v_mfma_f32_16x16x32_bf16 v[88:91], a[120:123], v[208:211], v[88:91]\n"
"v_exp_f32 v207, v207\n"
"v_mfma_f32_16x16x32_bf16 v[92:95], a[124:127], v[208:211], v[92:95]\n"
"buffer_load_dwordx4 v11, s[72:75], s85 offen offset:1024 lds\n"
"s_add_u32 s85, s85, 32768\n"
"v_mfma_f32_16x16x32_bf16 v[232:235], a[136:139], v[208:211], v[232:235]\n"
"v_cvt_pk_bf16_f32 v215, v206, v207\n"
"v_mfma_f32_16x16x32_bf16 v[200:203], a[32:35], a[240:243], v[228:231]\n"
"v_exp_f32 v192, v192\n"
"v_mfma_f32_16x16x32_bf16 v[200:203], a[36:39], a[244:247], v[200:203]\n"
"ds_read_b128 a[24:27], v36 offset:4096\n"
"v_mfma_f32_16x16x32_bf16 v[200:203], a[40:43], a[248:251], v[200:203]\n"
"v_exp_f32 v193, v193\n"
"v_mfma_f32_16x16x32_bf16 v[200:203], a[44:47], a[252:255], v[200:203]\n"
"ds_read_b64_tr_b16 a[72:73], v40 offset:0\n"
"buffer_store_dwordx4 v[46:49], v4, s[64:67], s53 offen offset:0 sc1\n"
"v_mfma_f32_16x16x32_bf16 v[204:207], a[48:51], a[240:243], v[228:231]\n"
"v_exp_f32 v194, v194\n"
"v_mfma_f32_16x16x32_bf16 v[204:207], a[52:55], a[244:247], v[204:207]\n"
"ds_read_b64_tr_b16 a[74:75], v40 offset:4096\n"
"v_cvt_pk_bf16_f32 v208, v192, v193\n"
"v_mfma_f32_16x16x32_bf16 v[204:207], a[56:59], a[248:251], v[204:207]\n"
"s_add_u32 m0, s84, 98304\n"
"v_exp_f32 v195, v195\n"
"v_mfma_f32_16x16x32_bf16 v[204:207], a[60:63], a[252:255], v[204:207]\n"
"buffer_load_dwordx4 v10, s[72:75], s85 offen lds\n"
"v_mfma_f32_16x16x32_bf16 v[96:99], a[96:99], v[212:215], v[96:99]\n"
"v_exp_f32 v196, v196\n"
"v_mfma_f32_16x16x32_bf16 v[100:103], a[100:103], v[212:215], v[100:103]\n"
"ds_read_b128 a[28:31], v37 offset:4096\n"
"v_cvt_pk_bf16_f32 v209, v194, v195\n"
"v_mfma_f32_16x16x32_bf16 v[104:107], a[104:107], v[212:215], v[104:107]\n"
"v_exp_f32 v197, v197\n"
"v_mfma_f32_16x16x32_bf16 v[108:111], a[108:111], v[212:215], v[108:111]\n"
"ds_read_b64_tr_b16 a[76:77], v41 offset:0\n"
"v_mfma_f32_16x16x32_bf16 v[112:115], a[112:115], v[212:215], v[112:115]\n"
"v_exp_f32 v198, v198\n"
"v_mfma_f32_16x16x32_bf16 v[116:119], a[116:119], v[212:215], v[116:119]\n"
"ds_read_b64_tr_b16 a[78:79], v41 offset:4096\n"
"v_cvt_pk_bf16_f32 v210, v196, v197\n"
"v_mfma_f32_16x16x32_bf16 v[120:123], a[120:123], v[212:215], v[120:123]\n"
"v_exp_f32 v199, v199\n"
"v_mfma_f32_16x16x32_bf16 v[124:127], a[124:127], v[212:215], v[124:127]\n"
"buffer_load_dwordx4 v11, s[72:75], s85 offen offset:1024 lds\n"
"s_add_u32 s85, s85, 32768\n"
"v_mfma_f32_16x16x32_bf16 v[236:239], a[136:139], v[212:215], v[236:239]\n"
"v_cvt_pk_bf16_f32 v211, v198, v199\n"
"v_mfma_f32_16x16x32_bf16 v[192:195], a[0:3], a[192:195], v[216:219]\n"
"v_exp_f32 v200, v200\n"
"v_mfma_f32_16x16x32_bf16 v[192:195], a[4:7], a[196:199], v[192:195]\n"
"ds_read_b128 a[32:35], v34 offset:8192\n"
"v_mfma_f32_16x16x32_bf16 v[192:195], a[8:11], a[200:203], v[192:195]\n"
"v_exp_f32 v201, v201\n"
"v_mfma_f32_16x16x32_bf16 v[192:195], a[12:15], a[204:207], v[192:195]\n"
"s_waitcnt lgkmcnt(3)\n"
"ds_read_b64_tr_b16 a[80:81], v42 offset:0\n"
"buffer_store_dwordx4 v[46:49], v4, s[64:67], s53 offen offset:1024 sc1\n"
"v_mfma_f32_16x16x32_bf16 v[196:199], a[16:19], a[192:195], v[216:219]\n"
"v_exp_f32 v202, v202\n"
"v_mfma_f32_16x16x32_bf16 v[196:199], a[20:23], a[196:199], v[196:199]\n"
"ds_read_b64_tr_b16 a[82:83], v42 offset:4096\n"
"v_cvt_pk_bf16_f32 v212, v200, v201\n"
"v_mfma_f32_16x16x32_bf16 v[196:199], a[24:27], a[200:203], v[196:199]\n"
"s_add_u32 m0, s84, 114688\n"
"v_exp_f32 v203, v203\n"
"v_mfma_f32_16x16x32_bf16 v[196:199], a[28:31], a[204:207], v[196:199]\n"
"buffer_load_dwordx4 v10, s[72:75], s85 offen lds\n"
"v_mfma_f32_16x16x32_bf16 v[128:131], a[96:99], v[208:211], v[128:131]\n"
"v_exp_f32 v204, v204\n"
"v_mfma_f32_16x16x32_bf16 v[132:135], a[100:103], v[208:211], v[132:135]\n"
"ds_read_b128 a[36:39], v35 offset:8192\n"
"v_cvt_pk_bf16_f32 v213, v202, v203\n"
"v_mfma_f32_16x16x32_bf16 v[136:139], a[104:107], v[208:211], v[136:139]\n"
"v_exp_f32 v205, v205\n"
"v_mfma_f32_16x16x32_bf16 v[140:143], a[108:111], v[208:211], v[140:143]\n"
"ds_read_b64_tr_b16 a[84:85], v43 offset:0\n"
"v_mfma_f32_16x16x32_bf16 v[144:147], a[112:115], v[208:211], v[144:147]\n"
"v_exp_f32 v206, v206\n"
"v_mfma_f32_16x16x32_bf16 v[148:151], a[116:119], v[208:211], v[148:151]\n"
"ds_read_b64_tr_b16 a[86:87], v43 offset:4096\n"
"v_cvt_pk_bf16_f32 v214, v204, v205\n"
"v_mfma_f32_16x16x32_bf16 v[152:155], a[120:123], v[208:211], v[152:155]\n"
"v_exp_f32 v207, v207\n"
"v_mfma_f32_16x16x32_bf16 v[156:159], a[124:127], v[208:211], v[156:159]\n"
"buffer_load_dwordx4 v11, s[72:75], s85 offen offset:1024 lds\n"
"s_add_u32 s85, s85, 32768\n"
"v_mfma_f32_16x16x32_bf16 v[240:243], a[136:139], v[208:211], v[240:243]\n"
"s_waitcnt vmcnt(8)\n"
"v_cvt_pk_bf16_f32 v215, v206, v207\n"
"v_cmp_ne_u32 s[18:19], s8, v9\n"
"s_nop 0\n"
"s_cmp_lg_u64 s[18:19], 0\n"
"s_cbranch_scc1 LHFR_%=\n"
"LHFK_%=:\n"
"v_mfma_f32_16x16x32_bf16 v[200:203], a[0:3], a[208:211], v[220:223]\n"
"v_exp_f32 v192, v192\n"
"v_mfma_f32_16x16x32_bf16 v[200:203], a[4:7], a[212:215], v[200:203]\n"
"ds_read_b128 a[40:43], v36 offset:8192\n"
"v_mfma_f32_16x16x32_bf16 v[200:203], a[8:11], a[216:219], v[200:203]\n"
"v_exp_f32 v193, v193\n"
"v_mfma_f32_16x16x32_bf16 v[200:203], a[12:15], a[220:223], v[200:203]\n"
"ds_read_b64_tr_b16 a[88:89], v44 offset:0\n"
"v_mfma_f32_16x16x32_bf16 v[204:207], a[16:19], a[208:211], v[220:223]\n"
"v_exp_f32 v194, v194\n"
"v_mfma_f32_16x16x32_bf16 v[204:207], a[20:23], a[212:215], v[204:207]\n"
"ds_read_b64_tr_b16 a[90:91], v44 offset:4096\n"
"v_cvt_pk_bf16_f32 v208, v192, v193\n"
"v_mfma_f32_16x16x32_bf16 v[204:207], a[24:27], a[216:219], v[204:207]\n"
"s_add_u32 m0, s84, 90112\n"
"v_exp_f32 v195, v195\n"
"v_mfma_f32_16x16x32_bf16 v[204:207], a[28:31], a[220:223], v[204:207]\n"
"buffer_load_dwordx4 v12, s[64:67], s48 offen sc1 lds\n"
"v_mfma_f32_16x16x32_bf16 v[160:163], a[96:99], v[212:215], v[160:163]\n"
"v_exp_f32 v196, v196\n"
"v_mfma_f32_16x16x32_bf16 v[164:167], a[100:103], v[212:215], v[164:167]\n"
"ds_read_b128 a[44:47], v37 offset:8192\n"
"v_cvt_pk_bf16_f32 v209, v194, v195\n"
"v_mfma_f32_16x16x32_bf16 v[168:171], a[104:107], v[212:215], v[168:171]\n"
"v_exp_f32 v197, v197\n"
"v_mfma_f32_16x16x32_bf16 v[172:175], a[108:111], v[212:215], v[172:175]\n"
"ds_read_b64_tr_b16 a[92:93], v45 offset:0\n"
"v_mfma_f32_16x16x32_bf16 v[176:179], a[112:115], v[212:215], v[176:179]\n"
"v_exp_f32 v198, v198\n"
"v_mfma_f32_16x16x32_bf16 v[180:183], a[116:119], v[212:215], v[180:183]\n"
"ds_read_b64_tr_b16 a[94:95], v45 offset:4096\n"
"v_cvt_pk_bf16_f32 v210, v196, v197\n"
"v_mfma_f32_16x16x32_bf16 v[184:187], a[120:123], v[212:215], v[184:187]\n"
"v_exp_f32 v199, v199\n"
"v_mfma_f32_16x16x32_bf16 v[188:191], a[124:127], v[212:215], v[188:191]\n"
"buffer_load_dwordx4 v13, s[64:67], s48 offen offset:1024 sc1 lds\n"
"s_add_u32 s48, s48, 1024\n"
"v_mfma_f32_16x16x32_bf16 v[244:247], a[136:139], v[212:215], v[244:247]\n"
"v_cvt_pk_bf16_f32 v211, v198, v199\n"
"v_mfma_f32_16x16x32_bf16 v[192:195], a[0:3], a[224:227], v[224:227]\n"
"v_exp_f32 v200, v200\n"
"v_mfma_f32_16x16x32_bf16 v[192:195], a[4:7], a[228:231], v[192:195]\n"
"ds_read_b128 a[48:51], v34 offset:12288\n"
"v_mfma_f32_16x16x32_bf16 v[192:195], a[8:11], a[232:235], v[192:195]\n"
"v_exp_f32 v201, v201\n"
"v_mfma_f32_16x16x32_bf16 v[192:195], a[12:15], a[236:239], v[192:195]\n"
"ds_read_b64_tr_b16 a[96:97], v38 offset:8192\n"
"buffer_store_dwordx4 v[46:49], v4, s[64:67], s53 offen offset:2048 sc1\n"
"v_mfma_f32_16x16x32_bf16 v[196:199], a[16:19], a[224:227], v[224:227]\n"
"v_exp_f32 v202, v202\n"
"v_mfma_f32_16x16x32_bf16 v[196:199], a[20:23], a[228:231], v[196:199]\n"
"ds_read_b64_tr_b16 a[98:99], v38 offset:12288\n"
"v_cvt_pk_bf16_f32 v212, v200, v201\n"
"v_mfma_f32_16x16x32_bf16 v[196:199], a[24:27], a[232:235], v[196:199]\n"
"s_add_u32 m0, s84, 106496\n"
"v_exp_f32 v203, v203\n"
"v_mfma_f32_16x16x32_bf16 v[196:199], a[28:31], a[236:239], v[196:199]\n"
"buffer_load_dwordx4 v12, s[64:67], s48 offen sc1 lds\n"
"v_add_u32 v38, s93, v38\n"
"v_mfma_f32_16x16x32_bf16 v[64:67], a[64:67], v[208:211], v[64:67]\n"
"v_exp_f32 v204, v204\n"
"v_mfma_f32_16x16x32_bf16 v[68:71], a[68:71], v[208:211], v[68:71]\n"
"ds_read_b128 a[52:55], v35 offset:12288\n"
"v_cvt_pk_bf16_f32 v213, v202, v203\n"
"v_mfma_f32_16x16x32_bf16 v[72:75], a[72:75], v[208:211], v[72:75]\n"
"s_waitcnt lgkmcnt(4)\n"
"v_exp_f32 v205, v205\n"
"v_mfma_f32_16x16x32_bf16 v[76:79], a[76:79], v[208:211], v[76:79]\n"
"ds_read_b64_tr_b16 a[100:101], v39 offset:8192\n"
"v_mfma_f32_16x16x32_bf16 v[80:83], a[80:83], v[208:211], v[80:83]\n"
"v_exp_f32 v206, v206\n"
"v_mfma_f32_16x16x32_bf16 v[84:87], a[84:87], v[208:211], v[84:87]\n"
"ds_read_b64_tr_b16 a[102:103], v39 offset:12288\n"
"v_cvt_pk_bf16_f32 v214, v204, v205\n"
"v_mfma_f32_16x16x32_bf16 v[88:91], a[88:91], v[208:211], v[88:91]\n"
"v_exp_f32 v207, v207\n"
"v_mfma_f32_16x16x32_bf16 v[92:95], a[92:95], v[208:211], v[92:95]\n"
"buffer_load_dwordx4 v13, s[64:67], s48 offen offset:1024 sc1 lds\n"
"v_add_u32 v34, s90, v34\n"
"v_add_u32 v35, s90, v35\n"
"v_mfma_f32_16x16x32_bf16 v[232:235], a[136:139], v[208:211], v[232:235]\n"
"s_add_u32 s48, s48, 1024\n"
"v_cvt_pk_bf16_f32 v215, v206, v207\n"
"v_add_u32 v39, s93, v39\n"
"v_mfma_f32_16x16x32_bf16 v[200:203], a[0:3], a[240:243], v[228:231]\n"
"v_exp_f32 v192, v192\n"
"v_mfma_f32_16x16x32_bf16 v[200:203], a[4:7], a[244:247], v[200:203]\n"
"ds_read_b128 a[56:59], v36 offset:12288\n"
"v_mfma_f32_16x16x32_bf16 v[200:203], a[8:11], a[248:251], v[200:203]\n"
"v_exp_f32 v193, v193\n"
"v_mfma_f32_16x16x32_bf16 v[200:203], a[12:15], a[252:255], v[200:203]\n"
"ds_read_b64_tr_b16 a[104:105], v40 offset:8192\n"
"buffer_store_dwordx4 v[46:49], v4, s[64:67], s53 offen offset:3072 sc1\n"
"v_mfma_f32_16x16x32_bf16 v[204:207], a[16:19], a[240:243], v[228:231]\n"
"v_exp_f32 v194, v194\n"
"v_mfma_f32_16x16x32_bf16 v[204:207], a[20:23], a[244:247], v[204:207]\n"
"ds_read_b64_tr_b16 a[106:107], v40 offset:12288\n"
"v_cvt_pk_bf16_f32 v208, v192, v193\n"
"v_mfma_f32_16x16x32_bf16 v[204:207], a[24:27], a[248:251], v[204:207]\n"
"s_add_u32 m0, s84, 122880\n"
"v_exp_f32 v195, v195\n"
"v_mfma_f32_16x16x32_bf16 v[204:207], a[28:31], a[252:255], v[204:207]\n"
"buffer_load_dwordx4 v12, s[64:67], s48 offen sc1 lds\n"
"v_add_u32 v40, s93, v40\n"
"v_mfma_f32_16x16x32_bf16 v[96:99], a[64:67], v[212:215], v[96:99]\n"
"v_exp_f32 v196, v196\n"
"v_mfma_f32_16x16x32_bf16 v[100:103], a[68:71], v[212:215], v[100:103]\n"
"ds_read_b128 a[60:63], v37 offset:12288\n"
"v_cvt_pk_bf16_f32 v209, v194, v195\n"
"v_mfma_f32_16x16x32_bf16 v[104:107], a[72:75], v[212:215], v[104:107]\n"
"v_exp_f32 v197, v197\n"
"v_mfma_f32_16x16x32_bf16 v[108:111], a[76:79], v[212:215], v[108:111]\n"
"ds_read_b64_tr_b16 a[108:109], v41 offset:8192\n"
"v_mfma_f32_16x16x32_bf16 v[112:115], a[80:83], v[212:215], v[112:115]\n"
"v_exp_f32 v198, v198\n"
"v_mfma_f32_16x16x32_bf16 v[116:119], a[84:87], v[212:215], v[116:119]\n"
"ds_read_b64_tr_b16 a[110:111], v41 offset:12288\n"
"v_cvt_pk_bf16_f32 v210, v196, v197\n"
"v_mfma_f32_16x16x32_bf16 v[120:123], a[88:91], v[212:215], v[120:123]\n"
"v_exp_f32 v199, v199\n"
"v_mfma_f32_16x16x32_bf16 v[124:127], a[92:95], v[212:215], v[124:127]\n"
"s_mov_b32 s43, s90\n"
"buffer_load_dwordx4 v13, s[64:67], s48 offen offset:1024 sc1 lds\n"
"v_add_u32 v36, s90, v36\n"
"v_add_u32 v37, s90, v37\n"
"s_mov_b32 s90, s91\n"
"s_mov_b32 s91, s92\n"
"s_mov_b32 s92, s43\n"
"v_mfma_f32_16x16x32_bf16 v[236:239], a[136:139], v[212:215], v[236:239]\n"
"s_add_u32 s48, s48, 1024\n"
"v_cvt_pk_bf16_f32 v211, v198, v199\n"
"v_add_u32 v41, s93, v41\n"
"s_waitcnt lgkmcnt(2)\n"
"s_barrier\n"
"v_mfma_f32_16x16x32_bf16 v[192:195], a[32:35], a[192:195], v[216:219]\n"
"v_exp_f32 v200, v200\n"
"v_mfma_f32_16x16x32_bf16 v[192:195], a[36:39], a[196:199], v[192:195]\n"
"ds_read_b128 a[0:3], v34 offset:0\n"
"v_mfma_f32_16x16x32_bf16 v[192:195], a[40:43], a[200:203], v[192:195]\n"
"v_exp_f32 v201, v201\n"
"v_mfma_f32_16x16x32_bf16 v[192:195], a[44:47], a[204:207], v[192:195]\n"
"ds_read_b64_tr_b16 a[112:113], v42 offset:8192\n"
"v_mfma_f32_16x16x32_bf16 v[196:199], a[48:51], a[192:195], v[216:219]\n"
"v_exp_f32 v202, v202\n"
"v_mfma_f32_16x16x32_bf16 v[196:199], a[52:55], a[196:199], v[196:199]\n"
"ds_read_b64_tr_b16 a[114:115], v42 offset:12288\n"
"v_cvt_pk_bf16_f32 v212, v200, v201\n"
"v_mfma_f32_16x16x32_bf16 v[196:199], a[56:59], a[200:203], v[196:199]\n"
"s_add_u32 m0, s84, 147456\n"
"v_exp_f32 v203, v203\n"
"v_mfma_f32_16x16x32_bf16 v[196:199], a[60:63], a[204:207], v[196:199]\n"
"buffer_load_dwordx4 v10, s[72:75], s85 offen lds\n"
"v_add_u32 v42, s93, v42\n"
"v_mfma_f32_16x16x32_bf16 v[128:131], a[64:67], v[208:211], v[128:131]\n"
"v_exp_f32 v204, v204\n"
"v_mfma_f32_16x16x32_bf16 v[132:135], a[68:71], v[208:211], v[132:135]\n"
"ds_read_b128 a[4:7], v35 offset:0\n"
"v_cvt_pk_bf16_f32 v213, v202, v203\n"
"v_mfma_f32_16x16x32_bf16 v[136:139], a[72:75], v[208:211], v[136:139]\n"
"v_exp_f32 v205, v205\n"
"v_mfma_f32_16x16x32_bf16 v[140:143], a[76:79], v[208:211], v[140:143]\n"
"ds_read_b64_tr_b16 a[116:117], v43 offset:8192\n"
"v_mfma_f32_16x16x32_bf16 v[144:147], a[80:83], v[208:211], v[144:147]\n"
"v_exp_f32 v206, v206\n"
"v_mfma_f32_16x16x32_bf16 v[148:151], a[84:87], v[208:211], v[148:151]\n"
"ds_read_b64_tr_b16 a[118:119], v43 offset:12288\n"
"v_cvt_pk_bf16_f32 v214, v204, v205\n"
"v_mfma_f32_16x16x32_bf16 v[152:155], a[88:91], v[208:211], v[152:155]\n"
"v_exp_f32 v207, v207\n"
"v_mfma_f32_16x16x32_bf16 v[156:159], a[92:95], v[208:211], v[156:159]\n"
"buffer_load_dwordx4 v11, s[72:75], s85 offen offset:1024 lds\n"
"s_add_u32 s85, s85, 32768\n"
"v_mfma_f32_16x16x32_bf16 v[240:243], a[136:139], v[208:211], v[240:243]\n"
"v_cvt_pk_bf16_f32 v215, v206, v207\n"
"v_add_u32 v43, s93, v43\n"
"v_mfma_f32_16x16x32_bf16 v[200:203], a[32:35], a[208:211], v[220:223]\n"
"v_exp_f32 v192, v192\n"
"v_mfma_f32_16x16x32_bf16 v[200:203], a[36:39], a[212:215], v[200:203]\n"
"ds_read_b128 a[8:11], v36 offset:0\n"
"v_mfma_f32_16x16x32_bf16 v[200:203], a[40:43], a[216:219], v[200:203]\n"
"v_exp_f32 v193, v193\n"
"v_mfma_f32_16x16x32_bf16 v[200:203], a[44:47], a[220:223], v[200:203]\n"
"ds_read_b64_tr_b16 a[120:121], v44 offset:8192\n"
"buffer_store_dwordx4 v[46:49], v4, s[64:67], s54 offen offset:0 sc1\n"
"v_mfma_f32_16x16x32_bf16 v[204:207], a[48:51], a[208:211], v[220:223]\n"
"v_exp_f32 v194, v194\n"
"v_mfma_f32_16x16x32_bf16 v[204:207], a[52:55], a[212:215], v[204:207]\n"
"ds_read_b64_tr_b16 a[122:123], v44 offset:12288\n"
"v_cvt_pk_bf16_f32 v208, v192, v193\n"
"v_mfma_f32_16x16x32_bf16 v[204:207], a[56:59], a[216:219], v[204:207]\n"
"s_add_u32 m0, s84, 155648\n"
"v_exp_f32 v195, v195\n"
"v_mfma_f32_16x16x32_bf16 v[204:207], a[60:63], a[220:223], v[204:207]\n"
"buffer_load_dwordx4 v12, s[64:67], s48 offen sc1 lds\n"
"v_add_u32 v44, s93, v44\n"
"v_mfma_f32_16x16x32_bf16 v[160:163], a[64:67], v[212:215], v[160:163]\n"
"v_exp_f32 v196, v196\n"
"v_mfma_f32_16x16x32_bf16 v[164:167], a[68:71], v[212:215], v[164:167]\n"
"ds_read_b128 a[12:15], v37 offset:0\n"
"v_cvt_pk_bf16_f32 v209, v194, v195\n"
"v_mfma_f32_16x16x32_bf16 v[168:171], a[72:75], v[212:215], v[168:171]\n"
"v_exp_f32 v197, v197\n"
"v_mfma_f32_16x16x32_bf16 v[172:175], a[76:79], v[212:215], v[172:175]\n"
"ds_read_b64_tr_b16 a[124:125], v45 offset:8192\n"
"v_mfma_f32_16x16x32_bf16 v[176:179], a[80:83], v[212:215], v[176:179]\n"
"v_exp_f32 v198, v198\n"
"v_mfma_f32_16x16x32_bf16 v[180:183], a[84:87], v[212:215], v[180:183]\n"
"ds_read_b64_tr_b16 a[126:127], v45 offset:12288\n"
"v_cvt_pk_bf16_f32 v210, v196, v197\n"
"v_mfma_f32_16x16x32_bf16 v[184:187], a[88:91], v[212:215], v[184:187]\n"
"v_exp_f32 v199, v199\n"
"v_mfma_f32_16x16x32_bf16 v[188:191], a[92:95], v[212:215], v[188:191]\n"
"buffer_load_dwordx4 v13, s[64:67], s48 offen offset:1024 sc1 lds\n"
"s_add_u32 s48, s48, 1024\n"
"v_mfma_f32_16x16x32_bf16 v[244:247], a[136:139], v[212:215], v[244:247]\n"
"s_mov_b32 s43, s93\n"
"v_cvt_pk_bf16_f32 v211, v198, v199\n"
"v_add_u32 v45, s93, v45\n"
"s_mov_b32 s93, s94\n"
"s_mov_b32 s94, s95\n"
"s_mov_b32 s95, s80\n"
"v_mfma_f32_16x16x32_bf16 v[192:195], a[32:35], a[224:227], v[224:227]\n"
"s_mov_b32 s80, s43\n"
"v_exp_f32 v200, v200\n"
"v_mfma_f32_16x16x32_bf16 v[192:195], a[36:39], a[228:231], v[192:195]\n"
"ds_read_b128 a[16:19], v34 offset:4096\n"
"v_mfma_f32_16x16x32_bf16 v[192:195], a[40:43], a[232:235], v[192:195]\n"
"v_exp_f32 v201, v201\n"
"v_mfma_f32_16x16x32_bf16 v[192:195], a[44:47], a[236:239], v[192:195]\n"
"ds_read_b64_tr_b16 a[64:65], v38 offset:0\n"
"buffer_store_dwordx4 v[46:49], v4, s[64:67], s54 offen offset:1024 sc1\n"
"v_mfma_f32_16x16x32_bf16 v[196:199], a[48:51], a[224:227], v[224:227]\n"
"v_exp_f32 v202, v202\n"
"v_mfma_f32_16x16x32_bf16 v[196:199], a[52:55], a[228:231], v[196:199]\n"
"ds_read_b64_tr_b16 a[66:67], v38 offset:4096\n"
"v_cvt_pk_bf16_f32 v212, v200, v201\n"
"v_mfma_f32_16x16x32_bf16 v[196:199], a[56:59], a[232:235], v[196:199]\n"
"s_add_u32 m0, s84, 65536\n"
"v_exp_f32 v203, v203\n"
"v_mfma_f32_16x16x32_bf16 v[196:199], a[60:63], a[236:239], v[196:199]\n"
"buffer_load_dwordx4 v10, s[72:75], s85 offen lds\n"
"v_mfma_f32_16x16x32_bf16 v[64:67], a[96:99], v[208:211], v[64:67]\n"
"v_exp_f32 v204, v204\n"
"v_mfma_f32_16x16x32_bf16 v[68:71], a[100:103], v[208:211], v[68:71]\n"
"ds_read_b128 a[20:23], v35 offset:4096\n"
"v_cvt_pk_bf16_f32 v213, v202, v203\n"
"v_mfma_f32_16x16x32_bf16 v[72:75], a[104:107], v[208:211], v[72:75]\n"
"s_waitcnt lgkmcnt(4)\n"
"v_exp_f32 v205, v205\n"
"v_mfma_f32_16x16x32_bf16 v[76:79], a[108:111], v[208:211], v[76:79]\n"
"ds_read_b64_tr_b16 a[68:69], v39 offset:0\n"
"v_mfma_f32_16x16x32_bf16 v[80:83], a[112:115], v[208:211], v[80:83]\n"
"v_exp_f32 v206, v206\n"
"v_mfma_f32_16x16x32_bf16 v[84:87], a[116:119], v[208:211], v[84:87]\n"
"ds_read_b64_tr_b16 a[70:71], v39 offset:4096\n"
"v_cvt_pk_bf16_f32 v214, v204, v205\n"
"v_mfma_f32_16x16x32_bf16 v[88:91], a[120:123], v[208:211], v[88:91]\n"
"v_exp_f32 v207, v207\n"
"v_mfma_f32_16x16x32_bf16 v[92:95], a[124:127], v[208:211], v[92:95]\n"
"buffer_load_dwordx4 v11, s[72:75], s85 offen offset:1024 lds\n"
"s_add_u32 s85, s85, 32768\n"
"v_mfma_f32_16x16x32_bf16 v[232:235], a[136:139], v[208:211], v[232:235]\n"
"v_cvt_pk_bf16_f32 v215, v206, v207\n"
"v_mfma_f32_16x16x32_bf16 v[200:203], a[32:35], a[240:243], v[228:231]\n"
"v_exp_f32 v192, v192\n"
"v_mfma_f32_16x16x32_bf16 v[200:203], a[36:39], a[244:247], v[200:203]\n"
"ds_read_b128 a[24:27], v36 offset:4096\n"
"v_mfma_f32_16x16x32_bf16 v[200:203], a[40:43], a[248:251], v[200:203]\n"
"v_exp_f32 v193, v193\n"
"v_mfma_f32_16x16x32_bf16 v[200:203], a[44:47], a[252:255], v[200:203]\n"
"ds_read_b64_tr_b16 a[72:73], v40 offset:0\n"
"buffer_store_dwordx4 v[46:49], v4, s[64:67], s32 offen offset:0 sc1\n"
"v_mfma_f32_16x16x32_bf16 v[204:207], a[48:51], a[240:243], v[228:231]\n"
"v_exp_f32 v194, v194\n"
"v_mfma_f32_16x16x32_bf16 v[204:207], a[52:55], a[244:247], v[204:207]\n"
"ds_read_b64_tr_b16 a[74:75], v40 offset:4096\n"
"v_cvt_pk_bf16_f32 v208, v192, v193\n"
"v_mfma_f32_16x16x32_bf16 v[204:207], a[56:59], a[248:251], v[204:207]\n"
"s_add_u32 m0, s84, 73728\n"
"v_exp_f32 v195, v195\n"
"v_mfma_f32_16x16x32_bf16 v[204:207], a[60:63], a[252:255], v[204:207]\n"
"buffer_load_dwordx4 v12, s[64:67], s48 offen sc1 lds\n"
"v_mfma_f32_16x16x32_bf16 v[96:99], a[96:99], v[212:215], v[96:99]\n"
"v_exp_f32 v196, v196\n"
"v_mfma_f32_16x16x32_bf16 v[100:103], a[100:103], v[212:215], v[100:103]\n"
"ds_read_b128 a[28:31], v37 offset:4096\n"
"v_cvt_pk_bf16_f32 v209, v194, v195\n"
"v_mfma_f32_16x16x32_bf16 v[104:107], a[104:107], v[212:215], v[104:107]\n"
"v_exp_f32 v197, v197\n"
"v_mfma_f32_16x16x32_bf16 v[108:111], a[108:111], v[212:215], v[108:111]\n"
"ds_read_b64_tr_b16 a[76:77], v41 offset:0\n"
"v_mfma_f32_16x16x32_bf16 v[112:115], a[112:115], v[212:215], v[112:115]\n"
"v_exp_f32 v198, v198\n"
"v_mfma_f32_16x16x32_bf16 v[116:119], a[116:119], v[212:215], v[116:119]\n"
"ds_read_b64_tr_b16 a[78:79], v41 offset:4096\n"
"v_cvt_pk_bf16_f32 v210, v196, v197\n"
"v_mfma_f32_16x16x32_bf16 v[120:123], a[120:123], v[212:215], v[120:123]\n"
"v_exp_f32 v199, v199\n"
"v_mfma_f32_16x16x32_bf16 v[124:127], a[124:127], v[212:215], v[124:127]\n"
"buffer_load_dwordx4 v13, s[64:67], s48 offen offset:1024 sc1 lds\n"
"s_add_u32 s48, s48, 1024\n"
"v_mfma_f32_16x16x32_bf16 v[236:239], a[136:139], v[212:215], v[236:239]\n"
"v_cvt_pk_bf16_f32 v211, v198, v199\n"
"v_mfma_f32_16x16x32_bf16 v[192:195], a[0:3], a[192:195], v[216:219]\n"
"v_exp_f32 v200, v200\n"
"v_mfma_f32_16x16x32_bf16 v[192:195], a[4:7], a[196:199], v[192:195]\n"
"ds_read_b128 a[32:35], v34 offset:8192\n"
"v_mfma_f32_16x16x32_bf16 v[192:195], a[8:11], a[200:203], v[192:195]\n"
"v_exp_f32 v201, v201\n"
"v_mfma_f32_16x16x32_bf16 v[192:195], a[12:15], a[204:207], v[192:195]\n"
"s_waitcnt lgkmcnt(3)\n"
"ds_read_b64_tr_b16 a[80:81], v42 offset:0\n"
"buffer_store_dwordx4 v[46:49], v4, s[64:67], s32 offen offset:1024 sc1\n"
"v_mfma_f32_16x16x32_bf16 v[196:199], a[16:19], a[192:195], v[216:219]\n"
"v_exp_f32 v202, v202\n"
"v_mfma_f32_16x16x32_bf16 v[196:199], a[20:23], a[196:199], v[196:199]\n"
"ds_read_b64_tr_b16 a[82:83], v42 offset:4096\n"
"v_cvt_pk_bf16_f32 v212, v200, v201\n"
"v_mfma_f32_16x16x32_bf16 v[196:199], a[24:27], a[200:203], v[196:199]\n"
"s_add_u32 m0, s84, 131072\n"
"v_exp_f32 v203, v203\n"
"v_mfma_f32_16x16x32_bf16 v[196:199], a[28:31], a[204:207], v[196:199]\n"
"buffer_load_dwordx4 v10, s[72:75], s85 offen lds\n"
"v_mfma_f32_16x16x32_bf16 v[128:131], a[96:99], v[208:211], v[128:131]\n"
"v_exp_f32 v204, v204\n"
"v_mfma_f32_16x16x32_bf16 v[132:135], a[100:103], v[208:211], v[132:135]\n"
"ds_read_b128 a[36:39], v35 offset:8192\n"
"v_cvt_pk_bf16_f32 v213, v202, v203\n"
"v_mfma_f32_16x16x32_bf16 v[136:139], a[104:107], v[208:211], v[136:139]\n"
"v_exp_f32 v205, v205\n"
"v_mfma_f32_16x16x32_bf16 v[140:143], a[108:111], v[208:211], v[140:143]\n"
"ds_read_b64_tr_b16 a[84:85], v43 offset:0\n"
"v_mfma_f32_16x16x32_bf16 v[144:147], a[112:115], v[208:211], v[144:147]\n"
"v_exp_f32 v206, v206\n"
"v_mfma_f32_16x16x32_bf16 v[148:151], a[116:119], v[208:211], v[148:151]\n"
"ds_read_b64_tr_b16 a[86:87], v43 offset:4096\n"
"v_cvt_pk_bf16_f32 v214, v204, v205\n"
"v_mfma_f32_16x16x32_bf16 v[152:155], a[120:123], v[208:211], v[152:155]\n"
"v_exp_f32 v207, v207\n"
"v_mfma_f32_16x16x32_bf16 v[156:159], a[124:127], v[208:211], v[156:159]\n"
"buffer_load_dwordx4 v11, s[72:75], s85 offen offset:1024 lds\n"
"s_add_u32 s85, s85, 32768\n"
"v_mfma_f32_16x16x32_bf16 v[240:243], a[136:139], v[208:211], v[240:243]\n"
"v_cvt_pk_bf16_f32 v215, v206, v207\n"
"v_mfma_f32_16x16x32_bf16 v[200:203], a[0:3], a[208:211], v[220:223]\n"
"v_exp_f32 v192, v192\n"
"v_mfma_f32_16x16x32_bf16 v[200:203], a[4:7], a[212:215], v[200:203]\n"
"ds_read_b128 a[40:43], v36 offset:8192\n"
"v_mfma_f32_16x16x32_bf16 v[200:203], a[8:11], a[216:219], v[200:203]\n"
"v_exp_f32 v193, v193\n"
"v_mfma_f32_16x16x32_bf16 v[200:203], a[12:15], a[220:223], v[200:203]\n"
"ds_read_b64_tr_b16 a[88:89], v44 offset:0\n"
"buffer_store_dwordx4 v[46:49], v4, s[64:67], s32 offen offset:2048 sc1\n"
"v_mfma_f32_16x16x32_bf16 v[204:207], a[16:19], a[208:211], v[220:223]\n"
"v_exp_f32 v194, v194\n"
"v_mfma_f32_16x16x32_bf16 v[204:207], a[20:23], a[212:215], v[204:207]\n"
"ds_read_b64_tr_b16 a[90:91], v44 offset:4096\n"
"v_cvt_pk_bf16_f32 v208, v192, v193\n"
"v_mfma_f32_16x16x32_bf16 v[204:207], a[24:27], a[216:219], v[204:207]\n"
"s_add_u32 m0, s84, 139264\n"
"v_exp_f32 v195, v195\n"
"v_mfma_f32_16x16x32_bf16 v[204:207], a[28:31], a[220:223], v[204:207]\n"
"buffer_load_dwordx4 v12, s[64:67], s48 offen sc1 lds\n"
"v_mfma_f32_16x16x32_bf16 v[160:163], a[96:99], v[212:215], v[160:163]\n"
"v_exp_f32 v196, v196\n"
"v_mfma_f32_16x16x32_bf16 v[164:167], a[100:103], v[212:215], v[164:167]\n"
"ds_read_b128 a[44:47], v37 offset:8192\n"
"v_cvt_pk_bf16_f32 v209, v194, v195\n"
"v_mfma_f32_16x16x32_bf16 v[168:171], a[104:107], v[212:215], v[168:171]\n"
"v_exp_f32 v197, v197\n"
"v_mfma_f32_16x16x32_bf16 v[172:175], a[108:111], v[212:215], v[172:175]\n"
"ds_read_b64_tr_b16 a[92:93], v45 offset:0\n"
"v_mfma_f32_16x16x32_bf16 v[176:179], a[112:115], v[212:215], v[176:179]\n"
"v_exp_f32 v198, v198\n"
"v_mfma_f32_16x16x32_bf16 v[180:183], a[116:119], v[212:215], v[180:183]\n"
"ds_read_b64_tr_b16 a[94:95], v45 offset:4096\n"
"v_cvt_pk_bf16_f32 v210, v196, v197\n"
"v_mfma_f32_16x16x32_bf16 v[184:187], a[120:123], v[212:215], v[184:187]\n"
"v_exp_f32 v199, v199\n"
"v_mfma_f32_16x16x32_bf16 v[188:191], a[124:127], v[212:215], v[188:191]\n"
"buffer_load_dwordx4 v13, s[64:67], s48 offen offset:1024 sc1 lds\n"
"s_add_u32 s48, s48, 1024\n"
"v_mfma_f32_16x16x32_bf16 v[244:247], a[136:139], v[212:215], v[244:247]\n"
"v_cvt_pk_bf16_f32 v211, v198, v199\n"
"v_mfma_f32_16x16x32_bf16 v[192:195], a[0:3], a[224:227], v[224:227]\n"
"v_exp_f32 v200, v200\n"
"v_mfma_f32_16x16x32_bf16 v[192:195], a[4:7], a[228:231], v[192:195]\n"
"ds_read_b128 a[48:51], v34 offset:12288\n"
"v_mfma_f32_16x16x32_bf16 v[192:195], a[8:11], a[232:235], v[192:195]\n"
"v_exp_f32 v201, v201\n"
"v_mfma_f32_16x16x32_bf16 v[192:195], a[12:15], a[236:239], v[192:195]\n"
"ds_read_b64_tr_b16 a[96:97], v38 offset:8192\n"
"buffer_store_dwordx4 v[46:49], v4, s[64:67], s32 offen offset:3072 sc1\n"
"v_mfma_f32_16x16x32_bf16 v[196:199], a[16:19], a[224:227], v[224:227]\n"
"v_exp_f32 v202, v202\n"
"v_mfma_f32_16x16x32_bf16 v[196:199], a[20:23], a[228:231], v[196:199]\n"
"ds_read_b64_tr_b16 a[98:99], v38 offset:12288\n"
"v_cvt_pk_bf16_f32 v212, v200, v201\n"
"v_mfma_f32_16x16x32_bf16 v[196:199], a[24:27], a[232:235], v[196:199]\n"
"s_add_u32 m0, s84, 16384\n"
"v_exp_f32 v203, v203\n"
"v_mfma_f32_16x16x32_bf16 v[196:199], a[28:31], a[236:239], v[196:199]\n"
"buffer_load_dwordx4 v10, s[72:75], s85 offen lds\n"
"v_add_u32 v38, s93, v38\n"
"v_mfma_f32_16x16x32_bf16 v[64:67], a[64:67], v[208:211], v[64:67]\n"
"v_exp_f32 v204, v204\n"
"v_mfma_f32_16x16x32_bf16 v[68:71], a[68:71], v[208:211], v[68:71]\n"
"ds_read_b128 a[52:55], v35 offset:12288\n"
"v_cvt_pk_bf16_f32 v213, v202, v203\n"
"v_mfma_f32_16x16x32_bf16 v[72:75], a[72:75], v[208:211], v[72:75]\n"
"s_waitcnt lgkmcnt(4)\n"
"v_exp_f32 v205, v205\n"
"v_mfma_f32_16x16x32_bf16 v[76:79], a[76:79], v[208:211], v[76:79]\n"
"ds_read_b64_tr_b16 a[100:101], v39 offset:8192\n"
"v_mfma_f32_16x16x32_bf16 v[80:83], a[80:83], v[208:211], v[80:83]\n"
"v_exp_f32 v206, v206\n"
"v_mfma_f32_16x16x32_bf16 v[84:87], a[84:87], v[208:211], v[84:87]\n"
"ds_read_b64_tr_b16 a[102:103], v39 offset:12288\n"
"v_cvt_pk_bf16_f32 v214, v204, v205\n"
"v_mfma_f32_16x16x32_bf16 v[88:91], a[88:91], v[208:211], v[88:91]\n"
"v_exp_f32 v207, v207\n"
"v_mfma_f32_16x16x32_bf16 v[92:95], a[92:95], v[208:211], v[92:95]\n"
"buffer_load_dwordx4 v11, s[72:75], s85 offen offset:1024 lds\n"
"v_add_u32 v34, s90, v34\n"
"v_add_u32 v35, s90, v35\n"
"v_mfma_f32_16x16x32_bf16 v[232:235], a[136:139], v[208:211], v[232:235]\n"
"s_add_u32 s85, s85, 32768\n"
"v_cvt_pk_bf16_f32 v215, v206, v207\n"
"v_add_u32 v39, s93, v39\n"
"v_mfma_f32_16x16x32_bf16 v[200:203], a[0:3], a[240:243], v[228:231]\n"
"v_exp_f32 v192, v192\n"
"v_mfma_f32_16x16x32_bf16 v[200:203], a[4:7], a[244:247], v[200:203]\n"
"ds_read_b128 a[56:59], v36 offset:12288\n"
"v_mfma_f32_16x16x32_bf16 v[200:203], a[8:11], a[248:251], v[200:203]\n"
"v_exp_f32 v193, v193\n"
"v_mfma_f32_16x16x32_bf16 v[200:203], a[12:15], a[252:255], v[200:203]\n"
"ds_read_b64_tr_b16 a[104:105], v40 offset:8192\n"
"buffer_store_dwordx4 v[46:49], v4, s[64:67], s33 offen offset:0 sc1\n"
"v_mfma_f32_16x16x32_bf16 v[204:207], a[16:19], a[240:243], v[228:231]\n"
"v_exp_f32 v194, v194\n"
"v_mfma_f32_16x16x32_bf16 v[204:207], a[20:23], a[244:247], v[204:207]\n"
"ds_read_b64_tr_b16 a[106:107], v40 offset:12288\n"
"v_cvt_pk_bf16_f32 v208, v192, v193\n"
"v_mfma_f32_16x16x32_bf16 v[204:207], a[24:27], a[248:251], v[204:207]\n"
"s_add_u32 m0, s84, 24576\n"
"v_exp_f32 v195, v195\n"
"v_mfma_f32_16x16x32_bf16 v[204:207], a[28:31], a[252:255], v[204:207]\n"
"buffer_load_dwordx4 v12, s[64:67], s48 offen sc1 lds\n"
"v_add_u32 v40, s93, v40\n"
"v_mfma_f32_16x16x32_bf16 v[96:99], a[64:67], v[212:215], v[96:99]\n"
"v_exp_f32 v196, v196\n"
"v_mfma_f32_16x16x32_bf16 v[100:103], a[68:71], v[212:215], v[100:103]\n"
"ds_read_b128 a[60:63], v37 offset:12288\n"
"v_cvt_pk_bf16_f32 v209, v194, v195\n"
"v_mfma_f32_16x16x32_bf16 v[104:107], a[72:75], v[212:215], v[104:107]\n"
"v_exp_f32 v197, v197\n"
"v_mfma_f32_16x16x32_bf16 v[108:111], a[76:79], v[212:215], v[108:111]\n"
"ds_read_b64_tr_b16 a[108:109], v41 offset:8192\n"
"v_mfma_f32_16x16x32_bf16 v[112:115], a[80:83], v[212:215], v[112:115]\n"
"v_exp_f32 v198, v198\n"
"v_mfma_f32_16x16x32_bf16 v[116:119], a[84:87], v[212:215], v[116:119]\n"
"ds_read_b64_tr_b16 a[110:111], v41 offset:12288\n"
"v_cvt_pk_bf16_f32 v210, v196, v197\n"
"v_mfma_f32_16x16x32_bf16 v[120:123], a[88:91], v[212:215], v[120:123]\n"
"v_exp_f32 v199, v199\n"
"v_mfma_f32_16x16x32_bf16 v[124:127], a[92:95], v[212:215], v[124:127]\n"
"s_mov_b32 s43, s90\n"
"buffer_load_dwordx4 v13, s[64:67], s48 offen offset:1024 sc1 lds\n"
"v_add_u32 v36, s90, v36\n"
"v_add_u32 v37, s90, v37\n"
"s_mov_b32 s90, s91\n"
"s_mov_b32 s91, s92\n"
"s_mov_b32 s92, s43\n"
"v_mfma_f32_16x16x32_bf16 v[236:239], a[136:139], v[212:215], v[236:239]\n"
"s_add_u32 s48, s48, 1024\n"
"v_cvt_pk_bf16_f32 v211, v198, v199\n"
"v_add_u32 v41, s93, v41\n"
"s_waitcnt lgkmcnt(2)\n"
"s_barrier\n"
"v_mfma_f32_16x16x32_bf16 v[192:195], a[32:35], a[192:195], v[216:219]\n"
"v_exp_f32 v200, v200\n"
"v_mfma_f32_16x16x32_bf16 v[192:195], a[36:39], a[196:199], v[192:195]\n"
"v_mfma_f32_16x16x32_bf16 v[192:195], a[40:43], a[200:203], v[192:195]\n"
"v_exp_f32 v201, v201\n"
"v_mfma_f32_16x16x32_bf16 v[192:195], a[44:47], a[204:207], v[192:195]\n"
"ds_read_b64_tr_b16 a[112:113], v42 offset:8192\n"
"v_mfma_f32_16x16x32_bf16 v[196:199], a[48:51], a[192:195], v[216:219]\n"
"v_exp_f32 v202, v202\n"
"v_mfma_f32_16x16x32_bf16 v[196:199], a[52:55], a[196:199], v[196:199]\n"
"ds_read_b64_tr_b16 a[114:115], v42 offset:12288\n"
"v_cvt_pk_bf16_f32 v212, v200, v201\n"
"v_mfma_f32_16x16x32_bf16 v[196:199], a[56:59], a[200:203], v[196:199]\n"
"s_add_u32 m0, s84, 0\n"
"v_exp_f32 v203, v203\n"
"v_mfma_f32_16x16x32_bf16 v[196:199], a[60:63], a[204:207], v[196:199]\n"
"buffer_load_dwordx4 v10, s[72:75], s85 offen lds\n"
"v_add_u32 v42, s93, v42\n"
"v_mfma_f32_16x16x32_bf16 v[128:131], a[64:67], v[208:211], v[128:131]\n"
"v_exp_f32 v204, v204\n"
"v_mfma_f32_16x16x32_bf16 v[132:135], a[68:71], v[208:211], v[132:135]\n"
"v_cvt_pk_bf16_f32 v213, v202, v203\n"
"v_mfma_f32_16x16x32_bf16 v[136:139], a[72:75], v[208:211], v[136:139]\n"
"v_exp_f32 v205, v205\n"
"v_mfma_f32_16x16x32_bf16 v[140:143], a[76:79], v[208:211], v[140:143]\n"
"ds_read_b64_tr_b16 a[116:117], v43 offset:8192\n"
"v_mfma_f32_16x16x32_bf16 v[144:147], a[80:83], v[208:211], v[144:147]\n"
"v_exp_f32 v206, v206\n"
"v_mfma_f32_16x16x32_bf16 v[148:151], a[84:87], v[208:211], v[148:151]\n"
"ds_read_b64_tr_b16 a[118:119], v43 offset:12288\n"
"v_cvt_pk_bf16_f32 v214, v204, v205\n"
"v_mfma_f32_16x16x32_bf16 v[152:155], a[88:91], v[208:211], v[152:155]\n"
"v_exp_f32 v207, v207\n"
"v_mfma_f32_16x16x32_bf16 v[156:159], a[92:95], v[208:211], v[156:159]\n"
"buffer_load_dwordx4 v11, s[72:75], s85 offen offset:1024 lds\n"
"s_add_u32 s85, s85, 32768\n"
"v_mfma_f32_16x16x32_bf16 v[240:243], a[136:139], v[208:211], v[240:243]\n"
"v_cvt_pk_bf16_f32 v215, v206, v207\n"
"v_add_u32 v43, s93, v43\n"
"v_mfma_f32_16x16x32_bf16 v[200:203], a[32:35], a[208:211], v[220:223]\n"
"v_exp_f32 v192, v192\n"
"v_mfma_f32_16x16x32_bf16 v[200:203], a[36:39], a[212:215], v[200:203]\n"
"v_mfma_f32_16x16x32_bf16 v[200:203], a[40:43], a[216:219], v[200:203]\n"
"v_exp_f32 v193, v193\n"
"v_mfma_f32_16x16x32_bf16 v[200:203], a[44:47], a[220:223], v[200:203]\n"
"ds_read_b64_tr_b16 a[120:121], v44 offset:8192\n"
"buffer_store_dwordx4 v[46:49], v4, s[64:67], s33 offen offset:1024 sc1\n"
"v_mfma_f32_16x16x32_bf16 v[204:207], a[48:51], a[208:211], v[220:223]\n"
"v_exp_f32 v194, v194\n"
"v_mfma_f32_16x16x32_bf16 v[204:207], a[52:55], a[212:215], v[204:207]\n"
"ds_read_b64_tr_b16 a[122:123], v44 offset:12288\n"
"v_cvt_pk_bf16_f32 v208, v192, v193\n"
"v_mfma_f32_16x16x32_bf16 v[204:207], a[56:59], a[216:219], v[204:207]\n"
"s_add_u32 m0, s84, 8192\n"
"v_exp_f32 v195, v195\n"
"v_mfma_f32_16x16x32_bf16 v[204:207], a[60:63], a[220:223], v[204:207]\n"
"buffer_load_dwordx4 v12, s[64:67], s48 offen sc1 lds\n"
"v_add_u32 v44, s93, v44\n"
"v_mfma_f32_16x16x32_bf16 v[160:163], a[64:67], v[212:215], v[160:163]\n"
"v_exp_f32 v196, v196\n"
"v_mfma_f32_16x16x32_bf16 v[164:167], a[68:71], v[212:215], v[164:167]\n"
"v_cvt_pk_bf16_f32 v209, v194, v195\n"
"v_mfma_f32_16x16x32_bf16 v[168:171], a[72:75], v[212:215], v[168:171]\n"
"v_exp_f32 v197, v197\n"
"v_mfma_f32_16x16x32_bf16 v[172:175], a[76:79], v[212:215], v[172:175]\n"
"ds_read_b64_tr_b16 a[124:125], v45 offset:8192\n"
"v_mfma_f32_16x16x32_bf16 v[176:179], a[80:83], v[212:215], v[176:179]\n"
"v_exp_f32 v198, v198\n"
"v_mfma_f32_16x16x32_bf16 v[180:183], a[84:87], v[212:215], v[180:183]\n"
"ds_read_b64_tr_b16 a[126:127], v45 offset:12288\n"
"v_cvt_pk_bf16_f32 v210, v196, v197\n"
"v_mfma_f32_16x16x32_bf16 v[184:187], a[88:91], v[212:215], v[184:187]\n"
"v_exp_f32 v199, v199\n"
"v_mfma_f32_16x16x32_bf16 v[188:191], a[92:95], v[212:215], v[188:191]\n"
"buffer_load_dwordx4 v13, s[64:67], s48 offen offset:1024 sc1 lds\n"
"s_add_u32 s48, s48, 1024\n"
"v_mfma_f32_16x16x32_bf16 v[244:247], a[136:139], v[212:215], v[244:247]\n"
"s_mov_b32 s43, s93\n"
"v_cvt_pk_bf16_f32 v211, v198, v199\n"
"v_add_u32 v45, s93, v45\n"
"s_mov_b32 s93, s94\n"
"s_mov_b32 s94, s95\n"
"s_mov_b32 s95, s80\n"
"s_mov_b32 s80, s43\n"
"s_waitcnt vmcnt(23) lgkmcnt(0)\n"
"s_barrier\n"
"v_mfma_f32_16x16x32_bf16 v[192:195], a[32:35], a[224:227], v[224:227]\n"
"v_exp_f32 v200, v200\n"
"v_mfma_f32_16x16x32_bf16 v[192:195], a[36:39], a[228:231], v[192:195]\n"
"v_add_u32 v36, 131072, v16\n"
"v_mfma_f32_16x16x32_bf16 v[192:195], a[40:43], a[232:235], v[192:195]\n"
"v_exp_f32 v201, v201\n"
"v_mfma_f32_16x16x32_bf16 v[192:195], a[44:47], a[236:239], v[192:195]\n"
"v_add_u32 v37, 131072, v17\n"
"v_mfma_f32_16x16x32_bf16 v[196:199], a[48:51], a[224:227], v[224:227]\n"
"v_exp_f32 v202, v202\n"
"v_mfma_f32_16x16x32_bf16 v[196:199], a[52:55], a[228:231], v[196:199]\n"
"v_cvt_pk_bf16_f32 v212, v200, v201\n"
"v_mfma_f32_16x16x32_bf16 v[196:199], a[56:59], a[232:235], v[196:199]\n"
"s_add_u32 m0, s84, 32768\n"
"v_exp_f32 v203, v203\n"
"v_mfma_f32_16x16x32_bf16 v[196:199], a[60:63], a[236:239], v[196:199]\n"
"buffer_load_dwordx4 v10, s[72:75], s85 offen lds\n"
"v_add_u32 v38, 131072, v18\n"
"v_mfma_f32_16x16x32_bf16 v[64:67], a[96:99], v[208:211], v[64:67]\n"
"v_exp_f32 v204, v204\n"
"v_mfma_f32_16x16x32_bf16 v[68:71], a[100:103], v[208:211], v[68:71]\n"
"v_add_u32 v39, 131072, v19\n"
"v_cvt_pk_bf16_f32 v213, v202, v203\n"
"v_mfma_f32_16x16x32_bf16 v[72:75], a[104:107], v[208:211], v[72:75]\n"
"v_exp_f32 v205, v205\n"
"v_mfma_f32_16x16x32_bf16 v[76:79], a[108:111], v[208:211], v[76:79]\n"
"buffer_load_dwordx4 v11, s[72:75], s85 offen offset:1024 lds\n"
"v_add_u32 v40, 131072, v20\n"
"v_mfma_f32_16x16x32_bf16 v[80:83], a[112:115], v[208:211], v[80:83]\n"
"s_add_u32 s85, s85, 32768\n"
"v_exp_f32 v206, v206\n"
"v_mfma_f32_16x16x32_bf16 v[84:87], a[116:119], v[208:211], v[84:87]\n"
"v_cvt_pk_bf16_f32 v214, v204, v205\n"
"v_mfma_f32_16x16x32_bf16 v[88:91], a[120:123], v[208:211], v[88:91]\n"
"v_exp_f32 v207, v207\n"
"v_mfma_f32_16x16x32_bf16 v[92:95], a[124:127], v[208:211], v[92:95]\n"
"v_add_u32 v41, 131072, v21\n"
"v_add_u32 v42, 131072, v22\n"
"v_add_u32 v43, 131072, v23\n"
"v_mfma_f32_16x16x32_bf16 v[232:235], a[136:139], v[208:211], v[232:235]\n"
"v_cvt_pk_bf16_f32 v215, v206, v207\n"
"v_mfma_f32_16x16x32_bf16 v[200:203], a[32:35], a[240:243], v[228:231]\n"
"v_exp_f32 v192, v192\n"
"v_mfma_f32_16x16x32_bf16 v[200:203], a[36:39], a[244:247], v[200:203]\n"
"v_add_u32 v28, 65536, v16\n"
"v_mfma_f32_16x16x32_bf16 v[200:203], a[40:43], a[248:251], v[200:203]\n"
"v_exp_f32 v193, v193\n"
"v_mfma_f32_16x16x32_bf16 v[200:203], a[44:47], a[252:255], v[200:203]\n"
"v_add_u32 v29, 65536, v17\n"
"v_mfma_f32_16x16x32_bf16 v[204:207], a[48:51], a[240:243], v[228:231]\n"
"v_exp_f32 v194, v194\n"
"v_mfma_f32_16x16x32_bf16 v[204:207], a[52:55], a[244:247], v[204:207]\n"
"v_cvt_pk_bf16_f32 v208, v192, v193\n"
"v_mfma_f32_16x16x32_bf16 v[204:207], a[56:59], a[248:251], v[204:207]\n"
"s_add_u32 m0, s84, 40960\n"
"v_exp_f32 v195, v195\n"
"v_mfma_f32_16x16x32_bf16 v[204:207], a[60:63], a[252:255], v[204:207]\n"
"buffer_load_dwordx4 v12, s[64:67], s48 offen sc1 lds\n"
"v_add_u32 v30, 65536, v18\n"
"v_mfma_f32_16x16x32_bf16 v[96:99], a[96:99], v[212:215], v[96:99]\n"
"v_exp_f32 v196, v196\n"
"v_mfma_f32_16x16x32_bf16 v[100:103], a[100:103], v[212:215], v[100:103]\n"
"v_add_u32 v31, 65536, v19\n"
"v_cvt_pk_bf16_f32 v209, v194, v195\n"
"v_mfma_f32_16x16x32_bf16 v[104:107], a[104:107], v[212:215], v[104:107]\n"
"v_exp_f32 v197, v197\n"
"v_mfma_f32_16x16x32_bf16 v[108:111], a[108:111], v[212:215], v[108:111]\n"
"buffer_load_dwordx4 v13, s[64:67], s48 offen offset:1024 sc1 lds\n"
"v_add_u32 v32, 65536, v20\n"
"v_mfma_f32_16x16x32_bf16 v[112:115], a[112:115], v[212:215], v[112:115]\n"
"s_add_u32 s48, s48, 1024\n"
"v_exp_f32 v198, v198\n"
"v_mfma_f32_16x16x32_bf16 v[116:119], a[116:119], v[212:215], v[116:119]\n"
"v_cvt_pk_bf16_f32 v210, v196, v197\n"
"v_mfma_f32_16x16x32_bf16 v[120:123], a[120:123], v[212:215], v[120:123]\n"
"s_lshl_b32 s17, s5, 12\n"
"v_exp_f32 v199, v199\n"
"v_mfma_f32_16x16x32_bf16 v[124:127], a[124:127], v[212:215], v[124:127]\n"
"s_xor_b32 s16, s82, 0\n"
"v_add_u32 v33, 65536, v21\n"
"v_add_u32 v34, 65536, v22\n"
"v_add_u32 v35, 65536, v23\n"
"v_mfma_f32_16x16x32_bf16 v[236:239], a[136:139], v[212:215], v[236:239]\n"
"s_add_u32 m0, s84, 49152\n"
"v_cvt_pk_bf16_f32 v211, v198, v199\n"
"s_nop 0\n"
"v_exp_f32 v200, v200\n"
"s_nop 0\n"
"buffer_load_dwordx4 v10, s[72:75], s85 offen lds\n"
"s_nop 0\n"
"v_exp_f32 v201, v201\n"
"s_nop 0\n"
"buffer_load_dwordx4 v11, s[72:75], s85 offen offset:1024 lds\n"
"s_nop 0\n"
"v_exp_f32 v202, v202\n"
"s_nop 0\n"
"s_add_u32 m0, s84, 57344\n"
"s_add_u32 s85, s85, 32768\n"
"buffer_load_dwordx4 v12, s[64:67], s48 offen sc1 lds\n"
"buffer_load_dwordx4 v13, s[64:67], s48 offen offset:1024 sc1 lds\n"
"v_cvt_pk_bf16_f32 v212, v200, v201\n"
"s_nop 0\n"
"v_exp_f32 v203, v203\n"
"s_nop 0\n"
"v_rcp_f32 v46, v232\n"
"s_add_u32 s16, s16, s81\n"
"s_mul_i32 s16, s16, 136192\n"
"s_add_u32 s16, s16, s17\n"
"v_pk_mul_f32 v[64:65], v[64:65], v[46:47] op_sel_hi:[1,0]\n"
"v_pk_mul_f32 v[66:67], v[66:67], v[46:47] op_sel_hi:[1,0]\n"
"v_pk_mul_f32 v[68:69], v[68:69], v[46:47] op_sel_hi:[1,0]\n"
"v_pk_mul_f32 v[70:71], v[70:71], v[46:47] op_sel_hi:[1,0]\n"
"v_cvt_pk_bf16_f32 v64, v64, v65\n"
"v_cvt_pk_bf16_f32 v65, v66, v67\n"
"v_cvt_pk_bf16_f32 v66, v68, v69\n"
"v_cvt_pk_bf16_f32 v67, v70, v71\n"
"buffer_store_dwordx4 v[64:67], v4, s[64:67], s16 offen offset:0 sc1\n"
"v_pk_mul_f32 v[72:73], v[72:73], v[46:47] op_sel_hi:[1,0]\n"
"v_pk_mul_f32 v[74:75], v[74:75], v[46:47] op_sel_hi:[1,0]\n"
"v_pk_mul_f32 v[76:77], v[76:77], v[46:47] op_sel_hi:[1,0]\n"
"v_pk_mul_f32 v[78:79], v[78:79], v[46:47] op_sel_hi:[1,0]\n"
"v_cvt_pk_bf16_f32 v72, v72, v73\n"
"v_cvt_pk_bf16_f32 v73, v74, v75\n"
"v_cvt_pk_bf16_f32 v74, v76, v77\n"
"v_cvt_pk_bf16_f32 v75, v78, v79\n"
"buffer_store_dwordx4 v[72:75], v4, s[64:67], s16 offen offset:1024 sc1\n"
"v_pk_mul_f32 v[80:81], v[80:81], v[46:47] op_sel_hi:[1,0]\n"
"v_pk_mul_f32 v[82:83], v[82:83], v[46:47] op_sel_hi:[1,0]\n"
"v_pk_mul_f32 v[84:85], v[84:85], v[46:47] op_sel_hi:[1,0]\n"
"v_pk_mul_f32 v[86:87], v[86:87], v[46:47] op_sel_hi:[1,0]\n"
"v_cvt_pk_bf16_f32 v80, v80, v81\n"
"v_cvt_pk_bf16_f32 v81, v82, v83\n"
"v_cvt_pk_bf16_f32 v82, v84, v85\n"
"v_cvt_pk_bf16_f32 v83, v86, v87\n"
"buffer_store_dwordx4 v[80:83], v4, s[64:67], s16 offen offset:2048 sc1\n"
"v_pk_mul_f32 v[88:89], v[88:89], v[46:47] op_sel_hi:[1,0]\n"
"v_pk_mul_f32 v[90:91], v[90:91], v[46:47] op_sel_hi:[1,0]\n"
"v_pk_mul_f32 v[92:93], v[92:93], v[46:47] op_sel_hi:[1,0]\n"
"v_pk_mul_f32 v[94:95], v[94:95], v[46:47] op_sel_hi:[1,0]\n"
"v_cvt_pk_bf16_f32 v88, v88, v89\n"
"v_cvt_pk_bf16_f32 v89, v90, v91\n"
"v_cvt_pk_bf16_f32 v90, v92, v93\n"
"v_cvt_pk_bf16_f32 v91, v94, v95\n"
"buffer_store_dwordx4 v[88:91], v4, s[64:67], s16 offen offset:3072 sc1\n"
"v_mfma_f32_16x16x32_bf16 v[128:131], a[96:99], v[208:211], v[128:131]\n"
"s_add_u32 s48, s48, 1024\n"
"v_exp_f32 v204, v204\n"
"v_mfma_f32_16x16x32_bf16 v[132:135], a[100:103], v[208:211], v[132:135]\n"
"v_cvt_pk_bf16_f32 v213, v202, v203\n"
"v_mfma_f32_16x16x32_bf16 v[136:139], a[104:107], v[208:211], v[136:139]\n"
"v_exp_f32 v205, v205\n"
"v_mfma_f32_16x16x32_bf16 v[140:143], a[108:111], v[208:211], v[140:143]\n"
"v_mfma_f32_16x16x32_bf16 v[144:147], a[112:115], v[208:211], v[144:147]\n"
"v_exp_f32 v206, v206\n"
"v_mfma_f32_16x16x32_bf16 v[148:151], a[116:119], v[208:211], v[148:151]\n"
"v_cvt_pk_bf16_f32 v214, v204, v205\n"
"v_mfma_f32_16x16x32_bf16 v[152:155], a[120:123], v[208:211], v[152:155]\n"
"v_exp_f32 v207, v207\n"
"v_mfma_f32_16x16x32_bf16 v[156:159], a[124:127], v[208:211], v[156:159]\n"
"s_lshl_b32 s17, s5, 12\n"
"v_mfma_f32_16x16x32_bf16 v[240:243], a[136:139], v[208:211], v[240:243]\n"
"s_xor_b32 s16, s82, 1\n"
"v_cvt_pk_bf16_f32 v215, v206, v207\n"
"s_nop 0\n"
"v_rcp_f32 v48, v236\n"
"s_add_u32 s16, s16, s81\n"
"s_mul_i32 s16, s16, 136192\n"
"s_add_u32 s16, s16, s17\n"
"v_pk_mul_f32 v[96:97], v[96:97], v[48:49] op_sel_hi:[1,0]\n"
"v_pk_mul_f32 v[98:99], v[98:99], v[48:49] op_sel_hi:[1,0]\n"
"v_pk_mul_f32 v[100:101], v[100:101], v[48:49] op_sel_hi:[1,0]\n"
"v_pk_mul_f32 v[102:103], v[102:103], v[48:49] op_sel_hi:[1,0]\n"
"v_cvt_pk_bf16_f32 v96, v96, v97\n"
"v_cvt_pk_bf16_f32 v97, v98, v99\n"
"v_cvt_pk_bf16_f32 v98, v100, v101\n"
"v_cvt_pk_bf16_f32 v99, v102, v103\n"
"buffer_store_dwordx4 v[96:99], v4, s[64:67], s16 offen offset:0 sc1\n"
"v_pk_mul_f32 v[104:105], v[104:105], v[48:49] op_sel_hi:[1,0]\n"
"v_pk_mul_f32 v[106:107], v[106:107], v[48:49] op_sel_hi:[1,0]\n"
"v_pk_mul_f32 v[108:109], v[108:109], v[48:49] op_sel_hi:[1,0]\n"
"v_pk_mul_f32 v[110:111], v[110:111], v[48:49] op_sel_hi:[1,0]\n"
"v_cvt_pk_bf16_f32 v104, v104, v105\n"
"v_cvt_pk_bf16_f32 v105, v106, v107\n"
"v_cvt_pk_bf16_f32 v106, v108, v109\n"
"v_cvt_pk_bf16_f32 v107, v110, v111\n"
"buffer_store_dwordx4 v[104:107], v4, s[64:67], s16 offen offset:1024 sc1\n"
"v_pk_mul_f32 v[112:113], v[112:113], v[48:49] op_sel_hi:[1,0]\n"
"v_pk_mul_f32 v[114:115], v[114:115], v[48:49] op_sel_hi:[1,0]\n"
"v_pk_mul_f32 v[116:117], v[116:117], v[48:49] op_sel_hi:[1,0]\n"
"v_pk_mul_f32 v[118:119], v[118:119], v[48:49] op_sel_hi:[1,0]\n"
"v_cvt_pk_bf16_f32 v112, v112, v113\n"
"v_cvt_pk_bf16_f32 v113, v114, v115\n"
"v_cvt_pk_bf16_f32 v114, v116, v117\n"
"v_cvt_pk_bf16_f32 v115, v118, v119\n"
"buffer_store_dwordx4 v[112:115], v4, s[64:67], s16 offen offset:2048 sc1\n"
"v_pk_mul_f32 v[120:121], v[120:121], v[48:49] op_sel_hi:[1,0]\n"
"v_pk_mul_f32 v[122:123], v[122:123], v[48:49] op_sel_hi:[1,0]\n"
"v_pk_mul_f32 v[124:125], v[124:125], v[48:49] op_sel_hi:[1,0]\n"
"v_pk_mul_f32 v[126:127], v[126:127], v[48:49] op_sel_hi:[1,0]\n"
"v_cvt_pk_bf16_f32 v120, v120, v121\n"
"v_cvt_pk_bf16_f32 v121, v122, v123\n"
"v_cvt_pk_bf16_f32 v122, v124, v125\n"
"v_cvt_pk_bf16_f32 v123, v126, v127\n"
"buffer_store_dwordx4 v[120:123], v4, s[64:67], s16 offen offset:3072 sc1\n"
"s_nop 0\n"
"ds_read_b64_tr_b16 a[140:141], v28 offset:16384\n"
"ds_read_b64_tr_b16 a[142:143], v28 offset:20480\n"
"s_nop 0\n"
"ds_read_b128 a[0:3], v32 offset:24576\n"
"ds_read_b128 a[4:7], v33 offset:24576\n"
"s_nop 0\n"
"ds_read_b128 a[8:11], v34 offset:24576\n"
"ds_read_b128 a[12:15], v35 offset:24576\n"
"s_nop 0\n"
"ds_read_b64_tr_b16 a[144:145], v29 offset:16384\n"
"ds_read_b64_tr_b16 a[146:147], v29 offset:20480\n"
"s_nop 0\n"
"ds_read_b64_tr_b16 a[148:149], v30 offset:16384\n"
"ds_read_b64_tr_b16 a[150:151], v30 offset:20480\n"
"s_nop 0\n"
"ds_read_b64_tr_b16 a[152:153], v31 offset:16384\n"
"ds_read_b64_tr_b16 a[154:155], v31 offset:20480\n"
"s_nop 0\n"
"v_mfma_f32_16x16x32_bf16 v[160:163], a[96:99], v[212:215], v[160:163]\n"
"v_mfma_f32_16x16x32_bf16 v[164:167], a[100:103], v[212:215], v[164:167]\n"
"v_mfma_f32_16x16x32_bf16 v[168:171], a[104:107], v[212:215], v[168:171]\n"
"v_mfma_f32_16x16x32_bf16 v[172:175], a[108:111], v[212:215], v[172:175]\n"
"v_mfma_f32_16x16x32_bf16 v[176:179], a[112:115], v[212:215], v[176:179]\n"
"v_mfma_f32_16x16x32_bf16 v[180:183], a[116:119], v[212:215], v[180:183]\n"
"v_mfma_f32_16x16x32_bf16 v[184:187], a[120:123], v[212:215], v[184:187]\n"
"v_mfma_f32_16x16x32_bf16 v[188:191], a[124:127], v[212:215], v[188:191]\n"
"s_lshl_b32 s17, s5, 12\n"
"v_mfma_f32_16x16x32_bf16 v[244:247], a[136:139], v[212:215], v[244:247]\n"
"s_xor_b32 s16, s82, 2\n"
"v_rcp_f32 v50, v240\n"
"s_add_u32 s16, s16, s81\n"
"s_mul_i32 s16, s16, 136192\n"
"s_add_u32 s16, s16, s17\n"
"v_pk_mul_f32 v[128:129], v[128:129], v[50:51] op_sel_hi:[1,0]\n"
"v_pk_mul_f32 v[130:131], v[130:131], v[50:51] op_sel_hi:[1,0]\n"
"v_pk_mul_f32 v[132:133], v[132:133], v[50:51] op_sel_hi:[1,0]\n"
"v_pk_mul_f32 v[134:135], v[134:135], v[50:51] op_sel_hi:[1,0]\n"
"v_cvt_pk_bf16_f32 v128, v128, v129\n"
"v_cvt_pk_bf16_f32 v129, v130, v131\n"
"v_cvt_pk_bf16_f32 v130, v132, v133\n"
"v_cvt_pk_bf16_f32 v131, v134, v135\n"
"buffer_store_dwordx4 v[128:131], v4, s[64:67], s16 offen offset:0 sc1\n"
"v_pk_mul_f32 v[136:137], v[136:137], v[50:51] op_sel_hi:[1,0]\n"
"v_pk_mul_f32 v[138:139], v[138:139], v[50:51] op_sel_hi:[1,0]\n"
"v_pk_mul_f32 v[140:141], v[140:141], v[50:51] op_sel_hi:[1,0]\n"
"v_pk_mul_f32 v[142:143], v[142:143], v[50:51] op_sel_hi:[1,0]\n"
"v_cvt_pk_bf16_f32 v136, v136, v137\n"
"v_cvt_pk_bf16_f32 v137, v138, v139\n"
"v_cvt_pk_bf16_f32 v138, v140, v141\n"
"v_cvt_pk_bf16_f32 v139, v142, v143\n"
"buffer_store_dwordx4 v[136:139], v4, s[64:67], s16 offen offset:1024 sc1\n"
"v_pk_mul_f32 v[144:145], v[144:145], v[50:51] op_sel_hi:[1,0]\n"
"v_pk_mul_f32 v[146:147], v[146:147], v[50:51] op_sel_hi:[1,0]\n"
"v_pk_mul_f32 v[148:149], v[148:149], v[50:51] op_sel_hi:[1,0]\n"
"v_pk_mul_f32 v[150:151], v[150:151], v[50:51] op_sel_hi:[1,0]\n"
"v_cvt_pk_bf16_f32 v144, v144, v145\n"
"v_cvt_pk_bf16_f32 v145, v146, v147\n"
"v_cvt_pk_bf16_f32 v146, v148, v149\n"
"v_cvt_pk_bf16_f32 v147, v150, v151\n"
"buffer_store_dwordx4 v[144:147], v4, s[64:67], s16 offen offset:2048 sc1\n"
"v_pk_mul_f32 v[152:153], v[152:153], v[50:51] op_sel_hi:[1,0]\n"
"v_pk_mul_f32 v[154:155], v[154:155], v[50:51] op_sel_hi:[1,0]\n"
"v_pk_mul_f32 v[156:157], v[156:157], v[50:51] op_sel_hi:[1,0]\n"
"v_pk_mul_f32 v[158:159], v[158:159], v[50:51] op_sel_hi:[1,0]\n"
"v_cvt_pk_bf16_f32 v152, v152, v153\n"
"v_cvt_pk_bf16_f32 v153, v154, v155\n"
"v_cvt_pk_bf16_f32 v154, v156, v157\n"
"v_cvt_pk_bf16_f32 v155, v158, v159\n"
"buffer_store_dwordx4 v[152:155], v4, s[64:67], s16 offen offset:3072 sc1\n"
"v_max3_f32 v54, v232, v236, v240\n"
"v_mov_b32 v55, 0x53800000\n"
"v_cmp_u_f32 s[18:19], v232, v236\n"
"v_cmp_nlt_f32 vcc, v54, v55\n"
"s_nop 0\n"
"s_or_b64 s[18:19], s[18:19], vcc\n"
"v_cmp_nlt_f32 vcc, v244, v55\n"
"s_nop 0\n"
"s_or_b64 s[18:19], s[18:19], vcc\n"
"v_cmp_u_f32 vcc, v240, v240\n"
"s_nop 0\n"
"s_or_b64 vcc, s[18:19], vcc\n"
"s_cbranch_vccnz LFB_%=\n"
"LFBOK_%=:\n"
"v_rcp_f32 v52, v244\n"
"s_xor_b32 s16, s82, 3\n"
"s_add_u32 s16, s16, s81\n"
"s_mul_i32 s16, s16, 136192\n"
"s_lshl_b32 s17, s5, 12\n"
"s_add_u32 s16, s16, s17\n"
"v_pk_mul_f32 v[160:161], v[160:161], v[52:53] op_sel_hi:[1,0]\n"
"v_pk_mul_f32 v[162:163], v[162:163], v[52:53] op_sel_hi:[1,0]\n"
"v_pk_mul_f32 v[164:165], v[164:165], v[52:53] op_sel_hi:[1,0]\n"
"v_pk_mul_f32 v[166:167], v[166:167], v[52:53] op_sel_hi:[1,0]\n"
"v_cvt_pk_bf16_f32 v160, v160, v161\n"
"v_cvt_pk_bf16_f32 v161, v162, v163\n"
"v_cvt_pk_bf16_f32 v162, v164, v165\n"
"v_cvt_pk_bf16_f32 v163, v166, v167\n"
"buffer_store_dwordx4 v[160:163], v4, s[64:67], s16 offen offset:0 sc1\n"
"v_pk_mul_f32 v[168:169], v[168:169], v[52:53] op_sel_hi:[1,0]\n"
"v_pk_mul_f32 v[170:171], v[170:171], v[52:53] op_sel_hi:[1,0]\n"
"v_pk_mul_f32 v[172:173], v[172:173], v[52:53] op_sel_hi:[1,0]\n"
"v_pk_mul_f32 v[174:175], v[174:175], v[52:53] op_sel_hi:[1,0]\n"
"v_cvt_pk_bf16_f32 v168, v168, v169\n"
"v_cvt_pk_bf16_f32 v169, v170, v171\n"
"v_cvt_pk_bf16_f32 v170, v172, v173\n"
"v_cvt_pk_bf16_f32 v171, v174, v175\n"
"buffer_store_dwordx4 v[168:171], v4, s[64:67], s16 offen offset:1024 sc1\n"
"v_pk_mul_f32 v[176:177], v[176:177], v[52:53] op_sel_hi:[1,0]\n"
"v_pk_mul_f32 v[178:179], v[178:179], v[52:53] op_sel_hi:[1,0]\n"
"v_pk_mul_f32 v[180:181], v[180:181], v[52:53] op_sel_hi:[1,0]\n"
"v_pk_mul_f32 v[182:183], v[182:183], v[52:53] op_sel_hi:[1,0]\n"
"v_cvt_pk_bf16_f32 v176, v176, v177\n"
"v_cvt_pk_bf16_f32 v177, v178, v179\n"
"v_cvt_pk_bf16_f32 v178, v180, v181\n"
"v_cvt_pk_bf16_f32 v179, v182, v183\n"
"buffer_store_dwordx4 v[176:179], v4, s[64:67], s16 offen offset:2048 sc1\n"
"v_pk_mul_f32 v[184:185], v[184:185], v[52:53] op_sel_hi:[1,0]\n"
"v_pk_mul_f32 v[186:187], v[186:187], v[52:53] op_sel_hi:[1,0]\n"
"v_pk_mul_f32 v[188:189], v[188:189], v[52:53] op_sel_hi:[1,0]\n"
"v_pk_mul_f32 v[190:191], v[190:191], v[52:53] op_sel_hi:[1,0]\n"
"v_cvt_pk_bf16_f32 v184, v184, v185\n"
"v_cvt_pk_bf16_f32 v185, v186, v187\n"
"v_cvt_pk_bf16_f32 v186, v188, v189\n"
"v_cvt_pk_bf16_f32 v187, v190, v191\n"
"buffer_store_dwordx4 v[184:187], v4, s[64:67], s16 offen offset:3072 sc1\n"
"s_mov_b32 s83, 24190976\n"
"s_lshl_b32 s16, s10, 8\n"
"s_lshl_b32 s17, s12, 5\n"
"s_add_u32 s16, s16, s17\n"
"s_add_u32 s16, s16, 24190976\n"
"v_and_b32 v54, 7, v1\n"
"v_bfe_u32 v55, v1, 3, 1\n"
"v_lshl_add_u32 v54, v55, 5, v54\n"
"v_lshl_add_u32 v25, v54, 2, s16\n"
"v_and_b32 v54, 3, v1\n"
"v_lshlrev_b32 v54, 5, v54\n"
"v_xor_b32 v54, v54, v6\n"
"v_add_u32 v54, 24190976, v54\n"
"v_cmp_gt_u32 vcc, 16, v1\n"
"s_nop 1\n"
"v_cndmask_b32 v25, v54, v25, vcc\n"
"s_lshr_b32 s16, s4, 1\n"
"s_lshl_b32 s17, s10, 1\n"
"s_add_u32 s16, s16, s17\n"
"s_lshl_b32 s16, s16, 2\n"
"s_add_u32 s16, s16, s12\n"
"s_lshl_b32 s16, s16, 14\n"
"s_and_b32 s17, s4, 1\n"
"s_lshl_b32 s42, s11, 1\n"
"s_add_u32 s17, s17, s42\n"
"s_lshl_b32 s52, s17, 7\n"
"s_add_u32 s51, s16, s52\n"
"s_mov_b32 s44, s20\n"
"s_mov_b32 s45, s21\n"
"s_mov_b32 s46, 2097152\n"
"s_mov_b32 s47, 0x20000\n"
"s_mov_b32 s56, s36\n"
"s_mov_b32 s57, s37\n"
"s_mov_b32 s58, 1024\n"
"s_mov_b32 s59, 0x20000\n"
"s_mov_b32 s60, s38\n"
"s_mov_b32 s61, s39\n"
"s_mov_b32 s62, 2097152\n"
"s_mov_b32 s63, 0x20000\n"
"v_lshlrev_b32 v56, 3, v3\n"
"v_lshl_add_u32 v24, v2, 10, v56\n"
"s_xor_b32 s16, s12, 1\n"
"s_add_u32 s16, s16, s50\n"
"s_lshl_b32 s16, s16, 2\n"
"s_add_u32 s16, s16, s4\n"
"s_mul_i32 s16, s16, 3\n"
"s_add_u32 s16, s16, 0\n"
"s_lshl_b32 s16, s16, 11\n"
"s_add_u32 s16, s16, 37748736\n"
"s_mov_b32 s23, s16\n"
"s_xor_b32 s16, s12, 2\n"
"s_add_u32 s16, s16, s50\n"
"s_lshl_b32 s16, s16, 2\n"
"s_add_u32 s16, s16, s4\n"
"s_mul_i32 s16, s16, 3\n"
"s_add_u32 s16, s16, 1\n"
"s_lshl_b32 s16, s16, 11\n"
"s_add_u32 s16, s16, 37748736\n"
"s_mov_b32 s24, s16\n"
"s_xor_b32 s16, s12, 3\n"
"s_add_u32 s16, s16, s50\n"
"s_lshl_b32 s16, s16, 2\n"
"s_add_u32 s16, s16, s4\n"
"s_mul_i32 s16, s16, 3\n"
"s_add_u32 s16, s16, 2\n"
"s_lshl_b32 s16, s16, 11\n"
"s_add_u32 s16, s16, 37748736\n"
"s_mov_b32 s25, s16\n"
"s_waitcnt lgkmcnt(9)\n"
"v_mfma_f32_16x16x32_bf16 v[192:195], a[140:143], a[0:3], 0\n"
"s_waitcnt lgkmcnt(2)\n"
"ds_read_b64_tr_b16 a[156:157], v28 offset:32768\n"
"v_mfma_f32_16x16x32_bf16 v[208:211], a[140:143], a[4:7], 0\n"
"ds_read_b64_tr_b16 a[158:159], v28 offset:36864\n"
"v_mfma_f32_16x16x32_bf16 v[224:227], a[140:143], a[8:11], 0\n"
"ds_read_b128 a[16:19], v32 offset:40960\n"
"v_mfma_f32_16x16x32_bf16 v[240:243], a[140:143], a[12:15], 0\n"
"ds_read_b128 a[20:23], v33 offset:40960\n"
"v_mfma_f32_16x16x32_bf16 v[196:199], a[144:147], a[0:3], 0\n"
"ds_read_b128 a[24:27], v34 offset:40960\n"
"v_mfma_f32_16x16x32_bf16 v[212:215], a[144:147], a[4:7], 0\n"
"ds_read_b128 a[28:31], v35 offset:40960\n"
"v_mfma_f32_16x16x32_bf16 v[228:231], a[144:147], a[8:11], 0\n"
"ds_read_b64_tr_b16 a[160:161], v29 offset:32768\n"
"v_mfma_f32_16x16x32_bf16 v[244:247], a[144:147], a[12:15], 0\n"
"ds_read_b64_tr_b16 a[162:163], v29 offset:36864\n"
"v_mfma_f32_16x16x32_bf16 v[200:203], a[148:151], a[0:3], 0\n"
"ds_read_b64_tr_b16 a[164:165], v30 offset:32768\n"
"v_mfma_f32_16x16x32_bf16 v[216:219], a[148:151], a[4:7], 0\n"
"ds_read_b64_tr_b16 a[166:167], v30 offset:36864\n"
"v_mfma_f32_16x16x32_bf16 v[232:235], a[148:151], a[8:11], 0\n"
"ds_read_b64_tr_b16 a[168:169], v31 offset:32768\n"
"v_mfma_f32_16x16x32_bf16 v[248:251], a[148:151], a[12:15], 0\n"
"s_waitcnt lgkmcnt(11)\n"
"ds_read_b64_tr_b16 a[170:171], v31 offset:36864\n"
"v_mfma_f32_16x16x32_bf16 v[204:207], a[152:155], a[0:3], 0\n"
"v_mfma_f32_16x16x32_bf16 v[220:223], a[152:155], a[4:7], 0\n"
"v_mfma_f32_16x16x32_bf16 v[236:239], a[152:155], a[8:11], 0\n"
"v_mfma_f32_16x16x32_bf16 v[252:255], a[152:155], a[12:15], 0\n"
"s_waitcnt lgkmcnt(0)\n"
"s_barrier\n"
"v_mfma_f32_16x16x32_bf16 v[192:195], a[156:159], a[16:19], v[192:195]\n"
"ds_read_b64_tr_b16 a[140:141], v28 offset:49152\n"
"v_mfma_f32_16x16x32_bf16 v[208:211], a[156:159], a[20:23], v[208:211]\n"
"ds_read_b64_tr_b16 a[142:143], v28 offset:53248\n"
"v_mfma_f32_16x16x32_bf16 v[224:227], a[156:159], a[24:27], v[224:227]\n"
"ds_read_b128 a[0:3], v32 offset:57344\n"
"v_mfma_f32_16x16x32_bf16 v[240:243], a[156:159], a[28:31], v[240:243]\n"
"ds_read_b128 a[4:7], v33 offset:57344\n"
"v_mfma_f32_16x16x32_bf16 v[196:199], a[160:163], a[16:19], v[196:199]\n"
"ds_read_b128 a[8:11], v34 offset:57344\n"
"v_mfma_f32_16x16x32_bf16 v[212:215], a[160:163], a[20:23], v[212:215]\n"
"ds_read_b128 a[12:15], v35 offset:57344\n"
"v_mfma_f32_16x16x32_bf16 v[228:231], a[160:163], a[24:27], v[228:231]\n"
"ds_read_b64_tr_b16 a[144:145], v29 offset:49152\n"
"v_mfma_f32_16x16x32_bf16 v[244:247], a[160:163], a[28:31], v[244:247]\n"
"ds_read_b64_tr_b16 a[146:147], v29 offset:53248\n"
"v_mfma_f32_16x16x32_bf16 v[200:203], a[164:167], a[16:19], v[200:203]\n"
"ds_read_b64_tr_b16 a[148:149], v30 offset:49152\n"
"v_mfma_f32_16x16x32_bf16 v[216:219], a[164:167], a[20:23], v[216:219]\n"
"ds_read_b64_tr_b16 a[150:151], v30 offset:53248\n"
"v_mfma_f32_16x16x32_bf16 v[232:235], a[164:167], a[24:27], v[232:235]\n"
"ds_read_b64_tr_b16 a[152:153], v31 offset:49152\n"
"v_mfma_f32_16x16x32_bf16 v[248:251], a[164:167], a[28:31], v[248:251]\n"
"ds_read_b64_tr_b16 a[154:155], v31 offset:53248\n"
"v_mfma_f32_16x16x32_bf16 v[204:207], a[168:171], a[16:19], v[204:207]\n"
"v_mfma_f32_16x16x32_bf16 v[220:223], a[168:171], a[20:23], v[220:223]\n"
"v_mfma_f32_16x16x32_bf16 v[236:239], a[168:171], a[24:27], v[236:239]\n"
"v_mfma_f32_16x16x32_bf16 v[252:255], a[168:171], a[28:31], v[252:255]\n"
"s_waitcnt lgkmcnt(2)\n"
"v_mfma_f32_16x16x32_bf16 v[192:195], a[140:143], a[0:3], v[192:195]\n"
"ds_read_b64_tr_b16 a[156:157], v36 offset:16384\n"
"v_mfma_f32_16x16x32_bf16 v[208:211], a[140:143], a[4:7], v[208:211]\n"
"ds_read_b64_tr_b16 a[158:159], v36 offset:20480\n"
"v_mfma_f32_16x16x32_bf16 v[224:227], a[140:143], a[8:11], v[224:227]\n"
"ds_read_b128 a[16:19], v40 offset:24576\n"
"v_mfma_f32_16x16x32_bf16 v[240:243], a[140:143], a[12:15], v[240:243]\n"
"ds_read_b128 a[20:23], v41 offset:24576\n"
"v_mfma_f32_16x16x32_bf16 v[196:199], a[144:147], a[0:3], v[196:199]\n"
"ds_read_b128 a[24:27], v42 offset:24576\n"
"v_mfma_f32_16x16x32_bf16 v[212:215], a[144:147], a[4:7], v[212:215]\n"
"ds_read_b128 a[28:31], v43 offset:24576\n"
"v_mfma_f32_16x16x32_bf16 v[228:231], a[144:147], a[8:11], v[228:231]\n"
"ds_read_b64_tr_b16 a[160:161], v37 offset:16384\n"
"v_mfma_f32_16x16x32_bf16 v[244:247], a[144:147], a[12:15], v[244:247]\n"
"ds_read_b64_tr_b16 a[162:163], v37 offset:20480\n"
"v_mfma_f32_16x16x32_bf16 v[200:203], a[148:151], a[0:3], v[200:203]\n"
"ds_read_b64_tr_b16 a[164:165], v38 offset:16384\n"
"v_mfma_f32_16x16x32_bf16 v[216:219], a[148:151], a[4:7], v[216:219]\n"
"ds_read_b64_tr_b16 a[166:167], v38 offset:20480\n"
"v_mfma_f32_16x16x32_bf16 v[232:235], a[148:151], a[8:11], v[232:235]\n"
"ds_read_b64_tr_b16 a[168:169], v39 offset:16384\n"
"v_mfma_f32_16x16x32_bf16 v[248:251], a[148:151], a[12:15], v[248:251]\n"
"s_waitcnt lgkmcnt(11)\n"
"ds_read_b64_tr_b16 a[170:171], v39 offset:20480\n"
"v_mfma_f32_16x16x32_bf16 v[204:207], a[152:155], a[0:3], v[204:207]\n"
"v_mfma_f32_16x16x32_bf16 v[220:223], a[152:155], a[4:7], v[220:223]\n"
"v_mfma_f32_16x16x32_bf16 v[236:239], a[152:155], a[8:11], v[236:239]\n"
"v_mfma_f32_16x16x32_bf16 v[252:255], a[152:155], a[12:15], v[252:255]\n"
"s_waitcnt vmcnt(35) lgkmcnt(0)\n"
"s_barrier\n"
"v_mfma_f32_16x16x32_bf16 v[192:195], a[156:159], a[16:19], v[192:195]\n"
"ds_read_b64_tr_b16 a[140:141], v28 offset:0\n"
"v_mfma_f32_16x16x32_bf16 v[208:211], a[156:159], a[20:23], v[208:211]\n"
"ds_read_b64_tr_b16 a[142:143], v28 offset:4096\n"
"v_mfma_f32_16x16x32_bf16 v[224:227], a[156:159], a[24:27], v[224:227]\n"
"ds_read_b128 a[0:3], v32 offset:8192\n"
"v_mfma_f32_16x16x32_bf16 v[240:243], a[156:159], a[28:31], v[240:243]\n"
"ds_read_b128 a[4:7], v33 offset:8192\n"
"v_mfma_f32_16x16x32_bf16 v[196:199], a[160:163], a[16:19], v[196:199]\n"
"ds_read_b128 a[8:11], v34 offset:8192\n"
"v_mfma_f32_16x16x32_bf16 v[212:215], a[160:163], a[20:23], v[212:215]\n"
"ds_read_b128 a[12:15], v35 offset:8192\n"
"v_mfma_f32_16x16x32_bf16 v[228:231], a[160:163], a[24:27], v[228:231]\n"
"ds_read_b64_tr_b16 a[144:145], v29 offset:0\n"
"v_mfma_f32_16x16x32_bf16 v[244:247], a[160:163], a[28:31], v[244:247]\n"
"ds_read_b64_tr_b16 a[146:147], v29 offset:4096\n"
"v_mfma_f32_16x16x32_bf16 v[200:203], a[164:167], a[16:19], v[200:203]\n"
"ds_read_b64_tr_b16 a[148:149], v30 offset:0\n"
"v_mfma_f32_16x16x32_bf16 v[216:219], a[164:167], a[20:23], v[216:219]\n"
"ds_read_b64_tr_b16 a[150:151], v30 offset:4096\n"
"v_mfma_f32_16x16x32_bf16 v[232:235], a[164:167], a[24:27], v[232:235]\n"
"ds_read_b64_tr_b16 a[152:153], v31 offset:0\n"
"v_mfma_f32_16x16x32_bf16 v[248:251], a[164:167], a[28:31], v[248:251]\n"
"ds_read_b64_tr_b16 a[154:155], v31 offset:4096\n"
"v_mfma_f32_16x16x32_bf16 v[204:207], a[168:171], a[16:19], v[204:207]\n"
"v_mfma_f32_16x16x32_bf16 v[220:223], a[168:171], a[20:23], v[220:223]\n"
"s_add_u32 m0, s84, 81920\n"
"v_mfma_f32_16x16x32_bf16 v[236:239], a[168:171], a[24:27], v[236:239]\n"
"buffer_load_dwordx4 v12, s[64:67], s48 offen sc1 lds\n"
"v_mfma_f32_16x16x32_bf16 v[252:255], a[168:171], a[28:31], v[252:255]\n"
"s_waitcnt lgkmcnt(2)\n"
"buffer_load_dwordx4 v13, s[64:67], s48 offen offset:1024 sc1 lds\n"
"s_add_u32 s48, s48, 1024\n"
"v_mfma_f32_16x16x32_bf16 v[192:195], a[140:143], a[0:3], v[192:195]\n"
"ds_read_b64_tr_b16 a[156:157], v36 offset:0\n"
"v_mfma_f32_16x16x32_bf16 v[208:211], a[140:143], a[4:7], v[208:211]\n"
"ds_read_b64_tr_b16 a[158:159], v36 offset:4096\n"
"v_mfma_f32_16x16x32_bf16 v[224:227], a[140:143], a[8:11], v[224:227]\n"
"ds_read_b128 a[16:19], v40 offset:8192\n"
"v_mfma_f32_16x16x32_bf16 v[240:243], a[140:143], a[12:15], v[240:243]\n"
"ds_read_b128 a[20:23], v41 offset:8192\n"
"v_mfma_f32_16x16x32_bf16 v[196:199], a[144:147], a[0:3], v[196:199]\n"
"ds_read_b128 a[24:27], v42 offset:8192\n"
"v_mfma_f32_16x16x32_bf16 v[212:215], a[144:147], a[4:7], v[212:215]\n"
"ds_read_b128 a[28:31], v43 offset:8192\n"
"v_mfma_f32_16x16x32_bf16 v[228:231], a[144:147], a[8:11], v[228:231]\n"
"ds_read_b64_tr_b16 a[160:161], v37 offset:0\n"
"v_mfma_f32_16x16x32_bf16 v[244:247], a[144:147], a[12:15], v[244:247]\n"
"ds_read_b64_tr_b16 a[162:163], v37 offset:4096\n"
"v_mfma_f32_16x16x32_bf16 v[200:203], a[148:151], a[0:3], v[200:203]\n"
"ds_read_b64_tr_b16 a[164:165], v38 offset:0\n"
"v_mfma_f32_16x16x32_bf16 v[216:219], a[148:151], a[4:7], v[216:219]\n"
"ds_read_b64_tr_b16 a[166:167], v38 offset:4096\n"
"v_mfma_f32_16x16x32_bf16 v[232:235], a[148:151], a[8:11], v[232:235]\n"
"ds_read_b64_tr_b16 a[168:169], v39 offset:0\n"
"v_mfma_f32_16x16x32_bf16 v[248:251], a[148:151], a[12:15], v[248:251]\n"
"s_waitcnt lgkmcnt(11)\n"
"ds_read_b64_tr_b16 a[170:171], v39 offset:4096\n"
"v_mfma_f32_16x16x32_bf16 v[204:207], a[152:155], a[0:3], v[204:207]\n"
"v_mfma_f32_16x16x32_bf16 v[220:223], a[152:155], a[4:7], v[220:223]\n"
"v_mfma_f32_16x16x32_bf16 v[236:239], a[152:155], a[8:11], v[236:239]\n"
"v_mfma_f32_16x16x32_bf16 v[252:255], a[152:155], a[12:15], v[252:255]\n"
"s_waitcnt vmcnt(26) lgkmcnt(0)\n"
"s_barrier\n"
"v_mfma_f32_16x16x32_bf16 v[192:195], a[156:159], a[16:19], v[192:195]\n"
"ds_read_b64_tr_b16 a[140:141], v16 offset:16384\n"
"v_mfma_f32_16x16x32_bf16 v[208:211], a[156:159], a[20:23], v[208:211]\n"
"ds_read_b64_tr_b16 a[142:143], v16 offset:20480\n"
"v_mfma_f32_16x16x32_bf16 v[224:227], a[156:159], a[24:27], v[224:227]\n"
"ds_read_b128 a[0:3], v20 offset:24576\n"
"v_mfma_f32_16x16x32_bf16 v[240:243], a[156:159], a[28:31], v[240:243]\n"
"ds_read_b128 a[4:7], v21 offset:24576\n"
"v_mfma_f32_16x16x32_bf16 v[196:199], a[160:163], a[16:19], v[196:199]\n"
"ds_read_b128 a[8:11], v22 offset:24576\n"
"v_mfma_f32_16x16x32_bf16 v[212:215], a[160:163], a[20:23], v[212:215]\n"
"ds_read_b128 a[12:15], v23 offset:24576\n"
"v_mfma_f32_16x16x32_bf16 v[228:231], a[160:163], a[24:27], v[228:231]\n"
"ds_read_b64_tr_b16 a[144:145], v17 offset:16384\n"
"v_mfma_f32_16x16x32_bf16 v[244:247], a[160:163], a[28:31], v[244:247]\n"
"ds_read_b64_tr_b16 a[146:147], v17 offset:20480\n"
"v_mfma_f32_16x16x32_bf16 v[200:203], a[164:167], a[16:19], v[200:203]\n"
"ds_read_b64_tr_b16 a[148:149], v18 offset:16384\n"
"v_mfma_f32_16x16x32_bf16 v[216:219], a[164:167], a[20:23], v[216:219]\n"
"ds_read_b64_tr_b16 a[150:151], v18 offset:20480\n"
"v_mfma_f32_16x16x32_bf16 v[232:235], a[164:167], a[24:27], v[232:235]\n"
"ds_read_b64_tr_b16 a[152:153], v19 offset:16384\n"
"v_mfma_f32_16x16x32_bf16 v[248:251], a[164:167], a[28:31], v[248:251]\n"
"ds_read_b64_tr_b16 a[154:155], v19 offset:20480\n"
"v_mfma_f32_16x16x32_bf16 v[204:207], a[168:171], a[16:19], v[204:207]\n"
"v_mfma_f32_16x16x32_bf16 v[220:223], a[168:171], a[20:23], v[220:223]\n"
"s_add_u32 m0, s84, 90112\n"
"v_mfma_f32_16x16x32_bf16 v[236:239], a[168:171], a[24:27], v[236:239]\n"
"buffer_load_dwordx4 v10, s[72:75], s85 offen lds\n"
"v_mfma_f32_16x16x32_bf16 v[252:255], a[168:171], a[28:31], v[252:255]\n"
"s_waitcnt lgkmcnt(2)\n"
"buffer_load_dwordx4 v11, s[72:75], s85 offen offset:1024 lds\n"
"s_add_u32 s85, s85, 32768\n"
"v_mfma_f32_16x16x32_bf16 v[192:195], a[140:143], a[0:3], v[192:195]\n"
"ds_read_b64_tr_b16 a[156:157], v16 offset:0\n"
"v_mfma_f32_16x16x32_bf16 v[208:211], a[140:143], a[4:7], v[208:211]\n"
"ds_read_b64_tr_b16 a[158:159], v16 offset:4096\n"
"v_mfma_f32_16x16x32_bf16 v[224:227], a[140:143], a[8:11], v[224:227]\n"
"ds_read_b128 a[16:19], v20 offset:8192\n"
"v_mfma_f32_16x16x32_bf16 v[240:243], a[140:143], a[12:15], v[240:243]\n"
"ds_read_b128 a[20:23], v21 offset:8192\n"
"v_mfma_f32_16x16x32_bf16 v[196:199], a[144:147], a[0:3], v[196:199]\n"
"ds_read_b128 a[24:27], v22 offset:8192\n"
"v_mfma_f32_16x16x32_bf16 v[212:215], a[144:147], a[4:7], v[212:215]\n"
"ds_read_b128 a[28:31], v23 offset:8192\n"
"v_mfma_f32_16x16x32_bf16 v[228:231], a[144:147], a[8:11], v[228:231]\n"
"ds_read_b64_tr_b16 a[160:161], v17 offset:0\n"
"v_mfma_f32_16x16x32_bf16 v[244:247], a[144:147], a[12:15], v[244:247]\n"
"ds_read_b64_tr_b16 a[162:163], v17 offset:4096\n"
"v_mfma_f32_16x16x32_bf16 v[200:203], a[148:151], a[0:3], v[200:203]\n"
"ds_read_b64_tr_b16 a[164:165], v18 offset:0\n"
"v_mfma_f32_16x16x32_bf16 v[216:219], a[148:151], a[4:7], v[216:219]\n"
"ds_read_b64_tr_b16 a[166:167], v18 offset:4096\n"
"v_mfma_f32_16x16x32_bf16 v[232:235], a[148:151], a[8:11], v[232:235]\n"
"ds_read_b64_tr_b16 a[168:169], v19 offset:0\n"
"v_mfma_f32_16x16x32_bf16 v[248:251], a[148:151], a[12:15], v[248:251]\n"
"s_waitcnt lgkmcnt(11)\n"
"ds_read_b64_tr_b16 a[170:171], v19 offset:4096\n"
"v_mfma_f32_16x16x32_bf16 v[204:207], a[152:155], a[0:3], v[204:207]\n"
"v_mfma_f32_16x16x32_bf16 v[220:223], a[152:155], a[4:7], v[220:223]\n"
"v_mfma_f32_16x16x32_bf16 v[236:239], a[152:155], a[8:11], v[236:239]\n"
"v_mfma_f32_16x16x32_bf16 v[252:255], a[152:155], a[12:15], v[252:255]\n"
"s_waitcnt vmcnt(4) lgkmcnt(0)\n"
"s_barrier\n"
"s_mov_b64 exec, 1\n"
"buffer_store_dword v7, v6, s[64:67], s83 offen sc1\n"
"s_mov_b64 exec, -1\n"
"v_mfma_f32_16x16x32_bf16 v[192:195], a[156:159], a[16:19], v[192:195]\n"
"ds_read_b64_tr_b16 a[140:141], v16 offset:32768\n"
"v_mfma_f32_16x16x32_bf16 v[208:211], a[156:159], a[20:23], v[208:211]\n"
"ds_read_b64_tr_b16 a[142:143], v16 offset:36864\n"
"v_mfma_f32_16x16x32_bf16 v[224:227], a[156:159], a[24:27], v[224:227]\n"
"ds_read_b128 a[0:3], v20 offset:40960\n"
"v_mfma_f32_16x16x32_bf16 v[240:243], a[156:159], a[28:31], v[240:243]\n"
"ds_read_b128 a[4:7], v21 offset:40960\n"
"v_mfma_f32_16x16x32_bf16 v[196:199], a[160:163], a[16:19], v[196:199]\n"
"s_add_u32 m0, s84, 98304\n"
"ds_read_b128 a[8:11], v22 offset:40960\n"
"buffer_load_dwordx4 v10, s[76:79], s22 offen lds\n"
"v_mfma_f32_16x16x32_bf16 v[212:215], a[160:163], a[20:23], v[212:215]\n"
"ds_read_b128 a[12:15], v23 offset:40960\n"
"v_mfma_f32_16x16x32_bf16 v[228:231], a[160:163], a[24:27], v[228:231]\n"
"ds_read_b64_tr_b16 a[144:145], v17 offset:32768\n"
"buffer_load_dwordx4 v11, s[76:79], s22 offen offset:1024 lds\n"
"v_mfma_f32_16x16x32_bf16 v[244:247], a[160:163], a[28:31], v[244:247]\n"
"s_add_u32 s22, s22, 32768\n"
"ds_read_b64_tr_b16 a[146:147], v17 offset:36864\n"
"v_mfma_f32_16x16x32_bf16 v[200:203], a[164:167], a[16:19], v[200:203]\n"
"s_add_u32 m0, s84, 106496\n"
"ds_read_b64_tr_b16 a[148:149], v18 offset:32768\n"
"buffer_load_dwordx4 v10, s[76:79], s22 offen lds\n"
"v_mfma_f32_16x16x32_bf16 v[216:219], a[164:167], a[20:23], v[216:219]\n"
"ds_read_b64_tr_b16 a[150:151], v18 offset:36864\n"
"v_mfma_f32_16x16x32_bf16 v[232:235], a[164:167], a[24:27], v[232:235]\n"
"ds_read_b64_tr_b16 a[152:153], v19 offset:32768\n"
"buffer_load_dwordx4 v11, s[76:79], s22 offen offset:1024 lds\n"
"v_mfma_f32_16x16x32_bf16 v[248:251], a[164:167], a[28:31], v[248:251]\n"
"s_add_u32 s22, s22, 32768\n"
"ds_read_b64_tr_b16 a[154:155], v19 offset:36864\n"
"v_mfma_f32_16x16x32_bf16 v[204:207], a[168:171], a[16:19], v[204:207]\n"
"buffer_load_dwordx2 v[164:165], v24, s[44:47], s51 offen offset:0\n"
"v_mfma_f32_16x16x32_bf16 v[220:223], a[168:171], a[20:23], v[220:223]\n"
"buffer_load_dwordx2 v[166:167], v24, s[44:47], s51 offen offset:32\n"
"v_mfma_f32_16x16x32_bf16 v[236:239], a[168:171], a[24:27], v[236:239]\n"
"buffer_load_dwordx2 v[172:173], v24, s[44:47], s51 offen offset:64\n"
"v_mfma_f32_16x16x32_bf16 v[252:255], a[168:171], a[28:31], v[252:255]\n"
"s_waitcnt lgkmcnt(2)\n"
"buffer_load_dwordx2 v[174:175], v24, s[44:47], s51 offen offset:96\n"
"v_mfma_f32_16x16x32_bf16 v[192:195], a[140:143], a[0:3], v[192:195]\n"
"ds_read_b64_tr_b16 a[156:157], v16 offset:49152\n"
"v_mfma_f32_16x16x32_bf16 v[208:211], a[140:143], a[4:7], v[208:211]\n"
"ds_read_b64_tr_b16 a[158:159], v16 offset:53248\n"
"v_mfma_f32_16x16x32_bf16 v[224:227], a[140:143], a[8:11], v[224:227]\n"
"ds_read_b128 a[16:19], v20 offset:57344\n"
"v_mfma_f32_16x16x32_bf16 v[240:243], a[140:143], a[12:15], v[240:243]\n"
"ds_read_b128 a[20:23], v21 offset:57344\n"
"v_mfma_f32_16x16x32_bf16 v[196:199], a[144:147], a[0:3], v[196:199]\n"
"s_add_u32 m0, s84, 114688\n"
"ds_read_b128 a[24:27], v22 offset:57344\n"
"buffer_load_dwordx4 v10, s[76:79], s22 offen lds\n"
"v_mfma_f32_16x16x32_bf16 v[212:215], a[144:147], a[4:7], v[212:215]\n"
"ds_read_b128 a[28:31], v23 offset:57344\n"
"v_mfma_f32_16x16x32_bf16 v[228:231], a[144:147], a[8:11], v[228:231]\n"
"ds_read_b64_tr_b16 a[160:161], v17 offset:49152\n"
"buffer_load_dwordx4 v11, s[76:79], s22 offen offset:1024 lds\n"
"v_mfma_f32_16x16x32_bf16 v[244:247], a[144:147], a[12:15], v[244:247]\n"
"s_add_u32 s22, s22, 32768\n"
"ds_read_b64_tr_b16 a[162:163], v17 offset:53248\n"
"v_mfma_f32_16x16x32_bf16 v[200:203], a[148:151], a[0:3], v[200:203]\n"
"s_add_u32 m0, s84, 122880\n"
"ds_read_b64_tr_b16 a[164:165], v18 offset:49152\n"
"buffer_load_dwordx4 v10, s[76:79], s22 offen lds\n"
"v_mfma_f32_16x16x32_bf16 v[216:219], a[148:151], a[4:7], v[216:219]\n"
"ds_read_b64_tr_b16 a[166:167], v18 offset:53248\n"
"v_mfma_f32_16x16x32_bf16 v[232:235], a[148:151], a[8:11], v[232:235]\n"
"ds_read_b64_tr_b16 a[168:169], v19 offset:49152\n"
"buffer_load_dwordx4 v11, s[76:79], s22 offen offset:1024 lds\n"
"v_mfma_f32_16x16x32_bf16 v[248:251], a[148:151], a[12:15], v[248:251]\n"
"s_add_u32 s22, s22, 32768\n"
"s_waitcnt lgkmcnt(11)\n"
"ds_read_b64_tr_b16 a[170:171], v19 offset:53248\n"
"v_mfma_f32_16x16x32_bf16 v[204:207], a[152:155], a[0:3], v[204:207]\n"
"buffer_load_dwordx2 v[180:181], v56, s[56:59], s52 offen offset:0\n"
"v_mfma_f32_16x16x32_bf16 v[220:223], a[152:155], a[4:7], v[220:223]\n"
"buffer_load_dwordx2 v[182:183], v56, s[56:59], s52 offen offset:32\n"
"v_mfma_f32_16x16x32_bf16 v[236:239], a[152:155], a[8:11], v[236:239]\n"
"buffer_load_dwordx2 v[188:189], v56, s[56:59], s52 offen offset:64\n"
"v_mfma_f32_16x16x32_bf16 v[252:255], a[152:155], a[12:15], v[252:255]\n"
"buffer_load_dwordx2 v[190:191], v56, s[56:59], s52 offen offset:96\n"
"s_waitcnt vmcnt(17) lgkmcnt(0)\n"
"s_barrier\n"
"v_mfma_f32_16x16x32_bf16 v[192:195], a[156:159], a[16:19], v[192:195]\n"
"ds_read_b64_tr_b16 a[140:141], v28 offset:24576\n"
"v_mfma_f32_16x16x32_bf16 v[208:211], a[156:159], a[20:23], v[208:211]\n"
"ds_read_b64_tr_b16 a[142:143], v28 offset:28672\n"
"v_mfma_f32_16x16x32_bf16 v[224:227], a[156:159], a[24:27], v[224:227]\n"
"ds_read_b128 a[0:3], v32 offset:16384\n"
"v_mfma_f32_16x16x32_bf16 v[240:243], a[156:159], a[28:31], v[240:243]\n"
"ds_read_b128 a[4:7], v33 offset:16384\n"
"v_mfma_f32_16x16x32_bf16 v[196:199], a[160:163], a[16:19], v[196:199]\n"
"ds_read_b128 a[8:11], v34 offset:16384\n"
"v_mfma_f32_16x16x32_bf16 v[212:215], a[160:163], a[20:23], v[212:215]\n"
"ds_read_b128 a[12:15], v35 offset:16384\n"
"v_mfma_f32_16x16x32_bf16 v[228:231], a[160:163], a[24:27], v[228:231]\n"
"ds_read_b64_tr_b16 a[144:145], v29 offset:24576\n"
"v_mfma_f32_16x16x32_bf16 v[244:247], a[160:163], a[28:31], v[244:247]\n"
"ds_read_b64_tr_b16 a[146:147], v29 offset:28672\n"
"v_mfma_f32_16x16x32_bf16 v[200:203], a[164:167], a[16:19], v[200:203]\n"
"ds_read_b64_tr_b16 a[148:149], v30 offset:24576\n"
"v_mfma_f32_16x16x32_bf16 v[216:219], a[164:167], a[20:23], v[216:219]\n"
"ds_read_b64_tr_b16 a[150:151], v30 offset:28672\n"
"v_mfma_f32_16x16x32_bf16 v[232:235], a[164:167], a[24:27], v[232:235]\n"
"ds_read_b64_tr_b16 a[152:153], v31 offset:24576\n"
"v_mfma_f32_16x16x32_bf16 v[248:251], a[164:167], a[28:31], v[248:251]\n"
"s_add_u32 m0, s84, 147456\n"
"ds_read_b64_tr_b16 a[154:155], v31 offset:28672\n"
"v_mfma_f32_16x16x32_bf16 v[204:207], a[168:171], a[16:19], v[204:207]\n"
"buffer_load_dwordx4 v10, s[76:79], s86 offen lds\n"
"v_mfma_f32_16x16x32_bf16 v[220:223], a[168:171], a[20:23], v[220:223]\n"
"buffer_load_dwordx4 v11, s[76:79], s86 offen offset:1024 lds\n"
"s_add_u32 m0, s84, 155648\n"
"v_mfma_f32_16x16x32_bf16 v[236:239], a[168:171], a[24:27], v[236:239]\n"
"s_add_u32 s86, s86, 32768\n"
"buffer_load_dwordx4 v10, s[76:79], s86 offen lds\n"
"v_mfma_f32_16x16x32_bf16 v[252:255], a[168:171], a[28:31], v[252:255]\n"
"buffer_load_dwordx4 v11, s[76:79], s86 offen offset:1024 lds\n"
"s_add_u32 m0, s84, 65536\n"
"s_add_u32 s86, s86, 32768\n"
"buffer_load_dwordx4 v10, s[76:79], s86 offen lds\n"
"buffer_load_dwordx4 v11, s[76:79], s86 offen offset:1024 lds\n"
"s_add_u32 m0, s84, 73728\n"
"s_add_u32 s86, s86, 32768\n"
"buffer_load_dwordx4 v10, s[76:79], s86 offen lds\n"
"buffer_load_dwordx4 v11, s[76:79], s86 offen offset:1024 lds\n"
"s_add_u32 s86, s86, 32768\n"
"s_cmp_eq_u32 s86, s30\n"
"s_cselect_b32 s42, 131072, 0\n"
"s_add_u32 s86, s86, s42\n"
"s_waitcnt lgkmcnt(0)\n"
"s_cmp_eq_u32 s12, 3\n"
"s_cbranch_scc1 LSK10_%=\n"
"v_mfma_f32_16x16x32_bf16 v[192:195], a[140:143], a[0:3], v[192:195]\n"
"v_mfma_f32_16x16x32_bf16 v[208:211], a[140:143], a[4:7], v[208:211]\n"
"v_mfma_f32_16x16x32_bf16 v[224:227], a[140:143], a[8:11], v[224:227]\n"
"v_mfma_f32_16x16x32_bf16 v[240:243], a[140:143], a[12:15], v[240:243]\n"
"v_mfma_f32_16x16x32_bf16 v[196:199], a[144:147], a[0:3], v[196:199]\n"
"v_mfma_f32_16x16x32_bf16 v[212:215], a[144:147], a[4:7], v[212:215]\n"
"v_mfma_f32_16x16x32_bf16 v[228:231], a[144:147], a[8:11], v[228:231]\n"
"v_mfma_f32_16x16x32_bf16 v[244:247], a[144:147], a[12:15], v[244:247]\n"
"v_mfma_f32_16x16x32_bf16 v[200:203], a[148:151], a[0:3], v[200:203]\n"
"v_mfma_f32_16x16x32_bf16 v[216:219], a[148:151], a[4:7], v[216:219]\n"
"v_mfma_f32_16x16x32_bf16 v[232:235], a[148:151], a[8:11], v[232:235]\n"
"v_mfma_f32_16x16x32_bf16 v[248:251], a[148:151], a[12:15], v[248:251]\n"
"v_mfma_f32_16x16x32_bf16 v[204:207], a[152:155], a[0:3], v[204:207]\n"
"v_mfma_f32_16x16x32_bf16 v[220:223], a[152:155], a[4:7], v[220:223]\n"
"v_mfma_f32_16x16x32_bf16 v[236:239], a[152:155], a[8:11], v[236:239]\n"
"v_mfma_f32_16x16x32_bf16 v[252:255], a[152:155], a[12:15], v[252:255]\n"
"LSK10_%=:\n"
"buffer_load_dword v26, v25, s[64:67], 0 offen sc1\n"
"s_add_u32 s16, s50, s12\n"
"s_lshl_b32 s16, s16, 2\n"
"s_add_u32 s16, s16, s4\n"
"s_lshl_b32 s16, s16, 2\n"
"v_mov_b32 v6, s16\n"
"s_mov_b32 s83, 24215552\n"
"s_nop 1\n"
"s_lshr_b32 s16, s4, 1\n"
"s_and_b32 s17, s10, 1\n"
"s_cmp_lg_u32 s16, s17\n"
"s_cbranch_scc1 LNOWN_%=\n"
"s_and_b32 s17, s4, 1\n"
"s_lshl_b32 s17, s17, 2\n"
"s_xor_b32 s16, s82, 0\n"
"s_add_u32 s16, s16, s17\n"
"s_lshl_b32 s16, s16, 10\n"
"s_add_u32 s42, s16, 0\n"
"v_add_u32 v46, s42, v4\n"
"s_add_u32 s42, s16, 65536\n"
"v_add_u32 v47, s42, v4\n"
"s_add_u32 s42, s16, 131072\n"
"v_add_u32 v48, s42, v4\n"
"ds_write_b128 v48, v[64:67] offset:0\n"
"ds_write_b128 v48, v[72:75] offset:8192\n"
"ds_write_b128 v46, v[80:83] offset:16384\n"
"ds_write_b128 v46, v[88:91] offset:24576\n"
"s_xor_b32 s16, s82, 1\n"
"s_add_u32 s16, s16, s17\n"
"s_lshl_b32 s16, s16, 10\n"
"s_add_u32 s42, s16, 0\n"
"v_add_u32 v46, s42, v4\n"
"s_add_u32 s42, s16, 65536\n"
"v_add_u32 v47, s42, v4\n"
"s_add_u32 s42, s16, 131072\n"
"v_add_u32 v48, s42, v4\n"
"ds_write_b128 v48, v[96:99] offset:0\n"
"ds_write_b128 v48, v[104:107] offset:8192\n"
"ds_write_b128 v46, v[112:115] offset:16384\n"
"ds_write_b128 v46, v[120:123] offset:24576\n"
"s_xor_b32 s16, s82, 2\n"
"s_add_u32 s16, s16, s17\n"
"s_lshl_b32 s16, s16, 10\n"
"s_add_u32 s42, s16, 0\n"
"v_add_u32 v46, s42, v4\n"
"s_add_u32 s42, s16, 65536\n"
"v_add_u32 v47, s42, v4\n"
"s_add_u32 s42, s16, 131072\n"
"v_add_u32 v48, s42, v4\n"
"ds_write_b128 v48, v[128:131] offset:0\n"
"ds_write_b128 v48, v[136:139] offset:8192\n"
"ds_write_b128 v46, v[144:147] offset:16384\n"
"ds_write_b128 v46, v[152:155] offset:24576\n"
"s_xor_b32 s16, s82, 3\n"
"s_add_u32 s16, s16, s17\n"
"s_lshl_b32 s16, s16, 10\n"
"s_add_u32 s42, s16, 0\n"
"v_add_u32 v46, s42, v4\n"
"s_add_u32 s42, s16, 65536\n"
"v_add_u32 v47, s42, v4\n"
"s_add_u32 s42, s16, 131072\n"
"v_add_u32 v48, s42, v4\n"
"ds_write_b128 v48, v[160:163] offset:0\n"
"ds_write_b128 v48, v[168:171] offset:8192\n"
"ds_write_b128 v46, v[176:179] offset:16384\n"
"ds_write_b128 v46, v[184:187] offset:24576\n"
"LNOWN_%=:\n"
"s_waitcnt vmcnt(23) lgkmcnt(0)\n"
"s_barrier\n"
"ds_read_b64_tr_b16 a[156:157], v28 offset:32768\n"
"ds_read_b64_tr_b16 a[158:159], v28 offset:36864\n"
"ds_read_b128 a[16:19], v40 offset:0\n"
"ds_read_b128 a[20:23], v41 offset:0\n"
"ds_read_b128 a[24:27], v42 offset:0\n"
"ds_read_b128 a[28:31], v43 offset:0\n"
"ds_read_b64_tr_b16 a[160:161], v29 offset:32768\n"
"ds_read_b64_tr_b16 a[162:163], v29 offset:36864\n"
"ds_read_b64_tr_b16 a[164:165], v30 offset:32768\n"
"ds_read_b64_tr_b16 a[166:167], v30 offset:36864\n"
"ds_read_b64_tr_b16 a[168:169], v31 offset:32768\n"
"ds_read_b64_tr_b16 a[170:171], v31 offset:36864\n"
"s_waitcnt vmcnt(0)\n"
"v_cmp_ne_u32 s[18:19], s8, v26\n"
"s_nop 0\n"
"s_cmp_lg_u64 s[18:19], 0\n"
"s_cbranch_scc1 LOFR_%=\n"
"LOFK_%=:\n"
"s_add_u32 m0, s84, 0\n"
"s_nop 0\n"
"buffer_load_dwordx4 v14, s[68:71], s49 offen sc1 lds\n"
"buffer_load_dwordx4 v15, s[68:71], s49 offen offset:1024 sc1 lds\n"
"s_add_u32 s49, s49, 1024\n"
"s_add_u32 m0, s84, 8192\n"
"s_nop 0\n"
"buffer_load_dwordx4 v14, s[68:71], s49 offen sc1 lds\n"
"buffer_load_dwordx4 v15, s[68:71], s49 offen offset:1024 sc1 lds\n"
"s_add_u32 s49, s49, 1024\n"
"v_and_b32 v54, 3, v1\n"
"v_max_u32 v54, 1, v54\n"
"v_xor_b32 v54, s12, v54\n"
"v_add_u32 v54, s50, v54\n"
"v_lshl_add_u32 v54, v54, 2, s4\n"
"v_lshlrev_b32 v25, 2, v54\n"
"s_waitcnt lgkmcnt(0)\n"
"s_barrier\n"
"v_mfma_f32_16x16x32_bf16 v[64:67], a[156:159], a[16:19], 0\n"
"ds_read_b64_tr_b16 a[140:141], v28 offset:40960\n"
"v_mfma_f32_16x16x32_bf16 v[80:83], a[156:159], a[20:23], 0\n"
"ds_read_b64_tr_b16 a[142:143], v28 offset:45056\n"
"v_mfma_f32_16x16x32_bf16 v[96:99], a[156:159], a[24:27], 0\n"
"ds_read_b128 a[0:3], v40 offset:8192\n"
"v_mfma_f32_16x16x32_bf16 v[112:115], a[156:159], a[28:31], 0\n"
"ds_read_b128 a[4:7], v41 offset:8192\n"
"v_mfma_f32_16x16x32_bf16 v[68:71], a[160:163], a[16:19], 0\n"
"ds_read_b128 a[8:11], v42 offset:8192\n"
"v_mfma_f32_16x16x32_bf16 v[84:87], a[160:163], a[20:23], 0\n"
"ds_read_b128 a[12:15], v43 offset:8192\n"
"v_mfma_f32_16x16x32_bf16 v[100:103], a[160:163], a[24:27], 0\n"
"ds_read_b64_tr_b16 a[144:145], v29 offset:40960\n"
"v_mfma_f32_16x16x32_bf16 v[116:119], a[160:163], a[28:31], 0\n"
"ds_read_b64_tr_b16 a[146:147], v29 offset:45056\n"
"v_mfma_f32_16x16x32_bf16 v[72:75], a[164:167], a[16:19], 0\n"
"s_add_u32 m0, s84, 32768\n"
"ds_read_b64_tr_b16 a[148:149], v30 offset:40960\n"
"buffer_load_dwordx4 v14, s[68:71], s49 offen sc1 lds\n"
"v_mfma_f32_16x16x32_bf16 v[88:91], a[164:167], a[20:23], 0\n"
"ds_read_b64_tr_b16 a[150:151], v30 offset:45056\n"
"v_mfma_f32_16x16x32_bf16 v[104:107], a[164:167], a[24:27], 0\n"
"ds_read_b64_tr_b16 a[152:153], v31 offset:40960\n"
"buffer_load_dwordx4 v15, s[68:71], s49 offen offset:1024 sc1 lds\n"
"v_mfma_f32_16x16x32_bf16 v[120:123], a[164:167], a[28:31], 0\n"
"s_add_u32 m0, s84, 40960\n"
"s_add_u32 s49, s49, 1024\n"
"ds_read_b64_tr_b16 a[154:155], v31 offset:45056\n"
"v_mfma_f32_16x16x32_bf16 v[76:79], a[168:171], a[16:19], 0\n"
"buffer_load_dwordx4 v14, s[68:71], s49 offen sc1 lds\n"
"v_mfma_f32_16x16x32_bf16 v[92:95], a[168:171], a[20:23], 0\n"
"buffer_load_dwordx4 v15, s[68:71], s49 offen offset:1024 sc1 lds\n"
"s_add_u32 s49, s49, 1024\n"
"s_cmp_eq_u32 s49, s31\n"
"s_cselect_b32 s42, 4096, 0\n"
"v_mfma_f32_16x16x32_bf16 v[108:111], a[168:171], a[24:27], 0\n"
"s_add_u32 m0, s84, 49152\n"
"s_add_u32 s49, s49, s42\n"
"buffer_load_dwordx4 v10, s[76:79], s86 offen lds\n"
"v_mfma_f32_16x16x32_bf16 v[124:127], a[168:171], a[28:31], 0\n"
"s_waitcnt lgkmcnt(2)\n"
"buffer_load_dwordx4 v11, s[76:79], s86 offen offset:1024 lds\n"
"s_add_u32 s86, s86, 32768\n"
"v_mfma_f32_16x16x32_bf16 v[64:67], a[140:143], a[0:3], v[64:67]\n"
"ds_read_b64_tr_b16 a[156:157], v28 offset:49152\n"
"v_mfma_f32_16x16x32_bf16 v[80:83], a[140:143], a[4:7], v[80:83]\n"
"ds_read_b64_tr_b16 a[158:159], v28 offset:53248\n"
"v_mfma_f32_16x16x32_bf16 v[96:99], a[140:143], a[8:11], v[96:99]\n"
"ds_read_b128 a[16:19], v20 offset:16384\n"
"v_mfma_f32_16x16x32_bf16 v[112:115], a[140:143], a[12:15], v[112:115]\n"
"ds_read_b128 a[20:23], v21 offset:16384\n"
"v_mfma_f32_16x16x32_bf16 v[68:71], a[144:147], a[0:3], v[68:71]\n"
"ds_read_b128 a[24:27], v22 offset:16384\n"
"v_mfma_f32_16x16x32_bf16 v[84:87], a[144:147], a[4:7], v[84:87]\n"
"ds_read_b128 a[28:31], v23 offset:16384\n"
"v_mfma_f32_16x16x32_bf16 v[100:103], a[144:147], a[8:11], v[100:103]\n"
"ds_read_b64_tr_b16 a[160:161], v29 offset:49152\n"
"v_mfma_f32_16x16x32_bf16 v[116:119], a[144:147], a[12:15], v[116:119]\n"
"ds_read_b64_tr_b16 a[162:163], v29 offset:53248\n"
"v_mfma_f32_16x16x32_bf16 v[72:75], a[148:151], a[0:3], v[72:75]\n"
"s_add_u32 m0, s84, 57344\n"
"ds_read_b64_tr_b16 a[164:165], v30 offset:49152\n"
"buffer_load_dwordx4 v14, s[68:71], s49 offen sc1 lds\n"
"v_mfma_f32_16x16x32_bf16 v[88:91], a[148:151], a[4:7], v[88:91]\n"
"ds_read_b64_tr_b16 a[166:167], v30 offset:53248\n"
"v_mfma_f32_16x16x32_bf16 v[104:107], a[148:151], a[8:11], v[104:107]\n"
"ds_read_b64_tr_b16 a[168:169], v31 offset:49152\n"
"buffer_load_dwordx4 v15, s[68:71], s49 offen offset:1024 sc1 lds\n"
"v_mfma_f32_16x16x32_bf16 v[120:123], a[148:151], a[12:15], v[120:123]\n"
"s_add_u32 m0, s84, 90112\n"
"s_waitcnt lgkmcnt(11)\n"
"ds_read_b64_tr_b16 a[170:171], v31 offset:53248\n"
"v_mfma_f32_16x16x32_bf16 v[76:79], a[152:155], a[0:3], v[76:79]\n"
"s_add_u32 s49, s49, 1024\n"
"buffer_load_dwordx4 v14, s[68:71], s49 offen sc1 lds\n"
"v_mfma_f32_16x16x32_bf16 v[92:95], a[152:155], a[4:7], v[92:95]\n"
"buffer_load_dwordx4 v15, s[68:71], s49 offen offset:1024 sc1 lds\n"
"s_add_u32 m0, s84, 81920\n"
"v_mfma_f32_16x16x32_bf16 v[108:111], a[152:155], a[8:11], v[108:111]\n"
"s_add_u32 s49, s49, 1024\n"
"buffer_load_dwordx4 v10, s[76:79], s86 offen lds\n"
"v_mfma_f32_16x16x32_bf16 v[124:127], a[152:155], a[12:15], v[124:127]\n"
"buffer_load_dwordx4 v11, s[76:79], s86 offen offset:1024 lds\n"
"s_add_u32 s86, s86, 32768\n"
"s_waitcnt vmcnt(14)\n"
"s_barrier\n"
"s_waitcnt lgkmcnt(9)\n"
"v_mfma_f32_16x16x32_bf16 v[64:67], a[156:159], a[16:19], v[64:67]\n"
"s_waitcnt lgkmcnt(2)\n"
"ds_read_b64_tr_b16 a[140:141], v28 offset:57344\n"
"v_mfma_f32_16x16x32_bf16 v[80:83], a[156:159], a[20:23], v[80:83]\n"
"ds_read_b64_tr_b16 a[142:143], v28 offset:61440\n"
"v_mfma_f32_16x16x32_bf16 v[96:99], a[156:159], a[24:27], v[96:99]\n"
"ds_read_b128 a[0:3], v20 offset:24576\n"
"v_mfma_f32_16x16x32_bf16 v[112:115], a[156:159], a[28:31], v[112:115]\n"
"ds_read_b128 a[4:7], v21 offset:24576\n"
"v_mfma_f32_16x16x32_bf16 v[68:71], a[160:163], a[16:19], v[68:71]\n"
"ds_read_b128 a[8:11], v22 offset:24576\n"
"v_mfma_f32_16x16x32_bf16 v[84:87], a[160:163], a[20:23], v[84:87]\n"
"ds_read_b128 a[12:15], v23 offset:24576\n"
"v_mfma_f32_16x16x32_bf16 v[100:103], a[160:163], a[24:27], v[100:103]\n"
"ds_read_b64_tr_b16 a[144:145], v29 offset:57344\n"
"v_mfma_f32_16x16x32_bf16 v[116:119], a[160:163], a[28:31], v[116:119]\n"
"ds_read_b64_tr_b16 a[146:147], v29 offset:61440\n"
"v_mfma_f32_16x16x32_bf16 v[72:75], a[164:167], a[16:19], v[72:75]\n"
"s_add_u32 m0, s84, 98304\n"
"ds_read_b64_tr_b16 a[148:149], v30 offset:57344\n"
"buffer_load_dwordx4 v14, s[68:71], s49 offen sc1 lds\n"
"v_mfma_f32_16x16x32_bf16 v[88:91], a[164:167], a[20:23], v[88:91]\n"
"ds_read_b64_tr_b16 a[150:151], v30 offset:61440\n"
"v_mfma_f32_16x16x32_bf16 v[104:107], a[164:167], a[24:27], v[104:107]\n"
"ds_read_b64_tr_b16 a[152:153], v31 offset:57344\n"
"buffer_load_dwordx4 v15, s[68:71], s49 offen offset:1024 sc1 lds\n"
"v_mfma_f32_16x16x32_bf16 v[120:123], a[164:167], a[28:31], v[120:123]\n"
"s_add_u32 m0, s84, 131072\n"
"s_waitcnt lgkmcnt(5)\n"
"ds_read_b64_tr_b16 a[154:155], v31 offset:61440\n"
"v_mfma_f32_16x16x32_bf16 v[76:79], a[168:171], a[16:19], v[76:79]\n"
"s_add_u32 s49, s49, 1024\n"
"buffer_load_dwordx4 v14, s[68:71], s49 offen sc1 lds\n"
"v_mfma_f32_16x16x32_bf16 v[92:95], a[168:171], a[20:23], v[92:95]\n"
"buffer_load_dwordx4 v15, s[68:71], s49 offen offset:1024 sc1 lds\n"
"s_add_u32 s49, s49, 1024\n"
"s_cmp_eq_u32 s49, s31\n"
"s_cselect_b32 s42, 4096, 0\n"
"v_mfma_f32_16x16x32_bf16 v[108:111], a[168:171], a[24:27], v[108:111]\n"
"s_add_u32 m0, s84, 106496\n"
"s_add_u32 s49, s49, s42\n"
"buffer_load_dwordx4 v10, s[76:79], s86 offen lds\n"
"v_mfma_f32_16x16x32_bf16 v[124:127], a[168:171], a[28:31], v[124:127]\n"
"buffer_load_dwordx4 v11, s[76:79], s86 offen offset:1024 lds\n"
"s_add_u32 s86, s86, 32768\n"
"v_mfma_f32_16x16x32_bf16 v[64:67], a[140:143], a[0:3], v[64:67]\n"
"ds_read_b64_tr_b16 a[156:157], v36 offset:16384\n"
"v_mfma_f32_16x16x32_bf16 v[80:83], a[140:143], a[4:7], v[80:83]\n"
"ds_read_b64_tr_b16 a[158:159], v36 offset:20480\n"
"v_mfma_f32_16x16x32_bf16 v[96:99], a[140:143], a[8:11], v[96:99]\n"
"ds_read_b128 a[16:19], v20 offset:0\n"
"v_mfma_f32_16x16x32_bf16 v[112:115], a[140:143], a[12:15], v[112:115]\n"
"s_waitcnt lgkmcnt(3)\n"
"ds_read_b128 a[20:23], v21 offset:0\n"
"v_mfma_f32_16x16x32_bf16 v[68:71], a[144:147], a[0:3], v[68:71]\n"
"ds_read_b128 a[24:27], v22 offset:0\n"
"v_mfma_f32_16x16x32_bf16 v[84:87], a[144:147], a[4:7], v[84:87]\n"
"ds_read_b128 a[28:31], v23 offset:0\n"
"v_mfma_f32_16x16x32_bf16 v[100:103], a[144:147], a[8:11], v[100:103]\n"
"ds_read_b64_tr_b16 a[160:161], v37 offset:16384\n"
"v_mfma_f32_16x16x32_bf16 v[116:119], a[144:147], a[12:15], v[116:119]\n"
"ds_read_b64_tr_b16 a[162:163], v37 offset:20480\n"
"v_mfma_f32_16x16x32_bf16 v[72:75], a[148:151], a[0:3], v[72:75]\n"
"ds_read_b64_tr_b16 a[164:165], v38 offset:16384\n"
"v_mfma_f32_16x16x32_bf16 v[88:91], a[148:151], a[4:7], v[88:91]\n"
"ds_read_b64_tr_b16 a[166:167], v38 offset:20480\n"
"v_mfma_f32_16x16x32_bf16 v[104:107], a[148:151], a[8:11], v[104:107]\n"
"ds_read_b64_tr_b16 a[168:169], v39 offset:16384\n"
"v_mfma_f32_16x16x32_bf16 v[120:123], a[148:151], a[12:15], v[120:123]\n"
"ds_read_b64_tr_b16 a[170:171], v39 offset:20480\n"
"v_mfma_f32_16x16x32_bf16 v[76:79], a[152:155], a[0:3], v[76:79]\n"
"v_mfma_f32_16x16x32_bf16 v[92:95], a[152:155], a[4:7], v[92:95]\n"
"s_add_u32 m0, s84, 139264\n"
"v_mfma_f32_16x16x32_bf16 v[108:111], a[152:155], a[8:11], v[108:111]\n"
"buffer_load_dwordx4 v14, s[68:71], s49 offen sc1 lds\n"
"v_mfma_f32_16x16x32_bf16 v[124:127], a[152:155], a[12:15], v[124:127]\n"
"buffer_load_dwordx4 v15, s[68:71], s49 offen offset:1024 sc1 lds\n"
"s_add_u32 s49, s49, 1024\n"
"s_waitcnt vmcnt(18)\n"
"s_barrier\n"
"s_waitcnt lgkmcnt(9)\n"
"v_mfma_f32_16x16x32_bf16 v[64:67], a[156:159], a[16:19], v[64:67]\n"
"s_waitcnt lgkmcnt(2)\n"
"ds_read_b64_tr_b16 a[140:141], v36 offset:24576\n"
"v_lshlrev_b32 v46, 16, v180\n"
"v_mfma_f32_16x16x32_bf16 v[80:83], a[156:159], a[20:23], v[80:83]\n"
"ds_read_b64_tr_b16 a[142:143], v36 offset:28672\n"
"v_and_b32 v47, 0xffff0000, v180\n"
"v_mfma_f32_16x16x32_bf16 v[96:99], a[156:159], a[24:27], v[96:99]\n"
"ds_read_b128 a[0:3], v20 offset:8192\n"
"v_lshlrev_b32 v48, 16, v181\n"
"v_mfma_f32_16x16x32_bf16 v[112:115], a[156:159], a[28:31], v[112:115]\n"
"ds_read_b128 a[4:7], v21 offset:8192\n"
"v_and_b32 v49, 0xffff0000, v181\n"
"v_mfma_f32_16x16x32_bf16 v[68:71], a[160:163], a[16:19], v[68:71]\n"
"ds_read_b128 a[8:11], v22 offset:8192\n"
"v_lshlrev_b32 v50, 16, v182\n"
"v_mfma_f32_16x16x32_bf16 v[84:87], a[160:163], a[20:23], v[84:87]\n"
"ds_read_b128 a[12:15], v23 offset:8192\n"
"v_and_b32 v51, 0xffff0000, v182\n"
"v_mfma_f32_16x16x32_bf16 v[100:103], a[160:163], a[24:27], v[100:103]\n"
"ds_read_b64_tr_b16 a[144:145], v37 offset:24576\n"
"v_lshlrev_b32 v52, 16, v183\n"
"v_mfma_f32_16x16x32_bf16 v[116:119], a[160:163], a[28:31], v[116:119]\n"
"ds_read_b64_tr_b16 a[146:147], v37 offset:28672\n"
"v_and_b32 v53, 0xffff0000, v183\n"
"v_mfma_f32_16x16x32_bf16 v[72:75], a[164:167], a[16:19], v[72:75]\n"
"ds_read_b64_tr_b16 a[148:149], v38 offset:24576\n"
"v_mfma_f32_16x16x32_bf16 v[88:91], a[164:167], a[20:23], v[88:91]\n"
"ds_read_b64_tr_b16 a[150:151], v38 offset:28672\n"
"v_mfma_f32_16x16x32_bf16 v[104:107], a[164:167], a[24:27], v[104:107]\n"
"ds_read_b64_tr_b16 a[152:153], v39 offset:24576\n"
"v_mfma_f32_16x16x32_bf16 v[120:123], a[164:167], a[28:31], v[120:123]\n"
"s_waitcnt lgkmcnt(5)\n"
"ds_read_b64_tr_b16 a[154:155], v39 offset:28672\n"
"v_mfma_f32_16x16x32_bf16 v[76:79], a[168:171], a[16:19], v[76:79]\n"
"v_mfma_f32_16x16x32_bf16 v[92:95], a[168:171], a[20:23], v[92:95]\n"
"s_add_u32 m0, s84, 114688\n"
"v_mfma_f32_16x16x32_bf16 v[108:111], a[168:171], a[24:27], v[108:111]\n"
"buffer_load_dwordx4 v10, s[76:79], s86 offen lds\n"
"v_mfma_f32_16x16x32_bf16 v[124:127], a[168:171], a[28:31], v[124:127]\n"
"buffer_load_dwordx4 v11, s[76:79], s86 offen offset:1024 lds\n"
"s_add_u32 s86, s86, 32768\n"
"s_cmp_eq_u32 s86, s30\n"
"s_cselect_b32 s42, 131072, 0\n"
"v_mfma_f32_16x16x32_bf16 v[64:67], a[140:143], a[0:3], v[64:67]\n"
"s_add_u32 s86, s86, s42\n"
"ds_read_b64_tr_b16 a[156:157], v28 offset:0\n"
"v_lshlrev_b32 v54, 16, v188\n"
"v_mfma_f32_16x16x32_bf16 v[80:83], a[140:143], a[4:7], v[80:83]\n"
"ds_read_b64_tr_b16 a[158:159], v28 offset:4096\n"
"v_and_b32 v55, 0xffff0000, v188\n"
"v_mfma_f32_16x16x32_bf16 v[96:99], a[140:143], a[8:11], v[96:99]\n"
"ds_read_b128 a[16:19], v20 offset:32768\n"
"v_lshlrev_b32 v56, 16, v189\n"
"v_mfma_f32_16x16x32_bf16 v[112:115], a[140:143], a[12:15], v[112:115]\n"
"s_waitcnt lgkmcnt(3)\n"
"ds_read_b128 a[20:23], v21 offset:32768\n"
"v_and_b32 v57, 0xffff0000, v189\n"
"v_mfma_f32_16x16x32_bf16 v[68:71], a[144:147], a[0:3], v[68:71]\n"
"ds_read_b128 a[24:27], v22 offset:32768\n"
"v_lshlrev_b32 v58, 16, v190\n"
"v_mfma_f32_16x16x32_bf16 v[84:87], a[144:147], a[4:7], v[84:87]\n"
"ds_read_b128 a[28:31], v23 offset:32768\n"
"v_and_b32 v59, 0xffff0000, v190\n"
"v_mfma_f32_16x16x32_bf16 v[100:103], a[144:147], a[8:11], v[100:103]\n"
"ds_read_b64_tr_b16 a[160:161], v29 offset:0\n"
"v_lshlrev_b32 v60, 16, v191\n"
"v_mfma_f32_16x16x32_bf16 v[116:119], a[144:147], a[12:15], v[116:119]\n"
"ds_read_b64_tr_b16 a[162:163], v29 offset:4096\n"
"v_and_b32 v61, 0xffff0000, v191\n"
"v_mfma_f32_16x16x32_bf16 v[72:75], a[148:151], a[0:3], v[72:75]\n"
"ds_read_b64_tr_b16 a[164:165], v30 offset:0\n"
"v_mfma_f32_16x16x32_bf16 v[88:91], a[148:151], a[4:7], v[88:91]\n"
"ds_read_b64_tr_b16 a[166:167], v30 offset:4096\n"
"v_mfma_f32_16x16x32_bf16 v[104:107], a[148:151], a[8:11], v[104:107]\n"
"ds_read_b64_tr_b16 a[168:169], v31 offset:0\n"
"v_mfma_f32_16x16x32_bf16 v[120:123], a[148:151], a[12:15], v[120:123]\n"
"ds_read_b64_tr_b16 a[170:171], v31 offset:4096\n"
"v_mfma_f32_16x16x32_bf16 v[76:79], a[152:155], a[0:3], v[76:79]\n"
"v_mfma_f32_16x16x32_bf16 v[92:95], a[152:155], a[4:7], v[92:95]\n"
"s_add_u32 m0, s84, 16384\n"
"v_mfma_f32_16x16x32_bf16 v[108:111], a[152:155], a[8:11], v[108:111]\n"
"buffer_load_dwordx4 v10, s[76:79], s86 offen lds\n"
"v_mfma_f32_16x16x32_bf16 v[124:127], a[152:155], a[12:15], v[124:127]\n"
"buffer_load_dwordx4 v11, s[76:79], s86 offen offset:1024 lds\n"
"s_add_u32 s86, s86, 32768\n"
"s_waitcnt vmcnt(16)\n"
"s_barrier\n"
"s_waitcnt lgkmcnt(9)\n"
"v_mfma_f32_16x16x32_bf16 v[64:67], a[156:159], a[16:19], v[64:67]\n"
"s_waitcnt lgkmcnt(2)\n"
"ds_read_b64_tr_b16 a[140:141], v28 offset:8192\n"
"v_add_f32 v192, v192, v46\n"
"v_mfma_f32_16x16x32_bf16 v[80:83], a[156:159], a[20:23], v[80:83]\n"
"ds_read_b64_tr_b16 a[142:143], v28 offset:12288\n"
"v_add_f32 v193, v193, v47\n"
"v_mfma_f32_16x16x32_bf16 v[96:99], a[156:159], a[24:27], v[96:99]\n"
"ds_read_b128 a[0:3], v20 offset:40960\n"
"v_add_f32 v194, v194, v48\n"
"v_mfma_f32_16x16x32_bf16 v[112:115], a[156:159], a[28:31], v[112:115]\n"
"ds_read_b128 a[4:7], v21 offset:40960\n"
"v_add_f32 v195, v195, v49\n"
"v_mfma_f32_16x16x32_bf16 v[68:71], a[160:163], a[16:19], v[68:71]\n"
"ds_read_b128 a[8:11], v22 offset:40960\n"
"v_add_f32 v196, v196, v50\n"
"v_mfma_f32_16x16x32_bf16 v[84:87], a[160:163], a[20:23], v[84:87]\n"
"ds_read_b128 a[12:15], v23 offset:40960\n"
"v_add_f32 v197, v197, v51\n"
"v_mfma_f32_16x16x32_bf16 v[100:103], a[160:163], a[24:27], v[100:103]\n"
"ds_read_b64_tr_b16 a[144:145], v29 offset:8192\n"
"v_add_f32 v198, v198, v52\n"
"v_mfma_f32_16x16x32_bf16 v[116:119], a[160:163], a[28:31], v[116:119]\n"
"ds_read_b64_tr_b16 a[146:147], v29 offset:12288\n"
"v_add_f32 v199, v199, v53\n"
"v_mfma_f32_16x16x32_bf16 v[72:75], a[164:167], a[16:19], v[72:75]\n"
"ds_read_b64_tr_b16 a[148:149], v30 offset:8192\n"
"v_mfma_f32_16x16x32_bf16 v[88:91], a[164:167], a[20:23], v[88:91]\n"
"ds_read_b64_tr_b16 a[150:151], v30 offset:12288\n"
"v_mfma_f32_16x16x32_bf16 v[104:107], a[164:167], a[24:27], v[104:107]\n"
"ds_read_b64_tr_b16 a[152:153], v31 offset:8192\n"
"v_mfma_f32_16x16x32_bf16 v[120:123], a[164:167], a[28:31], v[120:123]\n"
"s_waitcnt lgkmcnt(5)\n"
"ds_read_b64_tr_b16 a[154:155], v31 offset:12288\n"
"v_mfma_f32_16x16x32_bf16 v[76:79], a[168:171], a[16:19], v[76:79]\n"
"v_mfma_f32_16x16x32_bf16 v[92:95], a[168:171], a[20:23], v[92:95]\n"
"s_add_u32 m0, s84, 122880\n"
"v_mfma_f32_16x16x32_bf16 v[108:111], a[168:171], a[24:27], v[108:111]\n"
"buffer_load_dwordx4 v10, s[76:79], s86 offen lds\n"
"v_mfma_f32_16x16x32_bf16 v[124:127], a[168:171], a[28:31], v[124:127]\n"
"buffer_load_dwordx4 v11, s[76:79], s86 offen offset:1024 lds\n"
"s_add_u32 s86, s86, 32768\n"
"v_mfma_f32_16x16x32_bf16 v[64:67], a[140:143], a[0:3], v[64:67]\n"
"ds_read_b64_tr_b16 a[156:157], v16 offset:49152\n"
"v_add_f32 v200, v200, v54\n"
"v_mfma_f32_16x16x32_bf16 v[80:83], a[140:143], a[4:7], v[80:83]\n"
"ds_read_b64_tr_b16 a[158:159], v16 offset:53248\n"
"v_add_f32 v201, v201, v55\n"
"v_mfma_f32_16x16x32_bf16 v[96:99], a[140:143], a[8:11], v[96:99]\n"
"ds_read_b128 a[16:19], v20 offset:57344\n"
"v_add_f32 v202, v202, v56\n"
"v_mfma_f32_16x16x32_bf16 v[112:115], a[140:143], a[12:15], v[112:115]\n"
"s_waitcnt lgkmcnt(3)\n"
"ds_read_b128 a[20:23], v21 offset:57344\n"
"v_add_f32 v203, v203, v57\n"
"v_mfma_f32_16x16x32_bf16 v[68:71], a[144:147], a[0:3], v[68:71]\n"
"ds_read_b128 a[24:27], v22 offset:57344\n"
"v_add_f32 v204, v204, v58\n"
"v_mfma_f32_16x16x32_bf16 v[84:87], a[144:147], a[4:7], v[84:87]\n"
"ds_read_b128 a[28:31], v23 offset:57344\n"
"v_add_f32 v205, v205, v59\n"
"v_mfma_f32_16x16x32_bf16 v[100:103], a[144:147], a[8:11], v[100:103]\n"
"ds_read_b64_tr_b16 a[160:161], v17 offset:49152\n"
"v_add_f32 v206, v206, v60\n"
"v_mfma_f32_16x16x32_bf16 v[116:119], a[144:147], a[12:15], v[116:119]\n"
"ds_read_b64_tr_b16 a[162:163], v17 offset:53248\n"
"v_add_f32 v207, v207, v61\n"
"v_mfma_f32_16x16x32_bf16 v[72:75], a[148:151], a[0:3], v[72:75]\n"
"ds_read_b64_tr_b16 a[164:165], v18 offset:49152\n"
"v_mfma_f32_16x16x32_bf16 v[88:91], a[148:151], a[4:7], v[88:91]\n"
"ds_read_b64_tr_b16 a[166:167], v18 offset:53248\n"
"v_mfma_f32_16x16x32_bf16 v[104:107], a[148:151], a[8:11], v[104:107]\n"
"ds_read_b64_tr_b16 a[168:169], v19 offset:49152\n"
"v_mfma_f32_16x16x32_bf16 v[120:123], a[148:151], a[12:15], v[120:123]\n"
"s_add_u32 m0, s84, 24576\n"
"ds_read_b64_tr_b16 a[170:171], v19 offset:53248\n"
"v_mfma_f32_16x16x32_bf16 v[76:79], a[152:155], a[0:3], v[76:79]\n"
"buffer_load_dwordx4 v14, s[68:71], s49 offen sc1 lds\n"
"v_mfma_f32_16x16x32_bf16 v[92:95], a[152:155], a[4:7], v[92:95]\n"
"buffer_load_dwordx4 v15, s[68:71], s49 offen offset:1024 sc1 lds\n"
"s_add_u32 m0, s84, 147456\n"
"v_mfma_f32_16x16x32_bf16 v[108:111], a[152:155], a[8:11], v[108:111]\n"
"s_add_u32 s49, s49, 1024\n"
"buffer_load_dwordx4 v10, s[76:79], s86 offen lds\n"
"v_mfma_f32_16x16x32_bf16 v[124:127], a[152:155], a[12:15], v[124:127]\n"
"buffer_load_dwordx4 v11, s[76:79], s86 offen offset:1024 lds\n"
"s_add_u32 s86, s86, 32768\n"
"s_waitcnt vmcnt(12)\n"
"s_barrier\n"
"s_waitcnt lgkmcnt(9)\n"
"v_mfma_f32_16x16x32_bf16 v[64:67], a[156:159], a[16:19], v[64:67]\n"
"s_waitcnt lgkmcnt(2)\n"
"ds_read_b64_tr_b16 a[140:141], v28 offset:16384\n"
"v_lshlrev_b32 v128, 16, v164\n"
"v_mfma_f32_16x16x32_bf16 v[80:83], a[156:159], a[20:23], v[80:83]\n"
"ds_read_b64_tr_b16 a[142:143], v28 offset:20480\n"
"v_and_b32 v129, 0xffff0000, v164\n"
"v_mfma_f32_16x16x32_bf16 v[96:99], a[156:159], a[24:27], v[96:99]\n"
"ds_read_b128 a[0:3], v32 offset:24576\n"
"v_lshlrev_b32 v130, 16, v165\n"
"v_mfma_f32_16x16x32_bf16 v[112:115], a[156:159], a[28:31], v[112:115]\n"
"ds_read_b128 a[4:7], v33 offset:24576\n"
"v_and_b32 v131, 0xffff0000, v165\n"
"v_mfma_f32_16x16x32_bf16 v[68:71], a[160:163], a[16:19], v[68:71]\n"
"ds_read_b128 a[8:11], v34 offset:24576\n"
"v_lshlrev_b32 v132, 16, v166\n"
"v_mfma_f32_16x16x32_bf16 v[84:87], a[160:163], a[20:23], v[84:87]\n"
"ds_read_b128 a[12:15], v35 offset:24576\n"
"v_and_b32 v133, 0xffff0000, v166\n"
"v_mfma_f32_16x16x32_bf16 v[100:103], a[160:163], a[24:27], v[100:103]\n"
"ds_read_b64_tr_b16 a[144:145], v29 offset:16384\n"
"v_lshlrev_b32 v134, 16, v167\n"
"v_mfma_f32_16x16x32_bf16 v[116:119], a[160:163], a[28:31], v[116:119]\n"
"ds_read_b64_tr_b16 a[146:147], v29 offset:20480\n"
"v_and_b32 v135, 0xffff0000, v167\n"
"v_mfma_f32_16x16x32_bf16 v[72:75], a[164:167], a[16:19], v[72:75]\n"
"ds_read_b64_tr_b16 a[148:149], v30 offset:16384\n"
"v_mfma_f32_16x16x32_bf16 v[88:91], a[164:167], a[20:23], v[88:91]\n"
"ds_read_b64_tr_b16 a[150:151], v30 offset:20480\n"
"v_mfma_f32_16x16x32_bf16 v[104:107], a[164:167], a[24:27], v[104:107]\n"
"ds_read_b64_tr_b16 a[152:153], v31 offset:16384\n"
"v_mfma_f32_16x16x32_bf16 v[120:123], a[164:167], a[28:31], v[120:123]\n"
"s_add_u32 m0, s84, 0\n"
"s_waitcnt lgkmcnt(5)\n"
"ds_read_b64_tr_b16 a[154:155], v31 offset:20480\n"
"v_mfma_f32_16x16x32_bf16 v[76:79], a[168:171], a[16:19], v[76:79]\n"
"buffer_load_dwordx4 v14, s[68:71], s49 offen sc1 lds\n"
"v_mfma_f32_16x16x32_bf16 v[92:95], a[168:171], a[20:23], v[92:95]\n"
"buffer_load_dwordx4 v15, s[68:71], s49 offen offset:1024 sc1 lds\n"
"s_add_u32 m0, s84, 155648\n"
"v_mfma_f32_16x16x32_bf16 v[108:111], a[168:171], a[24:27], v[108:111]\n"
"s_add_u32 s49, s49, 1024\n"
"buffer_load_dwordx4 v10, s[76:79], s86 offen lds\n"
"v_mfma_f32_16x16x32_bf16 v[124:127], a[168:171], a[28:31], v[124:127]\n"
"buffer_load_dwordx4 v11, s[76:79], s86 offen offset:1024 lds\n"
"s_add_u32 s86, s86, 32768\n"
"s_cmp_eq_u32 s86, s30\n"
"s_cselect_b32 s42, 131072, 0\n"
"v_mfma_f32_16x16x32_bf16 v[64:67], a[140:143], a[0:3], v[64:67]\n"
"s_add_u32 s86, s86, s42\n"
"ds_read_b64_tr_b16 a[156:157], v28 offset:40960\n"
"v_lshlrev_b32 v136, 16, v172\n"
"v_mfma_f32_16x16x32_bf16 v[80:83], a[140:143], a[4:7], v[80:83]\n"
"ds_read_b64_tr_b16 a[158:159], v28 offset:45056\n"
"v_and_b32 v137, 0xffff0000, v172\n"
"v_mfma_f32_16x16x32_bf16 v[96:99], a[140:143], a[8:11], v[96:99]\n"
"ds_read_b128 a[16:19], v32 offset:32768\n"
"v_lshlrev_b32 v138, 16, v173\n"
"v_mfma_f32_16x16x32_bf16 v[112:115], a[140:143], a[12:15], v[112:115]\n"
"s_waitcnt lgkmcnt(3)\n"
"ds_read_b128 a[20:23], v33 offset:32768\n"
"v_and_b32 v139, 0xffff0000, v173\n"
"v_mfma_f32_16x16x32_bf16 v[68:71], a[144:147], a[0:3], v[68:71]\n"
"ds_read_b128 a[24:27], v34 offset:32768\n"
"v_lshlrev_b32 v140, 16, v174\n"
"v_mfma_f32_16x16x32_bf16 v[84:87], a[144:147], a[4:7], v[84:87]\n"
"ds_read_b128 a[28:31], v35 offset:32768\n"
"v_and_b32 v141, 0xffff0000, v174\n"
"v_mfma_f32_16x16x32_bf16 v[100:103], a[144:147], a[8:11], v[100:103]\n"
"ds_read_b64_tr_b16 a[160:161], v29 offset:40960\n"
"v_lshlrev_b32 v142, 16, v175\n"
"v_mfma_f32_16x16x32_bf16 v[116:119], a[144:147], a[12:15], v[116:119]\n"
"ds_read_b64_tr_b16 a[162:163], v29 offset:45056\n"
"v_and_b32 v143, 0xffff0000, v175\n"
"v_mfma_f32_16x16x32_bf16 v[72:75], a[148:151], a[0:3], v[72:75]\n"
"ds_read_b64_tr_b16 a[164:165], v30 offset:40960\n"
"v_mfma_f32_16x16x32_bf16 v[88:91], a[148:151], a[4:7], v[88:91]\n"
"ds_read_b64_tr_b16 a[166:167], v30 offset:45056\n"
"v_mfma_f32_16x16x32_bf16 v[104:107], a[148:151], a[8:11], v[104:107]\n"
"ds_read_b64_tr_b16 a[168:169], v31 offset:40960\n"
"v_mfma_f32_16x16x32_bf16 v[120:123], a[148:151], a[12:15], v[120:123]\n"
"s_add_u32 m0, s84, 8192\n"
"ds_read_b64_tr_b16 a[170:171], v31 offset:45056\n"
"v_mfma_f32_16x16x32_bf16 v[76:79], a[152:155], a[0:3], v[76:79]\n"
"buffer_load_dwordx4 v14, s[68:71], s49 offen sc1 lds\n"
"v_mfma_f32_16x16x32_bf16 v[92:95], a[152:155], a[4:7], v[92:95]\n"
"buffer_load_dwordx4 v15, s[68:71], s49 offen offset:1024 sc1 lds\n"
"s_add_u32 s49, s49, 1024\n"
"s_cmp_eq_u32 s49, s31\n"
"s_cselect_b32 s42, 4096, 0\n"
"v_mfma_f32_16x16x32_bf16 v[108:111], a[152:155], a[8:11], v[108:111]\n"
"s_add_u32 m0, s84, 65536\n"
"s_add_u32 s49, s49, s42\n"
"buffer_load_dwordx4 v10, s[76:79], s86 offen lds\n"
"v_mfma_f32_16x16x32_bf16 v[124:127], a[152:155], a[12:15], v[124:127]\n"
"buffer_load_dwordx4 v11, s[76:79], s86 offen offset:1024 lds\n"
"s_add_u32 s86, s86, 32768\n"
"s_waitcnt vmcnt(14)\n"
"s_barrier\n"
"s_waitcnt lgkmcnt(9)\n"
"v_mfma_f32_16x16x32_bf16 v[64:67], a[156:159], a[16:19], v[64:67]\n"
"s_waitcnt lgkmcnt(2)\n"
"ds_read_b64_tr_b16 a[140:141], v28 offset:49152\n"
"v_cvt_pk_bf16_f32 v208, v208, v209\n"
"v_mfma_f32_16x16x32_bf16 v[80:83], a[156:159], a[20:23], v[80:83]\n"
"ds_read_b64_tr_b16 a[142:143], v28 offset:53248\n"
"v_cvt_pk_bf16_f32 v209, v210, v211\n"
"v_mfma_f32_16x16x32_bf16 v[96:99], a[156:159], a[24:27], v[96:99]\n"
"ds_read_b128 a[0:3], v40 offset:0\n"
"v_cvt_pk_bf16_f32 v210, v212, v213\n"
"v_mfma_f32_16x16x32_bf16 v[112:115], a[156:159], a[28:31], v[112:115]\n"
"ds_read_b128 a[4:7], v41 offset:0\n"
"v_cvt_pk_bf16_f32 v211, v214, v215\n"
"v_mfma_f32_16x16x32_bf16 v[68:71], a[160:163], a[16:19], v[68:71]\n"
"ds_read_b128 a[8:11], v42 offset:0\n"
"v_cvt_pk_bf16_f32 v212, v216, v217\n"
"v_mfma_f32_16x16x32_bf16 v[84:87], a[160:163], a[20:23], v[84:87]\n"
"ds_read_b128 a[12:15], v43 offset:0\n"
"v_cvt_pk_bf16_f32 v213, v218, v219\n"
"v_mfma_f32_16x16x32_bf16 v[100:103], a[160:163], a[24:27], v[100:103]\n"
"ds_read_b64_tr_b16 a[144:145], v29 offset:49152\n"
"v_cvt_pk_bf16_f32 v214, v220, v221\n"
"v_mfma_f32_16x16x32_bf16 v[116:119], a[160:163], a[28:31], v[116:119]\n"
"ds_read_b64_tr_b16 a[146:147], v29 offset:53248\n"
"v_cvt_pk_bf16_f32 v215, v222, v223\n"
"v_mfma_f32_16x16x32_bf16 v[72:75], a[164:167], a[16:19], v[72:75]\n"
"ds_read_b64_tr_b16 a[148:149], v30 offset:49152\n"
"v_mfma_f32_16x16x32_bf16 v[88:91], a[164:167], a[20:23], v[88:91]\n"
"ds_read_b64_tr_b16 a[150:151], v30 offset:53248\n"
"v_mfma_f32_16x16x32_bf16 v[104:107], a[164:167], a[24:27], v[104:107]\n"
"ds_read_b64_tr_b16 a[152:153], v31 offset:49152\n"
"v_mfma_f32_16x16x32_bf16 v[120:123], a[164:167], a[28:31], v[120:123]\n"
"s_add_u32 m0, s84, 32768\n"
"s_waitcnt lgkmcnt(5)\n"
"ds_read_b64_tr_b16 a[154:155], v31 offset:53248\n"
"v_mfma_f32_16x16x32_bf16 v[76:79], a[168:171], a[16:19], v[76:79]\n"
"buffer_load_dwordx4 v14, s[68:71], s49 offen sc1 lds\n"
"v_mfma_f32_16x16x32_bf16 v[92:95], a[168:171], a[20:23], v[92:95]\n"
"buffer_load_dwordx4 v15, s[68:71], s49 offen offset:1024 sc1 lds\n"
"s_add_u32 m0, s84, 73728\n"
"v_mfma_f32_16x16x32_bf16 v[108:111], a[168:171], a[24:27], v[108:111]\n"
"s_add_u32 s49, s49, 1024\n"
"buffer_load_dwordx4 v10, s[76:79], s86 offen lds\n"
"v_mfma_f32_16x16x32_bf16 v[124:127], a[168:171], a[28:31], v[124:127]\n"
"buffer_load_dwordx4 v11, s[76:79], s86 offen offset:1024 lds\n"
"s_add_u32 s86, s86, 32768\n"
"v_mfma_f32_16x16x32_bf16 v[64:67], a[140:143], a[0:3], v[64:67]\n"
"ds_read_b64_tr_b16 a[156:157], v16 offset:16384\n"
"v_cvt_pk_bf16_f32 v224, v224, v225\n"
"v_mfma_f32_16x16x32_bf16 v[80:83], a[140:143], a[4:7], v[80:83]\n"
"ds_read_b64_tr_b16 a[158:159], v16 offset:20480\n"
"v_cvt_pk_bf16_f32 v225, v226, v227\n"
"v_mfma_f32_16x16x32_bf16 v[96:99], a[140:143], a[8:11], v[96:99]\n"
"ds_read_b128 a[16:19], v40 offset:8192\n"
"v_cvt_pk_bf16_f32 v226, v228, v229\n"
"v_mfma_f32_16x16x32_bf16 v[112:115], a[140:143], a[12:15], v[112:115]\n"
"s_waitcnt lgkmcnt(3)\n"
"ds_read_b128 a[20:23], v41 offset:8192\n"
"v_cvt_pk_bf16_f32 v227, v230, v231\n"
"v_mfma_f32_16x16x32_bf16 v[68:71], a[144:147], a[0:3], v[68:71]\n"
"ds_read_b128 a[24:27], v42 offset:8192\n"
"v_cvt_pk_bf16_f32 v228, v232, v233\n"
"v_mfma_f32_16x16x32_bf16 v[84:87], a[144:147], a[4:7], v[84:87]\n"
"ds_read_b128 a[28:31], v43 offset:8192\n"
"v_cvt_pk_bf16_f32 v229, v234, v235\n"
"v_mfma_f32_16x16x32_bf16 v[100:103], a[144:147], a[8:11], v[100:103]\n"
"ds_read_b64_tr_b16 a[160:161], v17 offset:16384\n"
"v_cvt_pk_bf16_f32 v230, v236, v237\n"
"v_mfma_f32_16x16x32_bf16 v[116:119], a[144:147], a[12:15], v[116:119]\n"
"ds_read_b64_tr_b16 a[162:163], v17 offset:20480\n"
"v_cvt_pk_bf16_f32 v231, v238, v239\n"
"v_mfma_f32_16x16x32_bf16 v[72:75], a[148:151], a[0:3], v[72:75]\n"
"ds_read_b64_tr_b16 a[164:165], v18 offset:16384\n"
"v_mfma_f32_16x16x32_bf16 v[88:91], a[148:151], a[4:7], v[88:91]\n"
"ds_read_b64_tr_b16 a[166:167], v18 offset:20480\n"
"v_mfma_f32_16x16x32_bf16 v[104:107], a[148:151], a[8:11], v[104:107]\n"
"ds_read_b64_tr_b16 a[168:169], v19 offset:16384\n"
"v_mfma_f32_16x16x32_bf16 v[120:123], a[148:151], a[12:15], v[120:123]\n"
"s_add_u32 m0, s84, 40960\n"
"ds_read_b64_tr_b16 a[170:171], v19 offset:20480\n"
"v_mfma_f32_16x16x32_bf16 v[76:79], a[152:155], a[0:3], v[76:79]\n"
"buffer_load_dwordx4 v14, s[68:71], s49 offen sc1 lds\n"
"v_mfma_f32_16x16x32_bf16 v[92:95], a[152:155], a[4:7], v[92:95]\n"
"buffer_load_dwordx4 v15, s[68:71], s49 offen offset:1024 sc1 lds\n"
"s_add_u32 m0, s84, 49152\n"
"v_mfma_f32_16x16x32_bf16 v[108:111], a[152:155], a[8:11], v[108:111]\n"
"s_add_u32 s49, s49, 1024\n"
"buffer_load_dwordx4 v10, s[76:79], s86 offen lds\n"
"v_mfma_f32_16x16x32_bf16 v[124:127], a[152:155], a[12:15], v[124:127]\n"
"buffer_load_dwordx4 v11, s[76:79], s86 offen offset:1024 lds\n"
"s_add_u32 s86, s86, 32768\n"
"s_waitcnt vmcnt(14)\n"
"s_barrier\n"
"s_waitcnt lgkmcnt(9)\n"
"v_mfma_f32_16x16x32_bf16 v[64:67], a[156:159], a[16:19], v[64:67]\n"
"s_waitcnt lgkmcnt(2)\n"
"ds_read_b64_tr_b16 a[140:141], v28 offset:57344\n"
"v_cvt_pk_bf16_f32 v240, v240, v241\n"
"v_mfma_f32_16x16x32_bf16 v[80:83], a[156:159], a[20:23], v[80:83]\n"
"ds_read_b64_tr_b16 a[142:143], v28 offset:61440\n"
"v_cvt_pk_bf16_f32 v241, v242, v243\n"
"v_mfma_f32_16x16x32_bf16 v[96:99], a[156:159], a[24:27], v[96:99]\n"
"ds_read_b128 a[0:3], v20 offset:24576\n"
"v_cvt_pk_bf16_f32 v242, v244, v245\n"
"v_mfma_f32_16x16x32_bf16 v[112:115], a[156:159], a[28:31], v[112:115]\n"
"ds_read_b128 a[4:7], v21 offset:24576\n"
"v_cvt_pk_bf16_f32 v243, v246, v247\n"
"v_mfma_f32_16x16x32_bf16 v[68:71], a[160:163], a[16:19], v[68:71]\n"
"ds_read_b128 a[8:11], v22 offset:24576\n"
"v_cvt_pk_bf16_f32 v244, v248, v249\n"
"v_mfma_f32_16x16x32_bf16 v[84:87], a[160:163], a[20:23], v[84:87]\n"
"ds_read_b128 a[12:15], v23 offset:24576\n"
"v_cvt_pk_bf16_f32 v245, v250, v251\n"
"v_mfma_f32_16x16x32_bf16 v[100:103], a[160:163], a[24:27], v[100:103]\n"
"ds_read_b64_tr_b16 a[144:145], v29 offset:57344\n"
"v_cvt_pk_bf16_f32 v246, v252, v253\n"
"v_mfma_f32_16x16x32_bf16 v[116:119], a[160:163], a[28:31], v[116:119]\n"
"ds_read_b64_tr_b16 a[146:147], v29 offset:61440\n"
"v_cvt_pk_bf16_f32 v247, v254, v255\n"
"v_mfma_f32_16x16x32_bf16 v[72:75], a[164:167], a[16:19], v[72:75]\n"
"ds_read_b64_tr_b16 a[148:149], v30 offset:57344\n"
"v_mfma_f32_16x16x32_bf16 v[88:91], a[164:167], a[20:23], v[88:91]\n"
"ds_read_b64_tr_b16 a[150:151], v30 offset:61440\n"
"v_mfma_f32_16x16x32_bf16 v[104:107], a[164:167], a[24:27], v[104:107]\n"
"ds_read_b64_tr_b16 a[152:153], v31 offset:57344\n"
"v_mfma_f32_16x16x32_bf16 v[120:123], a[164:167], a[28:31], v[120:123]\n"
"s_add_u32 m0, s84, 57344\n"
"s_waitcnt lgkmcnt(5)\n"
"ds_read_b64_tr_b16 a[154:155], v31 offset:61440\n"
"v_mfma_f32_16x16x32_bf16 v[76:79], a[168:171], a[16:19], v[76:79]\n"
"buffer_load_dwordx4 v14, s[68:71], s49 offen sc1 lds\n"
"v_mfma_f32_16x16x32_bf16 v[92:95], a[168:171], a[20:23], v[92:95]\n"
"buffer_load_dwordx4 v15, s[68:71], s49 offen offset:1024 sc1 lds\n"
"s_add_u32 m0, s84, 81920\n"
"v_mfma_f32_16x16x32_bf16 v[108:111], a[168:171], a[24:27], v[108:111]\n"
"s_add_u32 s49, s49, 1024\n"
"buffer_load_dwordx4 v10, s[76:79], s86 offen lds\n"
"v_mfma_f32_16x16x32_bf16 v[124:127], a[168:171], a[28:31], v[124:127]\n"
"buffer_load_dwordx4 v11, s[76:79], s86 offen offset:1024 lds\n"
"s_add_u32 s86, s86, 32768\n"
"s_cmp_eq_u32 s86, s30\n"
"v_mfma_f32_16x16x32_bf16 v[64:67], a[140:143], a[0:3], v[64:67]\n"
"s_cselect_b32 s42, 131072, 0\n"
"s_add_u32 s86, s86, s42\n"
"ds_read_b64_tr_b16 a[156:157], v36 offset:16384\n"
"v_mfma_f32_16x16x32_bf16 v[80:83], a[140:143], a[4:7], v[80:83]\n"
"ds_read_b64_tr_b16 a[158:159], v36 offset:20480\n"
"v_mfma_f32_16x16x32_bf16 v[96:99], a[140:143], a[8:11], v[96:99]\n"
"ds_read_b128 a[16:19], v20 offset:0\n"
"v_mfma_f32_16x16x32_bf16 v[112:115], a[140:143], a[12:15], v[112:115]\n"
"s_waitcnt lgkmcnt(3)\n"
"ds_read_b128 a[20:23], v21 offset:0\n"
"v_mfma_f32_16x16x32_bf16 v[68:71], a[144:147], a[0:3], v[68:71]\n"
"ds_read_b128 a[24:27], v22 offset:0\n"
"v_mfma_f32_16x16x32_bf16 v[84:87], a[144:147], a[4:7], v[84:87]\n"
"ds_read_b128 a[28:31], v23 offset:0\n"
"v_mfma_f32_16x16x32_bf16 v[100:103], a[144:147], a[8:11], v[100:103]\n"
"ds_read_b64_tr_b16 a[160:161], v37 offset:16384\n"
"v_mfma_f32_16x16x32_bf16 v[116:119], a[144:147], a[12:15], v[116:119]\n"
"ds_read_b64_tr_b16 a[162:163], v37 offset:20480\n"
"v_mfma_f32_16x16x32_bf16 v[72:75], a[148:151], a[0:3], v[72:75]\n"
"s_add_u32 m0, s84, 90112\n"
"ds_read_b64_tr_b16 a[164:165], v38 offset:16384\n"
"buffer_load_dwordx4 v14, s[68:71], s49 offen sc1 lds\n"
"v_mfma_f32_16x16x32_bf16 v[88:91], a[148:151], a[4:7], v[88:91]\n"
"ds_read_b64_tr_b16 a[166:167], v38 offset:20480\n"
"v_mfma_f32_16x16x32_bf16 v[104:107], a[148:151], a[8:11], v[104:107]\n"
"ds_read_b64_tr_b16 a[168:169], v39 offset:16384\n"
"buffer_load_dwordx4 v15, s[68:71], s49 offen offset:1024 sc1 lds\n"
"s_add_u32 s49, s49, 1024\n"
"s_cmp_eq_u32 s49, s31\n"
"s_cselect_b32 s42, 4096, 0\n"
"v_mfma_f32_16x16x32_bf16 v[120:123], a[148:151], a[12:15], v[120:123]\n"
"s_add_u32 m0, s84, 106496\n"
"s_add_u32 s49, s49, s42\n"
"ds_read_b64_tr_b16 a[170:171], v39 offset:20480\n"
"v_mfma_f32_16x16x32_bf16 v[76:79], a[152:155], a[0:3], v[76:79]\n"
"buffer_load_dwordx4 v10, s[76:79], s86 offen lds\n"
"v_mfma_f32_16x16x32_bf16 v[92:95], a[152:155], a[4:7], v[92:95]\n"
"buffer_load_dwordx4 v11, s[76:79], s86 offen offset:1024 lds\n"
"s_add_u32 s86, s86, 32768\n"
"v_mfma_f32_16x16x32_bf16 v[108:111], a[152:155], a[8:11], v[108:111]\n"
"s_sub_u32 s16, s23, 12582912\n"
"buffer_store_dwordx4 v[208:211], v4, s[64:67], s16 offen offset:0 sc1\n"
"v_mfma_f32_16x16x32_bf16 v[124:127], a[152:155], a[12:15], v[124:127]\n"
"buffer_store_dwordx4 v[212:215], v4, s[64:67], s16 offen offset:1024 sc1\n"
"s_waitcnt vmcnt(16)\n"
"s_barrier\n"
"s_waitcnt lgkmcnt(9)\n"
"v_mfma_f32_16x16x32_bf16 v[64:67], a[156:159], a[16:19], v[64:67]\n"
"s_waitcnt lgkmcnt(2)\n"
"ds_read_b64_tr_b16 a[140:141], v36 offset:24576\n"
"v_mfma_f32_16x16x32_bf16 v[80:83], a[156:159], a[20:23], v[80:83]\n"
"ds_read_b64_tr_b16 a[142:143], v36 offset:28672\n"
"v_mfma_f32_16x16x32_bf16 v[96:99], a[156:159], a[24:27], v[96:99]\n"
"ds_read_b128 a[0:3], v20 offset:8192\n"
"v_mfma_f32_16x16x32_bf16 v[112:115], a[156:159], a[28:31], v[112:115]\n"
"ds_read_b128 a[4:7], v21 offset:8192\n"
"v_mfma_f32_16x16x32_bf16 v[68:71], a[160:163], a[16:19], v[68:71]\n"
"ds_read_b128 a[8:11], v22 offset:8192\n"
"v_mfma_f32_16x16x32_bf16 v[84:87], a[160:163], a[20:23], v[84:87]\n"
"ds_read_b128 a[12:15], v23 offset:8192\n"
"v_mfma_f32_16x16x32_bf16 v[100:103], a[160:163], a[24:27], v[100:103]\n"
"ds_read_b64_tr_b16 a[144:145], v37 offset:24576\n"
"v_mfma_f32_16x16x32_bf16 v[116:119], a[160:163], a[28:31], v[116:119]\n"
"ds_read_b64_tr_b16 a[146:147], v37 offset:28672\n"
"v_mfma_f32_16x16x32_bf16 v[72:75], a[164:167], a[16:19], v[72:75]\n"
"s_add_u32 m0, s84, 98304\n"
"ds_read_b64_tr_b16 a[148:149], v38 offset:24576\n"
"buffer_load_dwordx4 v14, s[68:71], s49 offen sc1 lds\n"
"v_mfma_f32_16x16x32_bf16 v[88:91], a[164:167], a[20:23], v[88:91]\n"
"ds_read_b64_tr_b16 a[150:151], v38 offset:28672\n"
"v_mfma_f32_16x16x32_bf16 v[104:107], a[164:167], a[24:27], v[104:107]\n"
"ds_read_b64_tr_b16 a[152:153], v39 offset:24576\n"
"buffer_load_dwordx4 v15, s[68:71], s49 offen offset:1024 sc1 lds\n"
"v_mfma_f32_16x16x32_bf16 v[120:123], a[164:167], a[28:31], v[120:123]\n"
"s_add_u32 m0, s84, 114688\n"
"s_waitcnt lgkmcnt(5)\n"
"ds_read_b64_tr_b16 a[154:155], v39 offset:28672\n"
"v_mfma_f32_16x16x32_bf16 v[76:79], a[168:171], a[16:19], v[76:79]\n"
"s_add_u32 s49, s49, 1024\n"
"buffer_load_dwordx4 v10, s[76:79], s86 offen lds\n"
"v_mfma_f32_16x16x32_bf16 v[92:95], a[168:171], a[20:23], v[92:95]\n"
"buffer_load_dwordx4 v11, s[76:79], s86 offen offset:1024 lds\n"
"s_add_u32 s86, s86, 32768\n"
"v_mfma_f32_16x16x32_bf16 v[108:111], a[168:171], a[24:27], v[108:111]\n"
"s_sub_u32 s16, s24, 12582912\n"
"buffer_store_dwordx4 v[224:227], v4, s[64:67], s16 offen offset:0 sc1\n"
"v_mfma_f32_16x16x32_bf16 v[124:127], a[168:171], a[28:31], v[124:127]\n"
"buffer_store_dwordx4 v[228:231], v4, s[64:67], s16 offen offset:1024 sc1\n"
"v_mfma_f32_16x16x32_bf16 v[64:67], a[140:143], a[0:3], v[64:67]\n"
"ds_read_b64_tr_b16 a[156:157], v28 offset:0\n"
"v_mfma_f32_16x16x32_bf16 v[80:83], a[140:143], a[4:7], v[80:83]\n"
"ds_read_b64_tr_b16 a[158:159], v28 offset:4096\n"
"v_mfma_f32_16x16x32_bf16 v[96:99], a[140:143], a[8:11], v[96:99]\n"
"ds_read_b128 a[16:19], v20 offset:32768\n"
"v_mfma_f32_16x16x32_bf16 v[112:115], a[140:143], a[12:15], v[112:115]\n"
"s_waitcnt lgkmcnt(3)\n"
"ds_read_b128 a[20:23], v21 offset:32768\n"
"v_mfma_f32_16x16x32_bf16 v[68:71], a[144:147], a[0:3], v[68:71]\n"
"ds_read_b128 a[24:27], v22 offset:32768\n"
"v_mfma_f32_16x16x32_bf16 v[84:87], a[144:147], a[4:7], v[84:87]\n"
"ds_read_b128 a[28:31], v23 offset:32768\n"
"v_mfma_f32_16x16x32_bf16 v[100:103], a[144:147], a[8:11], v[100:103]\n"
"ds_read_b64_tr_b16 a[160:161], v29 offset:0\n"
"v_mfma_f32_16x16x32_bf16 v[116:119], a[144:147], a[12:15], v[116:119]\n"
"ds_read_b64_tr_b16 a[162:163], v29 offset:4096\n"
"v_mfma_f32_16x16x32_bf16 v[72:75], a[148:151], a[0:3], v[72:75]\n"
"s_add_u32 m0, s84, 131072\n"
"ds_read_b64_tr_b16 a[164:165], v30 offset:0\n"
"buffer_load_dwordx4 v14, s[68:71], s49 offen sc1 lds\n"
"v_mfma_f32_16x16x32_bf16 v[88:91], a[148:151], a[4:7], v[88:91]\n"
"ds_read_b64_tr_b16 a[166:167], v30 offset:4096\n"
"v_mfma_f32_16x16x32_bf16 v[104:107], a[148:151], a[8:11], v[104:107]\n"
"ds_read_b64_tr_b16 a[168:169], v31 offset:0\n"
"buffer_load_dwordx4 v15, s[68:71], s49 offen offset:1024 sc1 lds\n"
"v_mfma_f32_16x16x32_bf16 v[120:123], a[148:151], a[12:15], v[120:123]\n"
"s_add_u32 m0, s84, 16384\n"
"s_add_u32 s49, s49, 1024\n"
"ds_read_b64_tr_b16 a[170:171], v31 offset:4096\n"
"v_mfma_f32_16x16x32_bf16 v[76:79], a[152:155], a[0:3], v[76:79]\n"
"buffer_load_dwordx4 v10, s[76:79], s86 offen lds\n"
"v_mfma_f32_16x16x32_bf16 v[92:95], a[152:155], a[4:7], v[92:95]\n"
"buffer_load_dwordx4 v11, s[76:79], s86 offen offset:1024 lds\n"
"s_add_u32 s86, s86, 32768\n"
"v_mfma_f32_16x16x32_bf16 v[108:111], a[152:155], a[8:11], v[108:111]\n"
"s_sub_u32 s16, s25, 12582912\n"
"buffer_store_dwordx4 v[240:243], v4, s[64:67], s16 offen offset:0 sc1\n"
"v_mfma_f32_16x16x32_bf16 v[124:127], a[152:155], a[12:15], v[124:127]\n"
"buffer_store_dwordx4 v[244:247], v4, s[64:67], s16 offen offset:1024 sc1\n"
"s_waitcnt vmcnt(20)\n"
"s_barrier\n"
"s_waitcnt lgkmcnt(9)\n"
"v_mfma_f32_16x16x32_bf16 v[64:67], a[156:159], a[16:19], v[64:67]\n"
"s_waitcnt lgkmcnt(2)\n"
"ds_read_b64_tr_b16 a[140:141], v28 offset:8192\n"
"v_mfma_f32_16x16x32_bf16 v[80:83], a[156:159], a[20:23], v[80:83]\n"
"ds_read_b64_tr_b16 a[142:143], v28 offset:12288\n"
"v_mfma_f32_16x16x32_bf16 v[96:99], a[156:159], a[24:27], v[96:99]\n"
"ds_read_b128 a[0:3], v20 offset:40960\n"
"v_mfma_f32_16x16x32_bf16 v[112:115], a[156:159], a[28:31], v[112:115]\n"
"ds_read_b128 a[4:7], v21 offset:40960\n"
"v_mfma_f32_16x16x32_bf16 v[68:71], a[160:163], a[16:19], v[68:71]\n"
"ds_read_b128 a[8:11], v22 offset:40960\n"
"v_mfma_f32_16x16x32_bf16 v[84:87], a[160:163], a[20:23], v[84:87]\n"
"ds_read_b128 a[12:15], v23 offset:40960\n"
"v_mfma_f32_16x16x32_bf16 v[100:103], a[160:163], a[24:27], v[100:103]\n"
"ds_read_b64_tr_b16 a[144:145], v29 offset:8192\n"
"v_mfma_f32_16x16x32_bf16 v[116:119], a[160:163], a[28:31], v[116:119]\n"
"ds_read_b64_tr_b16 a[146:147], v29 offset:12288\n"
"v_mfma_f32_16x16x32_bf16 v[72:75], a[164:167], a[16:19], v[72:75]\n"
"ds_read_b64_tr_b16 a[148:149], v30 offset:8192\n"
"v_mfma_f32_16x16x32_bf16 v[88:91], a[164:167], a[20:23], v[88:91]\n"
"ds_read_b64_tr_b16 a[150:151], v30 offset:12288\n"
"v_mfma_f32_16x16x32_bf16 v[104:107], a[164:167], a[24:27], v[104:107]\n"
"ds_read_b64_tr_b16 a[152:153], v31 offset:8192\n"
"v_mfma_f32_16x16x32_bf16 v[120:123], a[164:167], a[28:31], v[120:123]\n"
"s_add_u32 m0, s84, 139264\n"
"s_waitcnt lgkmcnt(5)\n"
"ds_read_b64_tr_b16 a[154:155], v31 offset:12288\n"
"v_mfma_f32_16x16x32_bf16 v[76:79], a[168:171], a[16:19], v[76:79]\n"
"buffer_load_dwordx4 v14, s[68:71], s49 offen sc1 lds\n"
"v_mfma_f32_16x16x32_bf16 v[92:95], a[168:171], a[20:23], v[92:95]\n"
"buffer_load_dwordx4 v15, s[68:71], s49 offen offset:1024 sc1 lds\n"
"s_add_u32 m0, s84, 122880\n"
"v_mfma_f32_16x16x32_bf16 v[108:111], a[168:171], a[24:27], v[108:111]\n"
"s_add_u32 s49, s49, 1024\n"
"buffer_load_dwordx4 v10, s[76:79], s86 offen lds\n"
"v_mfma_f32_16x16x32_bf16 v[124:127], a[168:171], a[28:31], v[124:127]\n"
"buffer_load_dwordx4 v11, s[76:79], s86 offen offset:1024 lds\n"
"s_add_u32 s86, s86, 32768\n"
"s_cmp_eq_u32 s86, s30\n"
"v_mfma_f32_16x16x32_bf16 v[64:67], a[140:143], a[0:3], v[64:67]\n"
"s_cselect_b32 s42, 131072, 0\n"
"s_add_u32 s86, s86, s42\n"
"ds_read_b64_tr_b16 a[156:157], v16 offset:49152\n"
"v_mfma_f32_16x16x32_bf16 v[80:83], a[140:143], a[4:7], v[80:83]\n"
"ds_read_b64_tr_b16 a[158:159], v16 offset:53248\n"
"v_mfma_f32_16x16x32_bf16 v[96:99], a[140:143], a[8:11], v[96:99]\n"
"ds_read_b128 a[16:19], v20 offset:57344\n"
"v_mfma_f32_16x16x32_bf16 v[112:115], a[140:143], a[12:15], v[112:115]\n"
"s_waitcnt lgkmcnt(3)\n"
"ds_read_b128 a[20:23], v21 offset:57344\n"
"v_mfma_f32_16x16x32_bf16 v[68:71], a[144:147], a[0:3], v[68:71]\n"
"ds_read_b128 a[24:27], v22 offset:57344\n"
"v_mfma_f32_16x16x32_bf16 v[84:87], a[144:147], a[4:7], v[84:87]\n"
"ds_read_b128 a[28:31], v23 offset:57344\n"
"v_mfma_f32_16x16x32_bf16 v[100:103], a[144:147], a[8:11], v[100:103]\n"
"ds_read_b64_tr_b16 a[160:161], v17 offset:49152\n"
"v_mfma_f32_16x16x32_bf16 v[116:119], a[144:147], a[12:15], v[116:119]\n"
"ds_read_b64_tr_b16 a[162:163], v17 offset:53248\n"
"v_mfma_f32_16x16x32_bf16 v[72:75], a[148:151], a[0:3], v[72:75]\n"
"ds_read_b64_tr_b16 a[164:165], v18 offset:49152\n"
"v_mfma_f32_16x16x32_bf16 v[88:91], a[148:151], a[4:7], v[88:91]\n"
"ds_read_b64_tr_b16 a[166:167], v18 offset:53248\n"
"v_mfma_f32_16x16x32_bf16 v[104:107], a[148:151], a[8:11], v[104:107]\n"
"ds_read_b64_tr_b16 a[168:169], v19 offset:49152\n"
"v_mfma_f32_16x16x32_bf16 v[120:123], a[148:151], a[12:15], v[120:123]\n"
"s_add_u32 m0, s84, 24576\n"
"ds_read_b64_tr_b16 a[170:171], v19 offset:53248\n"
"v_mfma_f32_16x16x32_bf16 v[76:79], a[152:155], a[0:3], v[76:79]\n"
"buffer_load_dwordx4 v14, s[68:71], s49 offen sc1 lds\n"
"v_mfma_f32_16x16x32_bf16 v[92:95], a[152:155], a[4:7], v[92:95]\n"
"buffer_load_dwordx4 v15, s[68:71], s49 offen offset:1024 sc1 lds\n"
"s_add_u32 s49, s49, 1024\n"
"s_cmp_eq_u32 s49, s31\n"
"s_cselect_b32 s42, 4096, 0\n"
"v_mfma_f32_16x16x32_bf16 v[108:111], a[152:155], a[8:11], v[108:111]\n"
"s_add_u32 m0, s84, 147456\n"
"s_add_u32 s49, s49, s42\n"
"buffer_load_dwordx4 v10, s[76:79], s86 offen lds\n"
"v_mfma_f32_16x16x32_bf16 v[124:127], a[152:155], a[12:15], v[124:127]\n"
"buffer_load_dwordx4 v11, s[76:79], s86 offen offset:1024 lds\n"
"s_add_u32 s86, s86, 32768\n"
"s_waitcnt vmcnt(18)\n"
"s_barrier\n"
"s_waitcnt lgkmcnt(9)\n"
"v_mfma_f32_16x16x32_bf16 v[64:67], a[156:159], a[16:19], v[64:67]\n"
"s_waitcnt lgkmcnt(2)\n"
"ds_read_b64_tr_b16 a[140:141], v28 offset:16384\n"
"v_mfma_f32_16x16x32_bf16 v[80:83], a[156:159], a[20:23], v[80:83]\n"
"ds_read_b64_tr_b16 a[142:143], v28 offset:20480\n"
"v_mfma_f32_16x16x32_bf16 v[96:99], a[156:159], a[24:27], v[96:99]\n"
"ds_read_b128 a[0:3], v32 offset:24576\n"
"v_mfma_f32_16x16x32_bf16 v[112:115], a[156:159], a[28:31], v[112:115]\n"
"ds_read_b128 a[4:7], v33 offset:24576\n"
"v_mfma_f32_16x16x32_bf16 v[68:71], a[160:163], a[16:19], v[68:71]\n"
"ds_read_b128 a[8:11], v34 offset:24576\n"
"v_mfma_f32_16x16x32_bf16 v[84:87], a[160:163], a[20:23], v[84:87]\n"
"ds_read_b128 a[12:15], v35 offset:24576\n"
"v_mfma_f32_16x16x32_bf16 v[100:103], a[160:163], a[24:27], v[100:103]\n"
"ds_read_b64_tr_b16 a[144:145], v29 offset:16384\n"
"v_mfma_f32_16x16x32_bf16 v[116:119], a[160:163], a[28:31], v[116:119]\n"
"ds_read_b64_tr_b16 a[146:147], v29 offset:20480\n"
"v_mfma_f32_16x16x32_bf16 v[72:75], a[164:167], a[16:19], v[72:75]\n"
"ds_read_b64_tr_b16 a[148:149], v30 offset:16384\n"
"v_mfma_f32_16x16x32_bf16 v[88:91], a[164:167], a[20:23], v[88:91]\n"
"ds_read_b64_tr_b16 a[150:151], v30 offset:20480\n"
"v_mfma_f32_16x16x32_bf16 v[104:107], a[164:167], a[24:27], v[104:107]\n"
"ds_read_b64_tr_b16 a[152:153], v31 offset:16384\n"
"v_mfma_f32_16x16x32_bf16 v[120:123], a[164:167], a[28:31], v[120:123]\n"
"s_add_u32 m0, s84, 0\n"
"s_waitcnt lgkmcnt(5)\n"
"ds_read_b64_tr_b16 a[154:155], v31 offset:20480\n"
"v_mfma_f32_16x16x32_bf16 v[76:79], a[168:171], a[16:19], v[76:79]\n"
"buffer_load_dwordx4 v14, s[68:71], s49 offen sc1 lds\n"
"v_mfma_f32_16x16x32_bf16 v[92:95], a[168:171], a[20:23], v[92:95]\n"
"buffer_load_dwordx4 v15, s[68:71], s49 offen offset:1024 sc1 lds\n"
"s_add_u32 m0, s84, 155648\n"
"v_mfma_f32_16x16x32_bf16 v[108:111], a[168:171], a[24:27], v[108:111]\n"
"s_add_u32 s49, s49, 1024\n"
"buffer_load_dwordx4 v10, s[76:79], s86 offen lds\n"
"v_mfma_f32_16x16x32_bf16 v[124:127], a[168:171], a[28:31], v[124:127]\n"
"buffer_load_dwordx4 v11, s[76:79], s86 offen offset:1024 lds\n"
"s_add_u32 s86, s86, 32768\n"
"v_mfma_f32_16x16x32_bf16 v[64:67], a[140:143], a[0:3], v[64:67]\n"
"ds_read_b64_tr_b16 a[156:157], v28 offset:40960\n"
"v_mfma_f32_16x16x32_bf16 v[80:83], a[140:143], a[4:7], v[80:83]\n"
"ds_read_b64_tr_b16 a[158:159], v28 offset:45056\n"
"v_mfma_f32_16x16x32_bf16 v[96:99], a[140:143], a[8:11], v[96:99]\n"
"ds_read_b128 a[16:19], v32 offset:32768\n"
"v_mfma_f32_16x16x32_bf16 v[112:115], a[140:143], a[12:15], v[112:115]\n"
"s_waitcnt lgkmcnt(3)\n"
"ds_read_b128 a[20:23], v33 offset:32768\n"
"v_mfma_f32_16x16x32_bf16 v[68:71], a[144:147], a[0:3], v[68:71]\n"
"ds_read_b128 a[24:27], v34 offset:32768\n"
"v_mfma_f32_16x16x32_bf16 v[84:87], a[144:147], a[4:7], v[84:87]\n"
"ds_read_b128 a[28:31], v35 offset:32768\n"
"v_mfma_f32_16x16x32_bf16 v[100:103], a[144:147], a[8:11], v[100:103]\n"
"ds_read_b64_tr_b16 a[160:161], v29 offset:40960\n"
"v_mfma_f32_16x16x32_bf16 v[116:119], a[144:147], a[12:15], v[116:119]\n"
"ds_read_b64_tr_b16 a[162:163], v29 offset:45056\n"
"v_mfma_f32_16x16x32_bf16 v[72:75], a[148:151], a[0:3], v[72:75]\n"
"ds_read_b64_tr_b16 a[164:165], v30 offset:40960\n"
"v_mfma_f32_16x16x32_bf16 v[88:91], a[148:151], a[4:7], v[88:91]\n"
"ds_read_b64_tr_b16 a[166:167], v30 offset:45056\n"
"v_mfma_f32_16x16x32_bf16 v[104:107], a[148:151], a[8:11], v[104:107]\n"
"ds_read_b64_tr_b16 a[168:169], v31 offset:40960\n"
"v_mfma_f32_16x16x32_bf16 v[120:123], a[148:151], a[12:15], v[120:123]\n"
"s_add_u32 m0, s84, 8192\n"
"ds_read_b64_tr_b16 a[170:171], v31 offset:45056\n"
"v_mfma_f32_16x16x32_bf16 v[76:79], a[152:155], a[0:3], v[76:79]\n"
"buffer_load_dwordx4 v14, s[68:71], s49 offen sc1 lds\n"
"v_mfma_f32_16x16x32_bf16 v[92:95], a[152:155], a[4:7], v[92:95]\n"
"buffer_load_dwordx4 v15, s[68:71], s49 offen offset:1024 sc1 lds\n"
"s_add_u32 m0, s84, 65536\n"
"v_mfma_f32_16x16x32_bf16 v[108:111], a[152:155], a[8:11], v[108:111]\n"
"s_add_u32 s49, s49, 1024\n"
"buffer_load_dwordx4 v10, s[76:79], s86 offen lds\n"
"v_mfma_f32_16x16x32_bf16 v[124:127], a[152:155], a[12:15], v[124:127]\n"
"buffer_load_dwordx4 v11, s[76:79], s86 offen offset:1024 lds\n"
"s_add_u32 s86, s86, 32768\n"
"s_waitcnt vmcnt(14)\n"
"s_barrier\n"
"s_waitcnt lgkmcnt(9)\n"
"v_mfma_f32_16x16x32_bf16 v[64:67], a[156:159], a[16:19], v[64:67]\n"
"s_waitcnt lgkmcnt(2)\n"
"ds_read_b64_tr_b16 a[140:141], v28 offset:49152\n"
"v_mfma_f32_16x16x32_bf16 v[80:83], a[156:159], a[20:23], v[80:83]\n"
"ds_read_b64_tr_b16 a[142:143], v28 offset:53248\n"
"v_mfma_f32_16x16x32_bf16 v[96:99], a[156:159], a[24:27], v[96:99]\n"
"ds_read_b128 a[0:3], v40 offset:0\n"
"v_mfma_f32_16x16x32_bf16 v[112:115], a[156:159], a[28:31], v[112:115]\n"
"ds_read_b128 a[4:7], v41 offset:0\n"
"v_mfma_f32_16x16x32_bf16 v[68:71], a[160:163], a[16:19], v[68:71]\n"
"ds_read_b128 a[8:11], v42 offset:0\n"
"v_mfma_f32_16x16x32_bf16 v[84:87], a[160:163], a[20:23], v[84:87]\n"
"ds_read_b128 a[12:15], v43 offset:0\n"
"v_mfma_f32_16x16x32_bf16 v[100:103], a[160:163], a[24:27], v[100:103]\n"
"ds_read_b64_tr_b16 a[144:145], v29 offset:49152\n"
"v_mfma_f32_16x16x32_bf16 v[116:119], a[160:163], a[28:31], v[116:119]\n"
"ds_read_b64_tr_b16 a[146:147], v29 offset:53248\n"
"v_mfma_f32_16x16x32_bf16 v[72:75], a[164:167], a[16:19], v[72:75]\n"
"ds_read_b64_tr_b16 a[148:149], v30 offset:49152\n"
"v_mfma_f32_16x16x32_bf16 v[88:91], a[164:167], a[20:23], v[88:91]\n"
"ds_read_b64_tr_b16 a[150:151], v30 offset:53248\n"
"v_mfma_f32_16x16x32_bf16 v[104:107], a[164:167], a[24:27], v[104:107]\n"
"ds_read_b64_tr_b16 a[152:153], v31 offset:49152\n"
"v_mfma_f32_16x16x32_bf16 v[120:123], a[164:167], a[28:31], v[120:123]\n"
"s_add_u32 m0, s84, 32768\n"
"s_waitcnt lgkmcnt(5)\n"
"ds_read_b64_tr_b16 a[154:155], v31 offset:53248\n"
"v_mfma_f32_16x16x32_bf16 v[76:79], a[168:171], a[16:19], v[76:79]\n"
"buffer_load_dwordx4 v14, s[68:71], s49 offen sc1 lds\n"
"v_mfma_f32_16x16x32_bf16 v[92:95], a[168:171], a[20:23], v[92:95]\n"
"buffer_load_dwordx4 v15, s[68:71], s49 offen offset:1024 sc1 lds\n"
"s_add_u32 m0, s84, 73728\n"
"v_mfma_f32_16x16x32_bf16 v[108:111], a[168:171], a[24:27], v[108:111]\n"
"s_add_u32 s49, s49, 1024\n"
"buffer_load_dwordx4 v10, s[76:79], s86 offen lds\n"
"v_mfma_f32_16x16x32_bf16 v[124:127], a[168:171], a[28:31], v[124:127]\n"
"buffer_load_dwordx4 v11, s[76:79], s86 offen offset:1024 lds\n"
"s_add_u32 s86, s86, 32768\n"
"s_cmp_eq_u32 s86, s30\n"
"v_mfma_f32_16x16x32_bf16 v[64:67], a[140:143], a[0:3], v[64:67]\n"
"s_cselect_b32 s42, 131072, 0\n"
"s_add_u32 s86, s86, s42\n"
"ds_read_b64_tr_b16 a[156:157], v16 offset:16384\n"
"v_mfma_f32_16x16x32_bf16 v[80:83], a[140:143], a[4:7], v[80:83]\n"
"ds_read_b64_tr_b16 a[158:159], v16 offset:20480\n"
"v_mfma_f32_16x16x32_bf16 v[96:99], a[140:143], a[8:11], v[96:99]\n"
"ds_read_b128 a[16:19], v40 offset:8192\n"
"v_mfma_f32_16x16x32_bf16 v[112:115], a[140:143], a[12:15], v[112:115]\n"
"s_waitcnt lgkmcnt(3)\n"
"ds_read_b128 a[20:23], v41 offset:8192\n"
"v_mfma_f32_16x16x32_bf16 v[68:71], a[144:147], a[0:3], v[68:71]\n"
"ds_read_b128 a[24:27], v42 offset:8192\n"
"v_mfma_f32_16x16x32_bf16 v[84:87], a[144:147], a[4:7], v[84:87]\n"
"ds_read_b128 a[28:31], v43 offset:8192\n"
"v_mfma_f32_16x16x32_bf16 v[100:103], a[144:147], a[8:11], v[100:103]\n"
"ds_read_b64_tr_b16 a[160:161], v17 offset:16384\n"
"v_mfma_f32_16x16x32_bf16 v[116:119], a[144:147], a[12:15], v[116:119]\n"
"ds_read_b64_tr_b16 a[162:163], v17 offset:20480\n"
"v_mfma_f32_16x16x32_bf16 v[72:75], a[148:151], a[0:3], v[72:75]\n"
"ds_read_b64_tr_b16 a[164:165], v18 offset:16384\n"
"v_mfma_f32_16x16x32_bf16 v[88:91], a[148:151], a[4:7], v[88:91]\n"
"ds_read_b64_tr_b16 a[166:167], v18 offset:20480\n"
"v_mfma_f32_16x16x32_bf16 v[104:107], a[148:151], a[8:11], v[104:107]\n"
"ds_read_b64_tr_b16 a[168:169], v19 offset:16384\n"
"v_mfma_f32_16x16x32_bf16 v[120:123], a[148:151], a[12:15], v[120:123]\n"
"s_add_u32 m0, s84, 40960\n"
"ds_read_b64_tr_b16 a[170:171], v19 offset:20480\n"
"v_mfma_f32_16x16x32_bf16 v[76:79], a[152:155], a[0:3], v[76:79]\n"
"buffer_load_dwordx4 v14, s[68:71], s49 offen sc1 lds\n"
"v_mfma_f32_16x16x32_bf16 v[92:95], a[152:155], a[4:7], v[92:95]\n"
"buffer_load_dwordx4 v15, s[68:71], s49 offen offset:1024 sc1 lds\n"
"s_add_u32 s49, s49, 1024\n"
"s_cmp_eq_u32 s49, s31\n"
"s_cselect_b32 s42, 4096, 0\n"
"v_mfma_f32_16x16x32_bf16 v[108:111], a[152:155], a[8:11], v[108:111]\n"
"s_add_u32 m0, s84, 49152\n"
"s_add_u32 s49, s49, s42\n"
"buffer_load_dwordx4 v10, s[76:79], s86 offen lds\n"
"v_mfma_f32_16x16x32_bf16 v[124:127], a[152:155], a[12:15], v[124:127]\n"
"buffer_load_dwordx4 v11, s[76:79], s86 offen offset:1024 lds\n"
"s_add_u32 s86, s86, 32768\n"
"s_waitcnt vmcnt(14)\n"
"s_barrier\n"
"s_waitcnt lgkmcnt(9)\n"
"v_mfma_f32_16x16x32_bf16 v[64:67], a[156:159], a[16:19], v[64:67]\n"
"s_waitcnt lgkmcnt(2)\n"
"ds_read_b64_tr_b16 a[140:141], v28 offset:57344\n"
"v_mfma_f32_16x16x32_bf16 v[80:83], a[156:159], a[20:23], v[80:83]\n"
"ds_read_b64_tr_b16 a[142:143], v28 offset:61440\n"
"v_mfma_f32_16x16x32_bf16 v[96:99], a[156:159], a[24:27], v[96:99]\n"
"ds_read_b128 a[0:3], v20 offset:24576\n"
"v_mfma_f32_16x16x32_bf16 v[112:115], a[156:159], a[28:31], v[112:115]\n"
"ds_read_b128 a[4:7], v21 offset:24576\n"
"v_mfma_f32_16x16x32_bf16 v[68:71], a[160:163], a[16:19], v[68:71]\n"
"ds_read_b128 a[8:11], v22 offset:24576\n"
"v_mfma_f32_16x16x32_bf16 v[84:87], a[160:163], a[20:23], v[84:87]\n"
"ds_read_b128 a[12:15], v23 offset:24576\n"
"v_mfma_f32_16x16x32_bf16 v[100:103], a[160:163], a[24:27], v[100:103]\n"
"ds_read_b64_tr_b16 a[144:145], v29 offset:57344\n"
"v_mfma_f32_16x16x32_bf16 v[116:119], a[160:163], a[28:31], v[116:119]\n"
"ds_read_b64_tr_b16 a[146:147], v29 offset:61440\n"
"v_mfma_f32_16x16x32_bf16 v[72:75], a[164:167], a[16:19], v[72:75]\n"
"ds_read_b64_tr_b16 a[148:149], v30 offset:57344\n"
"v_mfma_f32_16x16x32_bf16 v[88:91], a[164:167], a[20:23], v[88:91]\n"
"ds_read_b64_tr_b16 a[150:151], v30 offset:61440\n"
"v_mfma_f32_16x16x32_bf16 v[104:107], a[164:167], a[24:27], v[104:107]\n"
"ds_read_b64_tr_b16 a[152:153], v31 offset:57344\n"
"v_mfma_f32_16x16x32_bf16 v[120:123], a[164:167], a[28:31], v[120:123]\n"
"s_add_u32 m0, s84, 57344\n"
"s_waitcnt lgkmcnt(5)\n"
"ds_read_b64_tr_b16 a[154:155], v31 offset:61440\n"
"v_mfma_f32_16x16x32_bf16 v[76:79], a[168:171], a[16:19], v[76:79]\n"
"buffer_load_dwordx4 v14, s[68:71], s49 offen sc1 lds\n"
"v_mfma_f32_16x16x32_bf16 v[92:95], a[168:171], a[20:23], v[92:95]\n"
"buffer_load_dwordx4 v15, s[68:71], s49 offen offset:1024 sc1 lds\n"
"s_add_u32 m0, s84, 81920\n"
"v_mfma_f32_16x16x32_bf16 v[108:111], a[168:171], a[24:27], v[108:111]\n"
"s_add_u32 s49, s49, 1024\n"
"buffer_load_dwordx4 v10, s[76:79], s86 offen lds\n"
"v_mfma_f32_16x16x32_bf16 v[124:127], a[168:171], a[28:31], v[124:127]\n"
"buffer_load_dwordx4 v11, s[76:79], s86 offen offset:1024 lds\n"
"s_add_u32 s86, s86, 32768\n"
"v_mfma_f32_16x16x32_bf16 v[64:67], a[140:143], a[0:3], v[64:67]\n"
"ds_read_b64_tr_b16 a[32:33], v36 offset:16384\n"
"v_mfma_f32_16x16x32_bf16 v[80:83], a[140:143], a[4:7], v[80:83]\n"
"ds_read_b64_tr_b16 a[34:35], v36 offset:20480\n"
"v_mfma_f32_16x16x32_bf16 v[96:99], a[140:143], a[8:11], v[96:99]\n"
"ds_read_b128 a[204:207], v20 offset:0\n"
"v_mfma_f32_16x16x32_bf16 v[112:115], a[140:143], a[12:15], v[112:115]\n"
"s_waitcnt lgkmcnt(3)\n"
"ds_read_b128 a[20:23], v21 offset:0\n"
"v_mfma_f32_16x16x32_bf16 v[68:71], a[144:147], a[0:3], v[68:71]\n"
"ds_read_b128 a[24:27], v22 offset:0\n"
"v_mfma_f32_16x16x32_bf16 v[84:87], a[144:147], a[4:7], v[84:87]\n"
"ds_read_b128 a[28:31], v23 offset:0\n"
"v_mfma_f32_16x16x32_bf16 v[100:103], a[144:147], a[8:11], v[100:103]\n"
"ds_read_b64_tr_b16 a[36:37], v37 offset:16384\n"
"v_mfma_f32_16x16x32_bf16 v[116:119], a[144:147], a[12:15], v[116:119]\n"
"ds_read_b64_tr_b16 a[38:39], v37 offset:20480\n"
"v_mfma_f32_16x16x32_bf16 v[72:75], a[148:151], a[0:3], v[72:75]\n"
"ds_read_b64_tr_b16 a[40:41], v38 offset:16384\n"
"v_mfma_f32_16x16x32_bf16 v[88:91], a[148:151], a[4:7], v[88:91]\n"
"ds_read_b64_tr_b16 a[42:43], v38 offset:20480\n"
"v_mfma_f32_16x16x32_bf16 v[104:107], a[148:151], a[8:11], v[104:107]\n"
"ds_read_b64_tr_b16 a[44:45], v39 offset:16384\n"
"v_mfma_f32_16x16x32_bf16 v[120:123], a[148:151], a[12:15], v[120:123]\n"
"s_add_u32 m0, s84, 90112\n"
"ds_read_b64_tr_b16 a[46:47], v39 offset:20480\n"
"v_mfma_f32_16x16x32_bf16 v[76:79], a[152:155], a[0:3], v[76:79]\n"
"buffer_load_dwordx4 v14, s[68:71], s49 offen sc1 lds\n"
"v_mfma_f32_16x16x32_bf16 v[92:95], a[152:155], a[4:7], v[92:95]\n"
"buffer_load_dwordx4 v15, s[68:71], s49 offen offset:1024 sc1 lds\n"
"s_add_u32 m0, s84, 106496\n"
"v_mfma_f32_16x16x32_bf16 v[108:111], a[152:155], a[8:11], v[108:111]\n"
"s_add_u32 s49, s49, 1024\n"
"buffer_load_dwordx4 v10, s[76:79], s86 offen lds\n"
"v_mfma_f32_16x16x32_bf16 v[124:127], a[152:155], a[12:15], v[124:127]\n"
"buffer_load_dwordx4 v11, s[76:79], s86 offen offset:1024 lds\n"
"s_add_u32 s86, s86, 32768\n"
"s_waitcnt vmcnt(14)\n"
"s_barrier\n"
"s_waitcnt lgkmcnt(8)\n"
"v_mfma_f32_16x16x32_bf16 v[80:83], a[32:35], a[20:23], v[80:83]\n"
"s_waitcnt lgkmcnt(0)\n"
"ds_read_b64_tr_b16 a[48:49], v36 offset:24576\n"
"v_mfma_f32_16x16x32_bf16 v[96:99], a[32:35], a[24:27], v[96:99]\n"
"ds_read_b64_tr_b16 a[50:51], v36 offset:28672\n"
"v_mfma_f32_16x16x32_bf16 v[112:115], a[32:35], a[28:31], v[112:115]\n"
"ds_read_b128 a[208:211], v20 offset:8192\n"
"v_mfma_f32_16x16x32_bf16 v[84:87], a[36:39], a[20:23], v[84:87]\n"
"ds_read_b128 a[4:7], v21 offset:8192\n"
"v_mfma_f32_16x16x32_bf16 v[100:103], a[36:39], a[24:27], v[100:103]\n"
"s_add_u32 m0, s84, 98304\n"
"ds_read_b128 a[8:11], v22 offset:8192\n"
"buffer_load_dwordx4 v14, s[68:71], s49 offen sc1 lds\n"
"v_mfma_f32_16x16x32_bf16 v[116:119], a[36:39], a[28:31], v[116:119]\n"
"ds_read_b128 a[12:15], v23 offset:8192\n"
"v_mfma_f32_16x16x32_bf16 v[88:91], a[40:43], a[20:23], v[88:91]\n"
"ds_read_b64_tr_b16 a[52:53], v37 offset:24576\n"
"buffer_load_dwordx4 v15, s[68:71], s49 offen offset:1024 sc1 lds\n"
"v_mfma_f32_16x16x32_bf16 v[104:107], a[40:43], a[24:27], v[104:107]\n"
"s_add_u32 s49, s49, 1024\n"
"ds_read_b64_tr_b16 a[54:55], v37 offset:28672\n"
"v_mfma_f32_16x16x32_bf16 v[120:123], a[40:43], a[28:31], v[120:123]\n"
"s_add_u32 m0, s84, 114688\n"
"ds_read_b64_tr_b16 a[56:57], v38 offset:24576\n"
"buffer_load_dwordx4 v10, s[76:79], s86 offen lds\n"
"v_mfma_f32_16x16x32_bf16 v[92:95], a[44:47], a[20:23], v[92:95]\n"
"ds_read_b64_tr_b16 a[58:59], v38 offset:28672\n"
"v_mfma_f32_16x16x32_bf16 v[108:111], a[44:47], a[24:27], v[108:111]\n"
"ds_read_b64_tr_b16 a[60:61], v39 offset:24576\n"
"buffer_load_dwordx4 v11, s[76:79], s86 offen offset:1024 lds\n"
"s_add_u32 s86, s86, 32768\n"
"s_cmp_eq_u32 s86, s30\n"
"s_cselect_b32 s42, 131072, 0\n"
"v_mfma_f32_16x16x32_bf16 v[124:127], a[44:47], a[28:31], v[124:127]\n"
"s_add_u32 s86, s86, s42\n"
"s_waitcnt lgkmcnt(5)\n"
"ds_read_b64_tr_b16 a[62:63], v39 offset:28672\n"
"v_mfma_f32_16x16x32_bf16 v[80:83], a[48:51], a[4:7], v[80:83]\n"
"ds_read_b64_tr_b16 a[64:65], v28 offset:0\n"
"v_mfma_f32_16x16x32_bf16 v[96:99], a[48:51], a[8:11], v[96:99]\n"
"ds_read_b64_tr_b16 a[66:67], v28 offset:4096\n"
"v_mfma_f32_16x16x32_bf16 v[112:115], a[48:51], a[12:15], v[112:115]\n"
"s_waitcnt lgkmcnt(4)\n"
"ds_read_b128 a[212:215], v20 offset:32768\n"
"v_mfma_f32_16x16x32_bf16 v[84:87], a[52:55], a[4:7], v[84:87]\n"
"ds_read_b128 a[20:23], v21 offset:32768\n"
"v_mfma_f32_16x16x32_bf16 v[100:103], a[52:55], a[8:11], v[100:103]\n"
"ds_read_b128 a[24:27], v22 offset:32768\n"
"v_mfma_f32_16x16x32_bf16 v[116:119], a[52:55], a[12:15], v[116:119]\n"
"ds_read_b128 a[28:31], v23 offset:32768\n"
"v_mfma_f32_16x16x32_bf16 v[88:91], a[56:59], a[4:7], v[88:91]\n"
"ds_read_b64_tr_b16 a[68:69], v29 offset:0\n"
"v_mfma_f32_16x16x32_bf16 v[104:107], a[56:59], a[8:11], v[104:107]\n"
"s_add_u32 m0, s84, 131072\n"
"ds_read_b64_tr_b16 a[70:71], v29 offset:4096\n"
"v_mfma_f32_16x16x32_bf16 v[120:123], a[56:59], a[12:15], v[120:123]\n"
"s_waitcnt lgkmcnt(8)\n"
"ds_read_b64_tr_b16 a[72:73], v30 offset:0\n"
"buffer_load_dwordx4 v14, s[68:71], s49 offen sc1 lds\n"
"v_mfma_f32_16x16x32_bf16 v[92:95], a[60:63], a[4:7], v[92:95]\n"
"ds_read_b64_tr_b16 a[74:75], v30 offset:4096\n"
"v_mfma_f32_16x16x32_bf16 v[108:111], a[60:63], a[8:11], v[108:111]\n"
"ds_read_b64_tr_b16 a[76:77], v31 offset:0\n"
"buffer_load_dwordx4 v15, s[68:71], s49 offen offset:1024 sc1 lds\n"
"s_add_u32 s49, s49, 1024\n"
"s_cmp_eq_u32 s49, s31\n"
"v_mfma_f32_16x16x32_bf16 v[124:127], a[60:63], a[12:15], v[124:127]\n"
"s_cselect_b32 s42, 4096, 0\n"
"s_add_u32 s49, s49, s42\n"
"ds_read_b64_tr_b16 a[78:79], v31 offset:4096\n"
"s_waitcnt vmcnt(12)\n"
"s_barrier\n"
"s_waitcnt lgkmcnt(8)\n"
"v_mfma_f32_16x16x32_bf16 v[80:83], a[64:67], a[20:23], v[80:83]\n"
"s_waitcnt lgkmcnt(0)\n"
"ds_read_b64_tr_b16 a[80:81], v28 offset:8192\n"
"v_mfma_f32_16x16x32_bf16 v[96:99], a[64:67], a[24:27], v[96:99]\n"
"ds_read_b64_tr_b16 a[82:83], v28 offset:12288\n"
"v_mfma_f32_16x16x32_bf16 v[112:115], a[64:67], a[28:31], v[112:115]\n"
"ds_read_b128 a[216:219], v20 offset:40960\n"
"v_mfma_f32_16x16x32_bf16 v[84:87], a[68:71], a[20:23], v[84:87]\n"
"ds_read_b128 a[4:7], v21 offset:40960\n"
"v_mfma_f32_16x16x32_bf16 v[100:103], a[68:71], a[24:27], v[100:103]\n"
"ds_read_b128 a[8:11], v22 offset:40960\n"
"v_mfma_f32_16x16x32_bf16 v[116:119], a[68:71], a[28:31], v[116:119]\n"
"ds_read_b128 a[12:15], v23 offset:40960\n"
"v_mfma_f32_16x16x32_bf16 v[88:91], a[72:75], a[20:23], v[88:91]\n"
"ds_read_b64_tr_b16 a[84:85], v29 offset:8192\n"
"v_mfma_f32_16x16x32_bf16 v[104:107], a[72:75], a[24:27], v[104:107]\n"
"ds_read_b64_tr_b16 a[86:87], v29 offset:12288\n"
"v_mfma_f32_16x16x32_bf16 v[120:123], a[72:75], a[28:31], v[120:123]\n"
"ds_read_b64_tr_b16 a[88:89], v30 offset:8192\n"
"v_mfma_f32_16x16x32_bf16 v[92:95], a[76:79], a[20:23], v[92:95]\n"
"ds_read_b64_tr_b16 a[90:91], v30 offset:12288\n"
"v_mfma_f32_16x16x32_bf16 v[108:111], a[76:79], a[24:27], v[108:111]\n"
"ds_read_b64_tr_b16 a[92:93], v31 offset:8192\n"
"v_mfma_f32_16x16x32_bf16 v[124:127], a[76:79], a[28:31], v[124:127]\n"
"s_waitcnt lgkmcnt(5)\n"
"ds_read_b64_tr_b16 a[94:95], v31 offset:12288\n"
"v_mfma_f32_16x16x32_bf16 v[80:83], a[80:83], a[4:7], v[80:83]\n"
"ds_read_b64_tr_b16 a[96:97], v16 offset:49152\n"
"v_mfma_f32_16x16x32_bf16 v[96:99], a[80:83], a[8:11], v[96:99]\n"
"ds_read_b64_tr_b16 a[98:99], v16 offset:53248\n"
"v_mfma_f32_16x16x32_bf16 v[112:115], a[80:83], a[12:15], v[112:115]\n"
"s_waitcnt lgkmcnt(4)\n"
"ds_read_b128 a[220:223], v20 offset:57344\n"
"v_mfma_f32_16x16x32_bf16 v[84:87], a[84:87], a[4:7], v[84:87]\n"
"ds_read_b128 a[20:23], v21 offset:57344\n"
"v_mfma_f32_16x16x32_bf16 v[100:103], a[84:87], a[8:11], v[100:103]\n"
"ds_read_b128 a[24:27], v22 offset:57344\n"
"v_mfma_f32_16x16x32_bf16 v[116:119], a[84:87], a[12:15], v[116:119]\n"
"ds_read_b128 a[28:31], v23 offset:57344\n"
"v_mfma_f32_16x16x32_bf16 v[88:91], a[88:91], a[4:7], v[88:91]\n"
"ds_read_b64_tr_b16 a[100:101], v17 offset:49152\n"
"v_mfma_f32_16x16x32_bf16 v[104:107], a[88:91], a[8:11], v[104:107]\n"
"ds_read_b64_tr_b16 a[102:103], v17 offset:53248\n"
"v_mfma_f32_16x16x32_bf16 v[120:123], a[88:91], a[12:15], v[120:123]\n"
"s_waitcnt lgkmcnt(8)\n"
"ds_read_b64_tr_b16 a[104:105], v18 offset:49152\n"
"v_mfma_f32_16x16x32_bf16 v[92:95], a[92:95], a[4:7], v[92:95]\n"
"ds_read_b64_tr_b16 a[106:107], v18 offset:53248\n"
"v_mfma_f32_16x16x32_bf16 v[108:111], a[92:95], a[8:11], v[108:111]\n"
"ds_read_b64_tr_b16 a[108:109], v19 offset:49152\n"
"v_mfma_f32_16x16x32_bf16 v[124:127], a[92:95], a[12:15], v[124:127]\n"
"ds_read_b64_tr_b16 a[110:111], v19 offset:53248\n"
"s_waitcnt vmcnt(4)\n"
"s_barrier\n"
"s_waitcnt lgkmcnt(8)\n"
"v_mfma_f32_16x16x32_bf16 v[80:83], a[96:99], a[20:23], v[80:83]\n"
"s_waitcnt lgkmcnt(0)\n"
"ds_read_b64_tr_b16 a[112:113], v28 offset:16384\n"
"v_mfma_f32_16x16x32_bf16 v[96:99], a[96:99], a[24:27], v[96:99]\n"
"ds_read_b64_tr_b16 a[114:115], v28 offset:20480\n"
"v_mfma_f32_16x16x32_bf16 v[112:115], a[96:99], a[28:31], v[112:115]\n"
"ds_read_b128 a[224:227], v32 offset:24576\n"
"v_mfma_f32_16x16x32_bf16 v[84:87], a[100:103], a[20:23], v[84:87]\n"
"ds_read_b128 a[4:7], v33 offset:24576\n"
"v_mfma_f32_16x16x32_bf16 v[100:103], a[100:103], a[24:27], v[100:103]\n"
"ds_read_b128 a[8:11], v34 offset:24576\n"
"v_mfma_f32_16x16x32_bf16 v[116:119], a[100:103], a[28:31], v[116:119]\n"
"ds_read_b128 a[12:15], v35 offset:24576\n"
"v_mfma_f32_16x16x32_bf16 v[88:91], a[104:107], a[20:23], v[88:91]\n"
"ds_read_b64_tr_b16 a[116:117], v29 offset:16384\n"
"v_mfma_f32_16x16x32_bf16 v[104:107], a[104:107], a[24:27], v[104:107]\n"
"ds_read_b64_tr_b16 a[118:119], v29 offset:20480\n"
"v_mfma_f32_16x16x32_bf16 v[120:123], a[104:107], a[28:31], v[120:123]\n"
"ds_read_b64_tr_b16 a[120:121], v30 offset:16384\n"
"v_mfma_f32_16x16x32_bf16 v[92:95], a[108:111], a[20:23], v[92:95]\n"
"ds_read_b64_tr_b16 a[122:123], v30 offset:20480\n"
"v_mfma_f32_16x16x32_bf16 v[108:111], a[108:111], a[24:27], v[108:111]\n"
"ds_read_b64_tr_b16 a[124:125], v31 offset:16384\n"
"v_mfma_f32_16x16x32_bf16 v[124:127], a[108:111], a[28:31], v[124:127]\n"
"s_waitcnt lgkmcnt(5)\n"
"ds_read_b64_tr_b16 a[126:127], v31 offset:20480\n"
"v_mfma_f32_16x16x32_bf16 v[80:83], a[112:115], a[4:7], v[80:83]\n"
"ds_read_b64_tr_b16 a[172:173], v28 offset:40960\n"
"v_mfma_f32_16x16x32_bf16 v[96:99], a[112:115], a[8:11], v[96:99]\n"
"ds_read_b64_tr_b16 a[174:175], v28 offset:45056\n"
"v_mfma_f32_16x16x32_bf16 v[112:115], a[112:115], a[12:15], v[112:115]\n"
"s_waitcnt lgkmcnt(4)\n"
"ds_read_b128 a[228:231], v32 offset:32768\n"
"v_mfma_f32_16x16x32_bf16 v[84:87], a[116:119], a[4:7], v[84:87]\n"
"ds_read_b128 a[20:23], v33 offset:32768\n"
"v_mfma_f32_16x16x32_bf16 v[100:103], a[116:119], a[8:11], v[100:103]\n"
"ds_read_b128 a[24:27], v34 offset:32768\n"
"v_mfma_f32_16x16x32_bf16 v[116:119], a[116:119], a[12:15], v[116:119]\n"
"ds_read_b128 a[28:31], v35 offset:32768\n"
"v_mfma_f32_16x16x32_bf16 v[88:91], a[120:123], a[4:7], v[88:91]\n"
"ds_read_b64_tr_b16 a[176:177], v29 offset:40960\n"
"v_mfma_f32_16x16x32_bf16 v[104:107], a[120:123], a[8:11], v[104:107]\n"
"ds_read_b64_tr_b16 a[178:179], v29 offset:45056\n"
"v_mfma_f32_16x16x32_bf16 v[120:123], a[120:123], a[12:15], v[120:123]\n"
"s_waitcnt lgkmcnt(8)\n"
"ds_read_b64_tr_b16 a[180:181], v30 offset:40960\n"
"v_mfma_f32_16x16x32_bf16 v[92:95], a[124:127], a[4:7], v[92:95]\n"
"ds_read_b64_tr_b16 a[182:183], v30 offset:45056\n"
"v_mfma_f32_16x16x32_bf16 v[108:111], a[124:127], a[8:11], v[108:111]\n"
"ds_read_b64_tr_b16 a[184:185], v31 offset:40960\n"
"v_mfma_f32_16x16x32_bf16 v[124:127], a[124:127], a[12:15], v[124:127]\n"
"ds_read_b64_tr_b16 a[186:187], v31 offset:45056\n"
"s_waitcnt vmcnt(0)\n"
"s_barrier\n"
"s_waitcnt lgkmcnt(8)\n"
"v_mfma_f32_16x16x32_bf16 v[80:83], a[172:175], a[20:23], v[80:83]\n"
"s_waitcnt lgkmcnt(0)\n"
"ds_read_b64_tr_b16 a[188:189], v28 offset:49152\n"
"v_mfma_f32_16x16x32_bf16 v[96:99], a[172:175], a[24:27], v[96:99]\n"
"ds_read_b64_tr_b16 a[190:191], v28 offset:53248\n"
"v_mfma_f32_16x16x32_bf16 v[112:115], a[172:175], a[28:31], v[112:115]\n"
"ds_read_b128 a[232:235], v40 offset:0\n"
"v_mfma_f32_16x16x32_bf16 v[84:87], a[176:179], a[20:23], v[84:87]\n"
"ds_read_b128 a[4:7], v41 offset:0\n"
"v_mfma_f32_16x16x32_bf16 v[100:103], a[176:179], a[24:27], v[100:103]\n"
"ds_read_b128 a[8:11], v42 offset:0\n"
"v_mfma_f32_16x16x32_bf16 v[116:119], a[176:179], a[28:31], v[116:119]\n"
"ds_read_b128 a[12:15], v43 offset:0\n"
"v_mfma_f32_16x16x32_bf16 v[88:91], a[180:183], a[20:23], v[88:91]\n"
"ds_read_b64_tr_b16 a[192:193], v29 offset:49152\n"
"buffer_load_dwordx4 v[144:147], v4, s[64:67], s32 offen offset:0 sc1\n"
"v_mfma_f32_16x16x32_bf16 v[104:107], a[180:183], a[24:27], v[104:107]\n"
"ds_read_b64_tr_b16 a[194:195], v29 offset:53248\n"
"v_mfma_f32_16x16x32_bf16 v[120:123], a[180:183], a[28:31], v[120:123]\n"
"ds_read_b64_tr_b16 a[196:197], v30 offset:49152\n"
"buffer_load_dwordx4 v[148:151], v4, s[64:67], s32 offen offset:1024 sc1\n"
"v_mfma_f32_16x16x32_bf16 v[92:95], a[184:187], a[20:23], v[92:95]\n"
"ds_read_b64_tr_b16 a[198:199], v30 offset:53248\n"
"v_mfma_f32_16x16x32_bf16 v[108:111], a[184:187], a[24:27], v[108:111]\n"
"ds_read_b64_tr_b16 a[200:201], v31 offset:49152\n"
"buffer_load_dwordx4 v[152:155], v4, s[64:67], s32 offen offset:2048 sc1\n"
"v_mfma_f32_16x16x32_bf16 v[124:127], a[184:187], a[28:31], v[124:127]\n"
"s_waitcnt lgkmcnt(5)\n"
"ds_read_b64_tr_b16 a[202:203], v31 offset:53248\n"
"v_mfma_f32_16x16x32_bf16 v[80:83], a[188:191], a[4:7], v[80:83]\n"
"v_mfma_f32_16x16x32_bf16 v[96:99], a[188:191], a[8:11], v[96:99]\n"
"v_mfma_f32_16x16x32_bf16 v[112:115], a[188:191], a[12:15], v[112:115]\n"
"s_waitcnt lgkmcnt(2)\n"
"v_mfma_f32_16x16x32_bf16 v[84:87], a[192:195], a[4:7], v[84:87]\n"
"v_mfma_f32_16x16x32_bf16 v[100:103], a[192:195], a[8:11], v[100:103]\n"
"v_mfma_f32_16x16x32_bf16 v[116:119], a[192:195], a[12:15], v[116:119]\n"
"v_mfma_f32_16x16x32_bf16 v[88:91], a[196:199], a[4:7], v[88:91]\n"
"v_mfma_f32_16x16x32_bf16 v[104:107], a[196:199], a[8:11], v[104:107]\n"
"v_mfma_f32_16x16x32_bf16 v[120:123], a[196:199], a[12:15], v[120:123]\n"
"s_waitcnt lgkmcnt(0)\n"
"v_mfma_f32_16x16x32_bf16 v[92:95], a[200:203], a[4:7], v[92:95]\n"
"buffer_load_dwordx4 v[156:159], v4, s[64:67], s32 offen offset:3072 sc1\n"
"v_mfma_f32_16x16x32_bf16 v[108:111], a[200:203], a[8:11], v[108:111]\n"
"buffer_load_dwordx4 v[160:163], v4, s[64:67], s33 offen offset:0 sc1\n"
"v_mfma_f32_16x16x32_bf16 v[124:127], a[200:203], a[12:15], v[124:127]\n"
"buffer_load_dwordx4 v[164:167], v4, s[64:67], s33 offen offset:1024 sc1\n"
"s_nop 6\n"
"v_cvt_pk_bf16_f32 v80, v80, v81\n"
"v_cvt_pk_bf16_f32 v81, v82, v83\n"
"v_cvt_pk_bf16_f32 v82, v84, v85\n"
"v_cvt_pk_bf16_f32 v83, v86, v87\n"
"v_cvt_pk_bf16_f32 v84, v88, v89\n"
"v_cvt_pk_bf16_f32 v85, v90, v91\n"
"v_cvt_pk_bf16_f32 v86, v92, v93\n"
"v_cvt_pk_bf16_f32 v87, v94, v95\n"
"buffer_store_dwordx4 v[80:83], v4, s[64:67], s23 offen offset:0 sc1\n"
"buffer_store_dwordx4 v[84:87], v4, s[64:67], s23 offen offset:1024 sc1\n"
"v_cvt_pk_bf16_f32 v96, v96, v97\n"
"v_cvt_pk_bf16_f32 v97, v98, v99\n"
"v_cvt_pk_bf16_f32 v98, v100, v101\n"
"v_cvt_pk_bf16_f32 v99, v102, v103\n"
"v_cvt_pk_bf16_f32 v100, v104, v105\n"
"v_cvt_pk_bf16_f32 v101, v106, v107\n"
"v_cvt_pk_bf16_f32 v102, v108, v109\n"
"v_cvt_pk_bf16_f32 v103, v110, v111\n"
"buffer_store_dwordx4 v[96:99], v4, s[64:67], s24 offen offset:0 sc1\n"
"buffer_store_dwordx4 v[100:103], v4, s[64:67], s24 offen offset:1024 sc1\n"
"v_cvt_pk_bf16_f32 v112, v112, v113\n"
"v_cvt_pk_bf16_f32 v113, v114, v115\n"
"v_cvt_pk_bf16_f32 v114, v116, v117\n"
"v_cvt_pk_bf16_f32 v115, v118, v119\n"
"v_cvt_pk_bf16_f32 v116, v120, v121\n"
"v_cvt_pk_bf16_f32 v117, v122, v123\n"
"v_cvt_pk_bf16_f32 v118, v124, v125\n"
"v_cvt_pk_bf16_f32 v119, v126, v127\n"
"buffer_store_dwordx4 v[112:115], v4, s[64:67], s25 offen offset:0 sc1\n"
"buffer_store_dwordx4 v[116:119], v4, s[64:67], s25 offen offset:1024 sc1\n"
"s_waitcnt vmcnt(11)\n"
"v_max_u32 v5, v144, v145\n"
"v_max3_u32 v5, v5, v146, v147\n"
"s_waitcnt vmcnt(10)\n"
"v_max3_u32 v5, v5, v148, v149\n"
"v_max3_u32 v5, v5, v150, v151\n"
"s_waitcnt vmcnt(9)\n"
"v_max3_u32 v5, v5, v152, v153\n"
"v_max3_u32 v5, v5, v154, v155\n"
"s_waitcnt vmcnt(8)\n"
"v_max3_u32 v5, v5, v156, v157\n"
"v_max3_u32 v5, v5, v158, v159\n"
"s_waitcnt vmcnt(7)\n"
"v_max3_u32 v5, v5, v160, v161\n"
"v_max3_u32 v5, v5, v162, v163\n"
"s_waitcnt vmcnt(6)\n"
"v_max3_u32 v5, v5, v164, v165\n"
"v_max3_u32 v5, v5, v166, v167\n"
"v_cmp_eq_u32 vcc, -1, v5\n"
"s_nop 0\n"
"s_cbranch_vccnz LFR_%=\n"
"LFK_%=:\n"
"v_mfma_f32_16x16x32_bf16 v[64:67], a[32:35], a[204:207], v[64:67]\n"
"v_lshlrev_b32 v46, 16, v144\n"
"v_and_b32 v47, 0xffff0000, v144\n"
"v_mfma_f32_16x16x32_bf16 v[64:67], a[48:51], a[208:211], v[64:67]\n"
"v_lshlrev_b32 v48, 16, v145\n"
"v_and_b32 v49, 0xffff0000, v145\n"
"v_mfma_f32_16x16x32_bf16 v[64:67], a[64:67], a[212:215], v[64:67]\n"
"v_lshlrev_b32 v50, 16, v146\n"
"v_and_b32 v51, 0xffff0000, v146\n"
"v_mfma_f32_16x16x32_bf16 v[64:67], a[80:83], a[216:219], v[64:67]\n"
"v_lshlrev_b32 v52, 16, v147\n"
"v_and_b32 v53, 0xffff0000, v147\n"
"v_mfma_f32_16x16x32_bf16 v[64:67], a[96:99], a[220:223], v[64:67]\n"
"v_lshlrev_b32 v54, 16, v148\n"
"v_and_b32 v55, 0xffff0000, v148\n"
"v_mfma_f32_16x16x32_bf16 v[64:67], a[112:115], a[224:227], v[64:67]\n"
"v_lshlrev_b32 v56, 16, v149\n"
"v_and_b32 v57, 0xffff0000, v149\n"
"v_mfma_f32_16x16x32_bf16 v[64:67], a[172:175], a[228:231], v[64:67]\n"
"v_lshlrev_b32 v58, 16, v150\n"
"v_and_b32 v59, 0xffff0000, v150\n"
"v_mfma_f32_16x16x32_bf16 v[64:67], a[188:191], a[232:235], v[64:67]\n"
"v_lshlrev_b32 v60, 16, v151\n"
"v_and_b32 v61, 0xffff0000, v151\n"
"v_mfma_f32_16x16x32_bf16 v[68:71], a[36:39], a[204:207], v[68:71]\n"
"v_add_f32 v192, v192, v46\n"
"v_add_f32 v193, v193, v47\n"
"v_mfma_f32_16x16x32_bf16 v[68:71], a[52:55], a[208:211], v[68:71]\n"
"v_add_f32 v194, v194, v48\n"
"v_add_f32 v195, v195, v49\n"
"v_mfma_f32_16x16x32_bf16 v[68:71], a[68:71], a[212:215], v[68:71]\n"
"v_add_f32 v196, v196, v50\n"
"v_add_f32 v197, v197, v51\n"
"v_mfma_f32_16x16x32_bf16 v[68:71], a[84:87], a[216:219], v[68:71]\n"
"v_add_f32 v198, v198, v52\n"
"v_add_f32 v199, v199, v53\n"
"v_mfma_f32_16x16x32_bf16 v[68:71], a[100:103], a[220:223], v[68:71]\n"
"v_add_f32 v200, v200, v54\n"
"v_add_f32 v201, v201, v55\n"
"v_mfma_f32_16x16x32_bf16 v[68:71], a[116:119], a[224:227], v[68:71]\n"
"v_add_f32 v202, v202, v56\n"
"v_add_f32 v203, v203, v57\n"
"v_mfma_f32_16x16x32_bf16 v[68:71], a[176:179], a[228:231], v[68:71]\n"
"v_add_f32 v204, v204, v58\n"
"v_add_f32 v205, v205, v59\n"
"v_mfma_f32_16x16x32_bf16 v[68:71], a[192:195], a[232:235], v[68:71]\n"
"v_add_f32 v206, v206, v60\n"
"v_add_f32 v207, v207, v61\n"
"v_mfma_f32_16x16x32_bf16 v[72:75], a[40:43], a[204:207], v[72:75]\n"
"v_lshlrev_b32 v46, 16, v152\n"
"v_and_b32 v47, 0xffff0000, v152\n"
"v_mfma_f32_16x16x32_bf16 v[72:75], a[56:59], a[208:211], v[72:75]\n"
"v_lshlrev_b32 v48, 16, v153\n"
"v_and_b32 v49, 0xffff0000, v153\n"
"v_mfma_f32_16x16x32_bf16 v[72:75], a[72:75], a[212:215], v[72:75]\n"
"v_lshlrev_b32 v50, 16, v154\n"
"v_and_b32 v51, 0xffff0000, v154\n"
"v_mfma_f32_16x16x32_bf16 v[72:75], a[88:91], a[216:219], v[72:75]\n"
"v_lshlrev_b32 v52, 16, v155\n"
"v_and_b32 v53, 0xffff0000, v155\n"
"v_mfma_f32_16x16x32_bf16 v[72:75], a[104:107], a[220:223], v[72:75]\n"
"v_lshlrev_b32 v54, 16, v156\n"
"v_and_b32 v55, 0xffff0000, v156\n"
"v_mfma_f32_16x16x32_bf16 v[72:75], a[120:123], a[224:227], v[72:75]\n"
"v_lshlrev_b32 v56, 16, v157\n"
"v_and_b32 v57, 0xffff0000, v157\n"
"v_mfma_f32_16x16x32_bf16 v[72:75], a[180:183], a[228:231], v[72:75]\n"
"v_lshlrev_b32 v58, 16, v158\n"
"v_and_b32 v59, 0xffff0000, v158\n"
"v_mfma_f32_16x16x32_bf16 v[72:75], a[196:199], a[232:235], v[72:75]\n"
"v_lshlrev_b32 v60, 16, v159\n"
"v_and_b32 v61, 0xffff0000, v159\n"
"v_mfma_f32_16x16x32_bf16 v[76:79], a[44:47], a[204:207], v[76:79]\n"
"v_add_f32 v192, v192, v46\n"
"v_add_f32 v193, v193, v47\n"
"v_mfma_f32_16x16x32_bf16 v[76:79], a[60:63], a[208:211], v[76:79]\n"
"v_add_f32 v194, v194, v48\n"
"v_add_f32 v195, v195, v49\n"
"v_mfma_f32_16x16x32_bf16 v[76:79], a[76:79], a[212:215], v[76:79]\n"
"v_add_f32 v196, v196, v50\n"
"v_add_f32 v197, v197, v51\n"
"v_mfma_f32_16x16x32_bf16 v[76:79], a[92:95], a[216:219], v[76:79]\n"
"v_add_f32 v198, v198, v52\n"
"v_add_f32 v199, v199, v53\n"
"v_mfma_f32_16x16x32_bf16 v[76:79], a[108:111], a[220:223], v[76:79]\n"
"v_add_f32 v200, v200, v54\n"
"v_add_f32 v201, v201, v55\n"
"v_mfma_f32_16x16x32_bf16 v[76:79], a[124:127], a[224:227], v[76:79]\n"
"v_add_f32 v202, v202, v56\n"
"v_add_f32 v203, v203, v57\n"
"v_mfma_f32_16x16x32_bf16 v[76:79], a[184:187], a[228:231], v[76:79]\n"
"v_add_f32 v204, v204, v58\n"
"v_add_f32 v205, v205, v59\n"
"v_mfma_f32_16x16x32_bf16 v[76:79], a[200:203], a[232:235], v[76:79]\n"
"s_or_b32 s42, %1, s4\n"
"v_add_f32 v206, v206, v60\n"
"v_add_f32 v207, v207, v61\n"
"v_lshlrev_b32 v46, 16, v160\n"
"v_and_b32 v47, 0xffff0000, v160\n"
"v_lshlrev_b32 v48, 16, v161\n"
"v_and_b32 v49, 0xffff0000, v161\n"
"v_lshlrev_b32 v50, 16, v162\n"
"v_and_b32 v51, 0xffff0000, v162\n"
"v_lshlrev_b32 v52, 16, v163\n"
"v_and_b32 v53, 0xffff0000, v163\n"
"v_lshlrev_b32 v54, 16, v164\n"
"v_and_b32 v55, 0xffff0000, v164\n"
"v_lshlrev_b32 v56, 16, v165\n"
"v_and_b32 v57, 0xffff0000, v165\n"
"v_lshlrev_b32 v58, 16, v166\n"
"v_and_b32 v59, 0xffff0000, v166\n"
"v_lshlrev_b32 v60, 16, v167\n"
"v_and_b32 v61, 0xffff0000, v167\n"
"v_add_f32 v192, v192, v46\n"
"v_add_f32 v193, v193, v47\n"
"v_add_f32 v194, v194, v48\n"
"v_add_f32 v195, v195, v49\n"
"v_add_f32 v196, v196, v50\n"
"v_add_f32 v197, v197, v51\n"
"v_add_f32 v198, v198, v52\n"
"v_add_f32 v199, v199, v53\n"
"v_add_f32 v200, v200, v54\n"
"v_add_f32 v201, v201, v55\n"
"v_add_f32 v202, v202, v56\n"
"v_add_f32 v203, v203, v57\n"
"v_add_f32 v204, v204, v58\n"
"v_add_f32 v205, v205, v59\n"
"v_add_f32 v206, v206, v60\n"
"v_add_f32 v207, v207, v61\n"
"v_mul_f32 v46, 0xbfb8aa3b, v192\n"
"v_mul_f32 v47, 0xbfb8aa3b, v193\n"
"v_mul_f32 v48, 0xbfb8aa3b, v194\n"
"v_mul_f32 v49, 0xbfb8aa3b, v195\n"
"v_mul_f32 v50, 0xbfb8aa3b, v196\n"
"v_mul_f32 v51, 0xbfb8aa3b, v197\n"
"v_mul_f32 v52, 0xbfb8aa3b, v198\n"
"v_mul_f32 v53, 0xbfb8aa3b, v199\n"
"v_mul_f32 v54, 0xbfb8aa3b, v200\n"
"v_mul_f32 v55, 0xbfb8aa3b, v201\n"
"v_mul_f32 v56, 0xbfb8aa3b, v202\n"
"v_mul_f32 v57, 0xbfb8aa3b, v203\n"
"v_mul_f32 v58, 0xbfb8aa3b, v204\n"
"v_mul_f32 v59, 0xbfb8aa3b, v205\n"
"v_mul_f32 v60, 0xbfb8aa3b, v206\n"
"v_mul_f32 v61, 0xbfb8aa3b, v207\n"
"v_exp_f32 v46, v46\n"
"v_exp_f32 v47, v47\n"
"v_exp_f32 v48, v48\n"
"v_exp_f32 v49, v49\n"
"v_exp_f32 v50, v50\n"
"v_exp_f32 v51, v51\n"
"v_exp_f32 v52, v52\n"
"v_exp_f32 v53, v53\n"
"v_exp_f32 v54, v54\n"
"v_exp_f32 v55, v55\n"
"v_exp_f32 v56, v56\n"
"v_exp_f32 v57, v57\n"
"v_exp_f32 v58, v58\n"
"v_exp_f32 v59, v59\n"
"v_exp_f32 v60, v60\n"
"v_exp_f32 v61, v61\n"
"v_add_f32 v46, 1.0, v46\n"
"v_add_f32 v47, 1.0, v47\n"
"v_add_f32 v48, 1.0, v48\n"
"v_add_f32 v49, 1.0, v49\n"
"v_add_f32 v50, 1.0, v50\n"
"v_add_f32 v51, 1.0, v51\n"
"v_add_f32 v52, 1.0, v52\n"
"v_add_f32 v53, 1.0, v53\n"
"v_add_f32 v54, 1.0, v54\n"
"v_add_f32 v55, 1.0, v55\n"
"v_add_f32 v56, 1.0, v56\n"
"v_add_f32 v57, 1.0, v57\n"
"v_add_f32 v58, 1.0, v58\n"
"v_add_f32 v59, 1.0, v59\n"
"v_add_f32 v60, 1.0, v60\n"
"v_add_f32 v61, 1.0, v61\n"
"v_rcp_f32 v46, v46\n"
"v_rcp_f32 v47, v47\n"
"v_rcp_f32 v48, v48\n"
"v_rcp_f32 v49, v49\n"
"v_rcp_f32 v50, v50\n"
"v_rcp_f32 v51, v51\n"
"v_rcp_f32 v52, v52\n"
"v_rcp_f32 v53, v53\n"
"v_rcp_f32 v54, v54\n"
"v_rcp_f32 v55, v55\n"
"v_rcp_f32 v56, v56\n"
"v_rcp_f32 v57, v57\n"
"v_rcp_f32 v58, v58\n"
"v_rcp_f32 v59, v59\n"
"v_rcp_f32 v60, v60\n"
"v_rcp_f32 v61, v61\n"
"v_mul_f32 v192, v192, v46\n"
"v_mul_f32 v193, v193, v47\n"
"v_mul_f32 v194, v194, v48\n"
"v_mul_f32 v195, v195, v49\n"
"v_mul_f32 v196, v196, v50\n"
"v_mul_f32 v197, v197, v51\n"
"v_mul_f32 v198, v198, v52\n"
"v_mul_f32 v199, v199, v53\n"
"v_mul_f32 v200, v200, v54\n"
"v_mul_f32 v201, v201, v55\n"
"v_mul_f32 v202, v202, v56\n"
"v_mul_f32 v203, v203, v57\n"
"v_mul_f32 v204, v204, v58\n"
"v_mul_f32 v205, v205, v59\n"
"v_mul_f32 v206, v206, v60\n"
"v_mul_f32 v207, v207, v61\n"
"s_cmp_eq_u32 s42, 0\n"
"s_cbranch_scc1 LE1_%=\n"
"LE1K_%=:\n"
"s_mov_b32 s14, 40000\n"
"LXF_%=:\n"
"buffer_load_dwordx4 v[208:211], v4, s[64:67], s53 offen offset:0 sc1\n"
"buffer_load_dwordx4 v[212:215], v4, s[64:67], s53 offen offset:1024 sc1\n"
"buffer_load_dwordx4 v[216:219], v4, s[64:67], s53 offen offset:2048 sc1\n"
"buffer_load_dwordx4 v[220:223], v4, s[64:67], s53 offen offset:3072 sc1\n"
"buffer_load_dwordx4 v[224:227], v4, s[64:67], s54 offen offset:0 sc1\n"
"buffer_load_dwordx4 v[228:231], v4, s[64:67], s54 offen offset:1024 sc1\n"
"s_waitcnt vmcnt(5)\n"
"v_max_u32 v5, v208, v209\n"
"v_max3_u32 v5, v5, v210, v211\n"
"s_waitcnt vmcnt(4)\n"
"v_max3_u32 v5, v5, v212, v213\n"
"v_max3_u32 v5, v5, v214, v215\n"
"s_waitcnt vmcnt(3)\n"
"v_max3_u32 v5, v5, v216, v217\n"
"v_max3_u32 v5, v5, v218, v219\n"
"s_waitcnt vmcnt(2)\n"
"v_max3_u32 v5, v5, v220, v221\n"
"v_max3_u32 v5, v5, v222, v223\n"
"s_waitcnt vmcnt(1)\n"
"v_max3_u32 v5, v5, v224, v225\n"
"v_max3_u32 v5, v5, v226, v227\n"
"s_waitcnt vmcnt(0)\n"
"v_max3_u32 v5, v5, v228, v229\n"
"v_max3_u32 v5, v5, v230, v231\n"
"v_cmp_eq_u32 vcc, -1, v5\n"
"s_sub_u32 s14, s14, 1\n"
"s_cmp_eq_u32 s14, 0\n"
"s_cbranch_scc1 LXK_%=\n"
"s_cbranch_vccnz LXF_%=\n"
"LXK_%=:\n"
"s_cmp_eq_u32 s42, 0\n"
"s_cbranch_scc1 LE2_%=\n"
"LE2K_%=:\n"
"v_lshlrev_b32 v46, 16, v208\n"
"v_and_b32 v47, 0xffff0000, v208\n"
"v_lshlrev_b32 v48, 16, v209\n"
"v_and_b32 v49, 0xffff0000, v209\n"
"v_lshlrev_b32 v50, 16, v210\n"
"v_and_b32 v51, 0xffff0000, v210\n"
"v_lshlrev_b32 v52, 16, v211\n"
"v_and_b32 v53, 0xffff0000, v211\n"
"v_lshlrev_b32 v54, 16, v212\n"
"v_and_b32 v55, 0xffff0000, v212\n"
"v_lshlrev_b32 v56, 16, v213\n"
"v_and_b32 v57, 0xffff0000, v213\n"
"v_lshlrev_b32 v58, 16, v214\n"
"v_and_b32 v59, 0xffff0000, v214\n"
"v_lshlrev_b32 v60, 16, v215\n"
"v_and_b32 v61, 0xffff0000, v215\n"
"v_pk_add_f32 v[64:65], v[64:65], v[46:47]\n"
"v_pk_add_f32 v[66:67], v[66:67], v[48:49]\n"
"v_pk_add_f32 v[68:69], v[68:69], v[50:51]\n"
"v_pk_add_f32 v[70:71], v[70:71], v[52:53]\n"
"v_pk_add_f32 v[72:73], v[72:73], v[54:55]\n"
"v_pk_add_f32 v[74:75], v[74:75], v[56:57]\n"
"v_pk_add_f32 v[76:77], v[76:77], v[58:59]\n"
"v_pk_add_f32 v[78:79], v[78:79], v[60:61]\n"
"v_lshlrev_b32 v46, 16, v216\n"
"v_and_b32 v47, 0xffff0000, v216\n"
"v_lshlrev_b32 v48, 16, v217\n"
"v_and_b32 v49, 0xffff0000, v217\n"
"v_lshlrev_b32 v50, 16, v218\n"
"v_and_b32 v51, 0xffff0000, v218\n"
"v_lshlrev_b32 v52, 16, v219\n"
"v_and_b32 v53, 0xffff0000, v219\n"
"v_lshlrev_b32 v54, 16, v220\n"
"v_and_b32 v55, 0xffff0000, v220\n"
"v_lshlrev_b32 v56, 16, v221\n"
"v_and_b32 v57, 0xffff0000, v221\n"
"v_lshlrev_b32 v58, 16, v222\n"
"v_and_b32 v59, 0xffff0000, v222\n"
"v_lshlrev_b32 v60, 16, v223\n"
"v_and_b32 v61, 0xffff0000, v223\n"
"v_pk_add_f32 v[64:65], v[64:65], v[46:47]\n"
"v_pk_add_f32 v[66:67], v[66:67], v[48:49]\n"
"v_pk_add_f32 v[68:69], v[68:69], v[50:51]\n"
"v_pk_add_f32 v[70:71], v[70:71], v[52:53]\n"
"v_pk_add_f32 v[72:73], v[72:73], v[54:55]\n"
"v_pk_add_f32 v[74:75], v[74:75], v[56:57]\n"
"v_pk_add_f32 v[76:77], v[76:77], v[58:59]\n"
"v_pk_add_f32 v[78:79], v[78:79], v[60:61]\n"
"v_lshlrev_b32 v46, 16, v224\n"
"v_and_b32 v47, 0xffff0000, v224\n"
"v_lshlrev_b32 v48, 16, v225\n"
"v_and_b32 v49, 0xffff0000, v225\n"
"v_lshlrev_b32 v50, 16, v226\n"
"v_and_b32 v51, 0xffff0000, v226\n"
"v_lshlrev_b32 v52, 16, v227\n"
"v_and_b32 v53, 0xffff0000, v227\n"
"v_lshlrev_b32 v54, 16, v228\n"
"v_and_b32 v55, 0xffff0000, v228\n"
"v_lshlrev_b32 v56, 16, v229\n"
"v_and_b32 v57, 0xffff0000, v229\n"
"v_lshlrev_b32 v58, 16, v230\n"
"v_and_b32 v59, 0xffff0000, v230\n"
"v_lshlrev_b32 v60, 16, v231\n"
"v_and_b32 v61, 0xffff0000, v231\n"
"v_pk_add_f32 v[64:65], v[64:65], v[46:47]\n"
"v_pk_add_f32 v[66:67], v[66:67], v[48:49]\n"
"v_pk_add_f32 v[68:69], v[68:69], v[50:51]\n"
"v_pk_add_f32 v[70:71], v[70:71], v[52:53]\n"
"v_pk_add_f32 v[72:73], v[72:73], v[54:55]\n"
"v_pk_add_f32 v[74:75], v[74:75], v[56:57]\n"
"v_pk_add_f32 v[76:77], v[76:77], v[58:59]\n"
"v_pk_add_f32 v[78:79], v[78:79], v[60:61]\n"
"v_pk_fma_f32 v[128:129], v[64:65], v[192:193], v[128:129]\n"
"v_pk_fma_f32 v[130:131], v[66:67], v[194:195], v[130:131]\n"
"v_pk_fma_f32 v[132:133], v[68:69], v[196:197], v[132:133]\n"
"v_pk_fma_f32 v[134:135], v[70:71], v[198:199], v[134:135]\n"
"v_pk_fma_f32 v[136:137], v[72:73], v[200:201], v[136:137]\n"
"v_pk_fma_f32 v[138:139], v[74:75], v[202:203], v[138:139]\n"
"v_pk_fma_f32 v[140:141], v[76:77], v[204:205], v[140:141]\n"
"v_pk_fma_f32 v[142:143], v[78:79], v[206:207], v[142:143]\n"
"v_cvt_pk_bf16_f32 v80, v128, v129\n"
"v_cvt_pk_bf16_f32 v81, v130, v131\n"
"buffer_store_dwordx2 v[80:81], v24, s[60:63], s51 offen offset:0 nt\n"
"v_cvt_pk_bf16_f32 v82, v132, v133\n"
"v_cvt_pk_bf16_f32 v83, v134, v135\n"
"buffer_store_dwordx2 v[82:83], v24, s[60:63], s51 offen offset:32 nt\n"
"v_cvt_pk_bf16_f32 v84, v136, v137\n"
"v_cvt_pk_bf16_f32 v85, v138, v139\n"
"buffer_store_dwordx2 v[84:85], v24, s[60:63], s51 offen offset:64 nt\n"
"v_cvt_pk_bf16_f32 v86, v140, v141\n"
"v_cvt_pk_bf16_f32 v87, v142, v143\n"
"buffer_store_dwordx2 v[86:87], v24, s[60:63], s51 offen offset:96 nt\n"
"LEND_%=:\n"
"s_branch LFIN_%=\n"
"LSTR_%=:\n"
"s_mov_b32 s14, 40000\n"
"LSTP_%=:\n"
"buffer_load_dwordx4 v[46:49], v188, s[64:67], s43 offen sc1\n"
"buffer_load_dwordx4 v[50:53], v189, s[64:67], s43 offen sc1\n"
"buffer_load_dwordx4 v[54:57], v190, s[64:67], s43 offen sc1\n"
"buffer_load_dwordx4 v[58:61], v191, s[64:67], s43 offen sc1\n"
"s_waitcnt vmcnt(0)\n"
"v_cmp_ne_u32 s[18:19], s8, v47\n"
"v_cmp_ne_u32 vcc, s8, v49\n"
"s_nop 0\n"
"s_or_b64 s[18:19], s[18:19], vcc\n"
"v_cmp_ne_u32 vcc, s8, v51\n"
"s_nop 0\n"
"s_or_b64 s[18:19], s[18:19], vcc\n"
"v_cmp_ne_u32 vcc, s8, v53\n"
"s_nop 0\n"
"s_or_b64 s[18:19], s[18:19], vcc\n"
"v_cmp_ne_u32 vcc, s8, v55\n"
"s_nop 0\n"
"s_or_b64 s[18:19], s[18:19], vcc\n"
"v_cmp_ne_u32 vcc, s8, v57\n"
"s_nop 0\n"
"s_or_b64 s[18:19], s[18:19], vcc\n"
"v_cmp_ne_u32 vcc, s8, v59\n"
"s_nop 0\n"
"s_or_b64 s[18:19], s[18:19], vcc\n"
"v_cmp_ne_u32 vcc, s8, v61\n"
"s_nop 0\n"
"s_or_b64 s[18:19], s[18:19], vcc\n"
"s_nop 0\n"
"s_cmp_lg_u64 s[18:19], 0\n"
"s_cbranch_scc0 LSTP_ok_%=\n"
"s_sleep 1\n"
"s_sub_u32 s14, s14, 1\n"
"s_cmp_lg_u32 s14, 0\n"
"s_cbranch_scc1 LSTP_%=\n"
"LSTP_ok_%=:\n"
"s_branch LSTK_%=\n"
"LHS_%=:\n"
"s_mov_b64 exec, 1\n"
"buffer_store_dword v7, v6, s[64:67], s83 offen sc1\n"
"s_mov_b64 exec, -1\n"
"s_branch LHSK_%=\n"
"LHFR_%=:\n"
"s_mov_b32 s14, 40000\n"
"LHFP_%=:\n"
"buffer_load_dword v9, v8, s[64:67], 0 offen sc1\n"
"s_waitcnt vmcnt(0)\n"
"v_cmp_ne_u32 s[18:19], s8, v9\n"
"s_nop 0\n"
"s_cmp_lg_u64 s[18:19], 0\n"
"s_cbranch_scc0 LHFP_ok_%=\n"
"s_sleep 1\n"
"s_sub_u32 s14, s14, 1\n"
"s_cmp_lg_u32 s14, 0\n"
"s_cbranch_scc1 LHFP_%=\n"
"LHFP_ok_%=:\n"
"s_branch LHFK_%=\n"
"LFB_%=:\n"
"s_waitcnt vmcnt(0) lgkmcnt(0)\n"
"v_lshlrev_b32 v5, 4, v3\n"
"v_lshl_add_u32 v5, v2, 8, v5\n"
"s_xor_b32 s16, s4, 0\n"
"s_lshl_b32 s16, s16, 6\n"
"v_add_u32 v44, s16, v5\n"
"s_xor_b32 s16, s4, 1\n"
"s_lshl_b32 s16, s16, 6\n"
"v_add_u32 v45, s16, v5\n"
"s_xor_b32 s16, s4, 2\n"
"s_lshl_b32 s16, s16, 6\n"
"v_add_u32 v25, s16, v5\n"
"s_xor_b32 s16, s4, 3\n"
"s_lshl_b32 s16, s16, 6\n"
"v_add_u32 v26, s16, v5\n"
"v_lshlrev_b32 v27, 1, v2\n"
"v_lshl_add_u32 v27, v3, 10, v27\n"
"v_mov_b32 v212, 0xff800000\n"
"v_mov_b32 v213, 0xff800000\n"
"v_mov_b32 v214, 0xff800000\n"
"v_mov_b32 v215, 0xff800000\n"
"s_mov_b32 s87, 0\n"
"LFB1_%=:\n"
"s_add_u32 s17, s87, 4096\n"
"buffer_load_dwordx4 a[32:35], v44, s[56:59], s87 offen\n"
"buffer_load_dwordx4 a[36:39], v45, s[56:59], s87 offen\n"
"buffer_load_dwordx4 a[40:43], v25, s[56:59], s87 offen\n"
"buffer_load_dwordx4 a[44:47], v26, s[56:59], s87 offen\n"
"buffer_load_dwordx4 a[48:51], v44, s[56:59], s17 offen\n"
"buffer_load_dwordx4 a[52:55], v45, s[56:59], s17 offen\n"
"buffer_load_dwordx4 a[56:59], v25, s[56:59], s17 offen\n"
"buffer_load_dwordx4 a[60:63], v26, s[56:59], s17 offen\n"
"s_waitcnt vmcnt(0)\n"
"v_mfma_f32_16x16x32_bf16 v[192:195], a[32:35], a[192:195], 0\n"
"v_mfma_f32_16x16x32_bf16 v[192:195], a[36:39], a[196:199], v[192:195]\n"
"v_mfma_f32_16x16x32_bf16 v[192:195], a[40:43], a[200:203], v[192:195]\n"
"v_mfma_f32_16x16x32_bf16 v[192:195], a[44:47], a[204:207], v[192:195]\n"
"v_mfma_f32_16x16x32_bf16 v[196:199], a[48:51], a[192:195], 0\n"
"v_mfma_f32_16x16x32_bf16 v[196:199], a[52:55], a[196:199], v[196:199]\n"
"v_mfma_f32_16x16x32_bf16 v[196:199], a[56:59], a[200:203], v[196:199]\n"
"v_mfma_f32_16x16x32_bf16 v[196:199], a[60:63], a[204:207], v[196:199]\n"
"s_nop 7\n"
"v_max3_f32 v212, v212, v192, v193\n"
"v_max3_f32 v212, v212, v194, v195\n"
"v_max3_f32 v212, v212, v196, v197\n"
"v_max3_f32 v212, v212, v198, v199\n"
"s_nop 1\n"
"v_mfma_f32_16x16x32_bf16 v[192:195], a[32:35], a[208:211], 0\n"
"v_mfma_f32_16x16x32_bf16 v[192:195], a[36:39], a[212:215], v[192:195]\n"
"v_mfma_f32_16x16x32_bf16 v[192:195], a[40:43], a[216:219], v[192:195]\n"
"v_mfma_f32_16x16x32_bf16 v[192:195], a[44:47], a[220:223], v[192:195]\n"
"v_mfma_f32_16x16x32_bf16 v[196:199], a[48:51], a[208:211], 0\n"
"v_mfma_f32_16x16x32_bf16 v[196:199], a[52:55], a[212:215], v[196:199]\n"
"v_mfma_f32_16x16x32_bf16 v[196:199], a[56:59], a[216:219], v[196:199]\n"
"v_mfma_f32_16x16x32_bf16 v[196:199], a[60:63], a[220:223], v[196:199]\n"
"s_nop 7\n"
"v_max3_f32 v213, v213, v192, v193\n"
"v_max3_f32 v213, v213, v194, v195\n"
"v_max3_f32 v213, v213, v196, v197\n"
"v_max3_f32 v213, v213, v198, v199\n"
"s_nop 1\n"
"v_mfma_f32_16x16x32_bf16 v[192:195], a[32:35], a[224:227], 0\n"
"v_mfma_f32_16x16x32_bf16 v[192:195], a[36:39], a[228:231], v[192:195]\n"
"v_mfma_f32_16x16x32_bf16 v[192:195], a[40:43], a[232:235], v[192:195]\n"
"v_mfma_f32_16x16x32_bf16 v[192:195], a[44:47], a[236:239], v[192:195]\n"
"v_mfma_f32_16x16x32_bf16 v[196:199], a[48:51], a[224:227], 0\n"
"v_mfma_f32_16x16x32_bf16 v[196:199], a[52:55], a[228:231], v[196:199]\n"
"v_mfma_f32_16x16x32_bf16 v[196:199], a[56:59], a[232:235], v[196:199]\n"
"v_mfma_f32_16x16x32_bf16 v[196:199], a[60:63], a[236:239], v[196:199]\n"
"s_nop 7\n"
"v_max3_f32 v214, v214, v192, v193\n"
"v_max3_f32 v214, v214, v194, v195\n"
"v_max3_f32 v214, v214, v196, v197\n"
"v_max3_f32 v214, v214, v198, v199\n"
"s_nop 1\n"
"v_mfma_f32_16x16x32_bf16 v[192:195], a[32:35], a[240:243], 0\n"
"v_mfma_f32_16x16x32_bf16 v[192:195], a[36:39], a[244:247], v[192:195]\n"
"v_mfma_f32_16x16x32_bf16 v[192:195], a[40:43], a[248:251], v[192:195]\n"
"v_mfma_f32_16x16x32_bf16 v[192:195], a[44:47], a[252:255], v[192:195]\n"
"v_mfma_f32_16x16x32_bf16 v[196:199], a[48:51], a[240:243], 0\n"
"v_mfma_f32_16x16x32_bf16 v[196:199], a[52:55], a[244:247], v[196:199]\n"
"v_mfma_f32_16x16x32_bf16 v[196:199], a[56:59], a[248:251], v[196:199]\n"
"v_mfma_f32_16x16x32_bf16 v[196:199], a[60:63], a[252:255], v[196:199]\n"
"s_nop 7\n"
"v_max3_f32 v215, v215, v192, v193\n"
"v_max3_f32 v215, v215, v194, v195\n"
"v_max3_f32 v215, v215, v196, v197\n"
"v_max3_f32 v215, v215, v198, v199\n"
"s_nop 1\n"
"s_add_u32 s87, s87, 8192\n"
"s_cmp_lt_u32 s87, 131072\n"
"s_cbranch_scc1 LFB1_%=\n"
"v_mov_b32 v112, v212\n"
"v_mov_b32 v113, v213\n"
"v_mov_b32 v114, v214\n"
"v_mov_b32 v115, v215\n"
"s_nop 0\n"
"v_permlane16_swap_b32 v212, v112\n"
"v_permlane16_swap_b32 v213, v113\n"
"v_permlane16_swap_b32 v214, v114\n"
"v_permlane16_swap_b32 v215, v115\n"
"v_max_f32 v212, v212, v112\n"
"v_max_f32 v213, v213, v113\n"
"v_max_f32 v214, v214, v114\n"
"v_max_f32 v215, v215, v115\n"
"v_mov_b32 v112, v212\n"
"v_mov_b32 v113, v213\n"
"v_mov_b32 v114, v214\n"
"v_mov_b32 v115, v215\n"
"s_nop 0\n"
"v_permlane32_swap_b32 v212, v112\n"
"v_permlane32_swap_b32 v213, v113\n"
"v_permlane32_swap_b32 v214, v114\n"
"v_permlane32_swap_b32 v215, v115\n"
"v_max_f32 v212, v212, v112\n"
"v_max_f32 v213, v213, v113\n"
"v_max_f32 v214, v214, v114\n"
"v_max_f32 v215, v215, v115\n"
"v_xor_b32 v216, 0x80000000, v212\n"
"v_xor_b32 v217, 0x80000000, v212\n"
"v_xor_b32 v218, 0x80000000, v212\n"
"v_xor_b32 v219, 0x80000000, v212\n"
"v_xor_b32 v220, 0x80000000, v213\n"
"v_xor_b32 v221, 0x80000000, v213\n"
"v_xor_b32 v222, 0x80000000, v213\n"
"v_xor_b32 v223, 0x80000000, v213\n"
"v_xor_b32 v224, 0x80000000, v214\n"
"v_xor_b32 v225, 0x80000000, v214\n"
"v_xor_b32 v226, 0x80000000, v214\n"
"v_xor_b32 v227, 0x80000000, v214\n"
"v_xor_b32 v228, 0x80000000, v215\n"
"v_xor_b32 v229, 0x80000000, v215\n"
"v_xor_b32 v230, 0x80000000, v215\n"
"v_xor_b32 v231, 0x80000000, v215\n"
"v_mov_b32 v64, 0\n"
"v_mov_b32 v65, 0\n"
"v_mov_b32 v66, 0\n"
"v_mov_b32 v67, 0\n"
"v_mov_b32 v68, 0\n"
"v_mov_b32 v69, 0\n"
"v_mov_b32 v70, 0\n"
"v_mov_b32 v71, 0\n"
"v_mov_b32 v72, 0\n"
"v_mov_b32 v73, 0\n"
"v_mov_b32 v74, 0\n"
"v_mov_b32 v75, 0\n"
"v_mov_b32 v76, 0\n"
"v_mov_b32 v77, 0\n"
"v_mov_b32 v78, 0\n"
"v_mov_b32 v79, 0\n"
"v_mov_b32 v80, 0\n"
"v_mov_b32 v81, 0\n"
"v_mov_b32 v82, 0\n"
"v_mov_b32 v83, 0\n"
"v_mov_b32 v84, 0\n"
"v_mov_b32 v85, 0\n"
"v_mov_b32 v86, 0\n"
"v_mov_b32 v87, 0\n"
"v_mov_b32 v88, 0\n"
"v_mov_b32 v89, 0\n"
"v_mov_b32 v90, 0\n"
"v_mov_b32 v91, 0\n"
"v_mov_b32 v92, 0\n"
"v_mov_b32 v93, 0\n"
"v_mov_b32 v94, 0\n"
"v_mov_b32 v95, 0\n"
"v_mov_b32 v96, 0\n"
"v_mov_b32 v97, 0\n"
"v_mov_b32 v98, 0\n"
"v_mov_b32 v99, 0\n"
"v_mov_b32 v100, 0\n"
"v_mov_b32 v101, 0\n"
"v_mov_b32 v102, 0\n"
"v_mov_b32 v103, 0\n"
"v_mov_b32 v104, 0\n"
"v_mov_b32 v105, 0\n"
"v_mov_b32 v106, 0\n"
"v_mov_b32 v107, 0\n"
"v_mov_b32 v108, 0\n"
"v_mov_b32 v109, 0\n"
"v_mov_b32 v110, 0\n"
"v_mov_b32 v111, 0\n"
"v_mov_b32 v112, 0\n"
"v_mov_b32 v113, 0\n"
"v_mov_b32 v114, 0\n"
"v_mov_b32 v115, 0\n"
"v_mov_b32 v116, 0\n"
"v_mov_b32 v117, 0\n"
"v_mov_b32 v118, 0\n"
"v_mov_b32 v119, 0\n"
"v_mov_b32 v120, 0\n"
"v_mov_b32 v121, 0\n"
"v_mov_b32 v122, 0\n"
"v_mov_b32 v123, 0\n"
"v_mov_b32 v124, 0\n"
"v_mov_b32 v125, 0\n"
"v_mov_b32 v126, 0\n"
"v_mov_b32 v127, 0\n"
"v_mov_b32 v128, 0\n"
"v_mov_b32 v129, 0\n"
"v_mov_b32 v130, 0\n"
"v_mov_b32 v131, 0\n"
"v_mov_b32 v132, 0\n"
"v_mov_b32 v133, 0\n"
"v_mov_b32 v134, 0\n"
"v_mov_b32 v135, 0\n"
"v_mov_b32 v136, 0\n"
"v_mov_b32 v137, 0\n"
"v_mov_b32 v138, 0\n"
"v_mov_b32 v139, 0\n"
"v_mov_b32 v140, 0\n"
"v_mov_b32 v141, 0\n"
"v_mov_b32 v142, 0\n"
"v_mov_b32 v143, 0\n"
"v_mov_b32 v144, 0\n"
"v_mov_b32 v145, 0\n"
"v_mov_b32 v146, 0\n"
"v_mov_b32 v147, 0\n"
"v_mov_b32 v148, 0\n"
"v_mov_b32 v149, 0\n"
"v_mov_b32 v150, 0\n"
"v_mov_b32 v151, 0\n"
"v_mov_b32 v152, 0\n"
"v_mov_b32 v153, 0\n"
"v_mov_b32 v154, 0\n"
"v_mov_b32 v155, 0\n"
"v_mov_b32 v156, 0\n"
"v_mov_b32 v157, 0\n"
"v_mov_b32 v158, 0\n"
"v_mov_b32 v159, 0\n"
"v_mov_b32 v160, 0\n"
"v_mov_b32 v161, 0\n"
"v_mov_b32 v162, 0\n"
"v_mov_b32 v163, 0\n"
"v_mov_b32 v164, 0\n"
"v_mov_b32 v165, 0\n"
"v_mov_b32 v166, 0\n"
"v_mov_b32 v167, 0\n"
"v_mov_b32 v168, 0\n"
"v_mov_b32 v169, 0\n"
"v_mov_b32 v170, 0\n"
"v_mov_b32 v171, 0\n"
"v_mov_b32 v172, 0\n"
"v_mov_b32 v173, 0\n"
"v_mov_b32 v174, 0\n"
"v_mov_b32 v175, 0\n"
"v_mov_b32 v176, 0\n"
"v_mov_b32 v177, 0\n"
"v_mov_b32 v178, 0\n"
"v_mov_b32 v179, 0\n"
"v_mov_b32 v180, 0\n"
"v_mov_b32 v181, 0\n"
"v_mov_b32 v182, 0\n"
"v_mov_b32 v183, 0\n"
"v_mov_b32 v184, 0\n"
"v_mov_b32 v185, 0\n"
"v_mov_b32 v186, 0\n"
"v_mov_b32 v187, 0\n"
"v_mov_b32 v188, 0\n"
"v_mov_b32 v189, 0\n"
"v_mov_b32 v190, 0\n"
"v_mov_b32 v191, 0\n"
"v_mov_b32 v232, 0\n"
"v_mov_b32 v233, 0\n"
"v_mov_b32 v234, 0\n"
"v_mov_b32 v235, 0\n"
"v_mov_b32 v236, 0\n"
"v_mov_b32 v237, 0\n"
"v_mov_b32 v238, 0\n"
"v_mov_b32 v239, 0\n"
"v_mov_b32 v240, 0\n"
"v_mov_b32 v241, 0\n"
"v_mov_b32 v242, 0\n"
"v_mov_b32 v243, 0\n"
"v_mov_b32 v244, 0\n"
"v_mov_b32 v245, 0\n"
"v_mov_b32 v246, 0\n"
"v_mov_b32 v247, 0\n"
"s_mov_b32 s87, 0\n"
"LFB2_%=:\n"
"s_add_u32 s17, s87, 4096\n"
"buffer_load_dwordx4 a[32:35], v44, s[56:59], s87 offen\n"
"buffer_load_dwordx4 a[36:39], v45, s[56:59], s87 offen\n"
"buffer_load_dwordx4 a[40:43], v25, s[56:59], s87 offen\n"
"buffer_load_dwordx4 a[44:47], v26, s[56:59], s87 offen\n"
"buffer_load_dwordx4 a[48:51], v44, s[56:59], s17 offen\n"
"buffer_load_dwordx4 a[52:55], v45, s[56:59], s17 offen\n"
"buffer_load_dwordx4 a[56:59], v25, s[56:59], s17 offen\n"
"buffer_load_dwordx4 a[60:63], v26, s[56:59], s17 offen\n"
"s_waitcnt vmcnt(0)\n"
"v_mfma_f32_16x16x32_bf16 v[192:195], a[32:35], a[192:195], v[216:219]\n"
"v_mfma_f32_16x16x32_bf16 v[192:195], a[36:39], a[196:199], v[192:195]\n"
"v_mfma_f32_16x16x32_bf16 v[192:195], a[40:43], a[200:203], v[192:195]\n"
"v_mfma_f32_16x16x32_bf16 v[192:195], a[44:47], a[204:207], v[192:195]\n"
"v_mfma_f32_16x16x32_bf16 v[196:199], a[48:51], a[192:195], v[216:219]\n"
"v_mfma_f32_16x16x32_bf16 v[196:199], a[52:55], a[196:199], v[196:199]\n"
"v_mfma_f32_16x16x32_bf16 v[196:199], a[56:59], a[200:203], v[196:199]\n"
"v_mfma_f32_16x16x32_bf16 v[196:199], a[60:63], a[204:207], v[196:199]\n"
"s_nop 7\n"
"v_exp_f32 v192, v192\n"
"v_exp_f32 v193, v193\n"
"v_exp_f32 v194, v194\n"
"v_exp_f32 v195, v195\n"
"v_exp_f32 v196, v196\n"
"v_exp_f32 v197, v197\n"
"v_exp_f32 v198, v198\n"
"v_exp_f32 v199, v199\n"
"s_nop 0\n"
"v_cvt_pk_bf16_f32 v46, v192, v193\n"
"v_cvt_pk_bf16_f32 v47, v194, v195\n"
"v_cvt_pk_bf16_f32 v48, v196, v197\n"
"v_cvt_pk_bf16_f32 v49, v198, v199\n"
"s_nop 1\n"
"v_mfma_f32_16x16x32_bf16 v[232:235], a[136:139], v[46:49], v[232:235]\n"
"v_mfma_f32_16x16x32_bf16 v[192:195], a[32:35], a[208:211], v[220:223]\n"
"v_mfma_f32_16x16x32_bf16 v[192:195], a[36:39], a[212:215], v[192:195]\n"
"v_mfma_f32_16x16x32_bf16 v[192:195], a[40:43], a[216:219], v[192:195]\n"
"v_mfma_f32_16x16x32_bf16 v[192:195], a[44:47], a[220:223], v[192:195]\n"
"v_mfma_f32_16x16x32_bf16 v[196:199], a[48:51], a[208:211], v[220:223]\n"
"v_mfma_f32_16x16x32_bf16 v[196:199], a[52:55], a[212:215], v[196:199]\n"
"v_mfma_f32_16x16x32_bf16 v[196:199], a[56:59], a[216:219], v[196:199]\n"
"v_mfma_f32_16x16x32_bf16 v[196:199], a[60:63], a[220:223], v[196:199]\n"
"s_nop 7\n"
"v_exp_f32 v192, v192\n"
"v_exp_f32 v193, v193\n"
"v_exp_f32 v194, v194\n"
"v_exp_f32 v195, v195\n"
"v_exp_f32 v196, v196\n"
"v_exp_f32 v197, v197\n"
"v_exp_f32 v198, v198\n"
"v_exp_f32 v199, v199\n"
"s_nop 0\n"
"v_cvt_pk_bf16_f32 v50, v192, v193\n"
"v_cvt_pk_bf16_f32 v51, v194, v195\n"
"v_cvt_pk_bf16_f32 v52, v196, v197\n"
"v_cvt_pk_bf16_f32 v53, v198, v199\n"
"s_nop 1\n"
"v_mfma_f32_16x16x32_bf16 v[236:239], a[136:139], v[50:53], v[236:239]\n"
"v_mfma_f32_16x16x32_bf16 v[192:195], a[32:35], a[224:227], v[224:227]\n"
"v_mfma_f32_16x16x32_bf16 v[192:195], a[36:39], a[228:231], v[192:195]\n"
"v_mfma_f32_16x16x32_bf16 v[192:195], a[40:43], a[232:235], v[192:195]\n"
"v_mfma_f32_16x16x32_bf16 v[192:195], a[44:47], a[236:239], v[192:195]\n"
"v_mfma_f32_16x16x32_bf16 v[196:199], a[48:51], a[224:227], v[224:227]\n"
"v_mfma_f32_16x16x32_bf16 v[196:199], a[52:55], a[228:231], v[196:199]\n"
"v_mfma_f32_16x16x32_bf16 v[196:199], a[56:59], a[232:235], v[196:199]\n"
"v_mfma_f32_16x16x32_bf16 v[196:199], a[60:63], a[236:239], v[196:199]\n"
"s_nop 7\n"
"v_exp_f32 v192, v192\n"
"v_exp_f32 v193, v193\n"
"v_exp_f32 v194, v194\n"
"v_exp_f32 v195, v195\n"
"v_exp_f32 v196, v196\n"
"v_exp_f32 v197, v197\n"
"v_exp_f32 v198, v198\n"
"v_exp_f32 v199, v199\n"
"s_nop 0\n"
"v_cvt_pk_bf16_f32 v54, v192, v193\n"
"v_cvt_pk_bf16_f32 v55, v194, v195\n"
"v_cvt_pk_bf16_f32 v56, v196, v197\n"
"v_cvt_pk_bf16_f32 v57, v198, v199\n"
"s_nop 1\n"
"v_mfma_f32_16x16x32_bf16 v[240:243], a[136:139], v[54:57], v[240:243]\n"
"v_mfma_f32_16x16x32_bf16 v[192:195], a[32:35], a[240:243], v[228:231]\n"
"v_mfma_f32_16x16x32_bf16 v[192:195], a[36:39], a[244:247], v[192:195]\n"
"v_mfma_f32_16x16x32_bf16 v[192:195], a[40:43], a[248:251], v[192:195]\n"
"v_mfma_f32_16x16x32_bf16 v[192:195], a[44:47], a[252:255], v[192:195]\n"
"v_mfma_f32_16x16x32_bf16 v[196:199], a[48:51], a[240:243], v[228:231]\n"
"v_mfma_f32_16x16x32_bf16 v[196:199], a[52:55], a[244:247], v[196:199]\n"
"v_mfma_f32_16x16x32_bf16 v[196:199], a[56:59], a[248:251], v[196:199]\n"
"v_mfma_f32_16x16x32_bf16 v[196:199], a[60:63], a[252:255], v[196:199]\n"
"s_nop 7\n"
"v_exp_f32 v192, v192\n"
"v_exp_f32 v193, v193\n"
"v_exp_f32 v194, v194\n"
"v_exp_f32 v195, v195\n"
"v_exp_f32 v196, v196\n"
"v_exp_f32 v197, v197\n"
"v_exp_f32 v198, v198\n"
"v_exp_f32 v199, v199\n"
"s_nop 0\n"
"v_cvt_pk_bf16_f32 v58, v192, v193\n"
"v_cvt_pk_bf16_f32 v59, v194, v195\n"
"v_cvt_pk_bf16_f32 v60, v196, v197\n"
"v_cvt_pk_bf16_f32 v61, v198, v199\n"
"s_nop 1\n"
"v_mfma_f32_16x16x32_bf16 v[244:247], a[136:139], v[58:61], v[244:247]\n"
"buffer_load_ushort v208, v27, s[60:63], s87 offen offset:0\n"
"buffer_load_ushort v209, v27, s[60:63], s87 offen offset:512\n"
"buffer_load_ushort v210, v27, s[60:63], s17 offen offset:0\n"
"buffer_load_ushort v211, v27, s[60:63], s17 offen offset:512\n"
"buffer_load_ushort v212, v27, s[60:63], s87 offen offset:256\n"
"buffer_load_ushort v213, v27, s[60:63], s87 offen offset:768\n"
"buffer_load_ushort v214, v27, s[60:63], s17 offen offset:256\n"
"buffer_load_ushort v215, v27, s[60:63], s17 offen offset:768\n"
"s_waitcnt vmcnt(0)\n"
"v_lshl_or_b32 v208, v212, 16, v208\n"
"v_lshl_or_b32 v209, v213, 16, v209\n"
"v_lshl_or_b32 v210, v214, 16, v210\n"
"v_lshl_or_b32 v211, v215, 16, v211\n"
"s_nop 1\n"
"v_mfma_f32_16x16x32_bf16 v[64:67], v[208:211], v[46:49], v[64:67]\n"
"v_mfma_f32_16x16x32_bf16 v[96:99], v[208:211], v[50:53], v[96:99]\n"
"v_mfma_f32_16x16x32_bf16 v[128:131], v[208:211], v[54:57], v[128:131]\n"
"v_mfma_f32_16x16x32_bf16 v[160:163], v[208:211], v[58:61], v[160:163]\n"
"s_nop 3\n"
"buffer_load_ushort v208, v27, s[60:63], s87 offen offset:32\n"
"buffer_load_ushort v209, v27, s[60:63], s87 offen offset:544\n"
"buffer_load_ushort v210, v27, s[60:63], s17 offen offset:32\n"
"buffer_load_ushort v211, v27, s[60:63], s17 offen offset:544\n"
"buffer_load_ushort v212, v27, s[60:63], s87 offen offset:288\n"
"buffer_load_ushort v213, v27, s[60:63], s87 offen offset:800\n"
"buffer_load_ushort v214, v27, s[60:63], s17 offen offset:288\n"
"buffer_load_ushort v215, v27, s[60:63], s17 offen offset:800\n"
"s_waitcnt vmcnt(0)\n"
"v_lshl_or_b32 v208, v212, 16, v208\n"
"v_lshl_or_b32 v209, v213, 16, v209\n"
"v_lshl_or_b32 v210, v214, 16, v210\n"
"v_lshl_or_b32 v211, v215, 16, v211\n"
"s_nop 1\n"
"v_mfma_f32_16x16x32_bf16 v[68:71], v[208:211], v[46:49], v[68:71]\n"
"v_mfma_f32_16x16x32_bf16 v[100:103], v[208:211], v[50:53], v[100:103]\n"
"v_mfma_f32_16x16x32_bf16 v[132:135], v[208:211], v[54:57], v[132:135]\n"
"v_mfma_f32_16x16x32_bf16 v[164:167], v[208:211], v[58:61], v[164:167]\n"
"s_nop 3\n"
"buffer_load_ushort v208, v27, s[60:63], s87 offen offset:64\n"
"buffer_load_ushort v209, v27, s[60:63], s87 offen offset:576\n"
"buffer_load_ushort v210, v27, s[60:63], s17 offen offset:64\n"
"buffer_load_ushort v211, v27, s[60:63], s17 offen offset:576\n"
"buffer_load_ushort v212, v27, s[60:63], s87 offen offset:320\n"
"buffer_load_ushort v213, v27, s[60:63], s87 offen offset:832\n"
"buffer_load_ushort v214, v27, s[60:63], s17 offen offset:320\n"
"buffer_load_ushort v215, v27, s[60:63], s17 offen offset:832\n"
"s_waitcnt vmcnt(0)\n"
"v_lshl_or_b32 v208, v212, 16, v208\n"
"v_lshl_or_b32 v209, v213, 16, v209\n"
"v_lshl_or_b32 v210, v214, 16, v210\n"
"v_lshl_or_b32 v211, v215, 16, v211\n"
"s_nop 1\n"
"v_mfma_f32_16x16x32_bf16 v[72:75], v[208:211], v[46:49], v[72:75]\n"
"v_mfma_f32_16x16x32_bf16 v[104:107], v[208:211], v[50:53], v[104:107]\n"
"v_mfma_f32_16x16x32_bf16 v[136:139], v[208:211], v[54:57], v[136:139]\n"
"v_mfma_f32_16x16x32_bf16 v[168:171], v[208:211], v[58:61], v[168:171]\n"
"s_nop 3\n"
"buffer_load_ushort v208, v27, s[60:63], s87 offen offset:96\n"
"buffer_load_ushort v209, v27, s[60:63], s87 offen offset:608\n"
"buffer_load_ushort v210, v27, s[60:63], s17 offen offset:96\n"
"buffer_load_ushort v211, v27, s[60:63], s17 offen offset:608\n"
"buffer_load_ushort v212, v27, s[60:63], s87 offen offset:352\n"
"buffer_load_ushort v213, v27, s[60:63], s87 offen offset:864\n"
"buffer_load_ushort v214, v27, s[60:63], s17 offen offset:352\n"
"buffer_load_ushort v215, v27, s[60:63], s17 offen offset:864\n"
"s_waitcnt vmcnt(0)\n"
"v_lshl_or_b32 v208, v212, 16, v208\n"
"v_lshl_or_b32 v209, v213, 16, v209\n"
"v_lshl_or_b32 v210, v214, 16, v210\n"
"v_lshl_or_b32 v211, v215, 16, v211\n"
"s_nop 1\n"
"v_mfma_f32_16x16x32_bf16 v[76:79], v[208:211], v[46:49], v[76:79]\n"
"v_mfma_f32_16x16x32_bf16 v[108:111], v[208:211], v[50:53], v[108:111]\n"
"v_mfma_f32_16x16x32_bf16 v[140:143], v[208:211], v[54:57], v[140:143]\n"
"v_mfma_f32_16x16x32_bf16 v[172:175], v[208:211], v[58:61], v[172:175]\n"
"s_nop 3\n"
"buffer_load_ushort v208, v27, s[60:63], s87 offen offset:128\n"
"buffer_load_ushort v209, v27, s[60:63], s87 offen offset:640\n"
"buffer_load_ushort v210, v27, s[60:63], s17 offen offset:128\n"
"buffer_load_ushort v211, v27, s[60:63], s17 offen offset:640\n"
"buffer_load_ushort v212, v27, s[60:63], s87 offen offset:384\n"
"buffer_load_ushort v213, v27, s[60:63], s87 offen offset:896\n"
"buffer_load_ushort v214, v27, s[60:63], s17 offen offset:384\n"
"buffer_load_ushort v215, v27, s[60:63], s17 offen offset:896\n"
"s_waitcnt vmcnt(0)\n"
"v_lshl_or_b32 v208, v212, 16, v208\n"
"v_lshl_or_b32 v209, v213, 16, v209\n"
"v_lshl_or_b32 v210, v214, 16, v210\n"
"v_lshl_or_b32 v211, v215, 16, v211\n"
"s_nop 1\n"
"v_mfma_f32_16x16x32_bf16 v[80:83], v[208:211], v[46:49], v[80:83]\n"
"v_mfma_f32_16x16x32_bf16 v[112:115], v[208:211], v[50:53], v[112:115]\n"
"v_mfma_f32_16x16x32_bf16 v[144:147], v[208:211], v[54:57], v[144:147]\n"
"v_mfma_f32_16x16x32_bf16 v[176:179], v[208:211], v[58:61], v[176:179]\n"
"s_nop 3\n"
"buffer_load_ushort v208, v27, s[60:63], s87 offen offset:160\n"
"buffer_load_ushort v209, v27, s[60:63], s87 offen offset:672\n"
"buffer_load_ushort v210, v27, s[60:63], s17 offen offset:160\n"
"buffer_load_ushort v211, v27, s[60:63], s17 offen offset:672\n"
"buffer_load_ushort v212, v27, s[60:63], s87 offen offset:416\n"
"buffer_load_ushort v213, v27, s[60:63], s87 offen offset:928\n"
"buffer_load_ushort v214, v27, s[60:63], s17 offen offset:416\n"
"buffer_load_ushort v215, v27, s[60:63], s17 offen offset:928\n"
"s_waitcnt vmcnt(0)\n"
"v_lshl_or_b32 v208, v212, 16, v208\n"
"v_lshl_or_b32 v209, v213, 16, v209\n"
"v_lshl_or_b32 v210, v214, 16, v210\n"
"v_lshl_or_b32 v211, v215, 16, v211\n"
"s_nop 1\n"
"v_mfma_f32_16x16x32_bf16 v[84:87], v[208:211], v[46:49], v[84:87]\n"
"v_mfma_f32_16x16x32_bf16 v[116:119], v[208:211], v[50:53], v[116:119]\n"
"v_mfma_f32_16x16x32_bf16 v[148:151], v[208:211], v[54:57], v[148:151]\n"
"v_mfma_f32_16x16x32_bf16 v[180:183], v[208:211], v[58:61], v[180:183]\n"
"s_nop 3\n"
"buffer_load_ushort v208, v27, s[60:63], s87 offen offset:192\n"
"buffer_load_ushort v209, v27, s[60:63], s87 offen offset:704\n"
"buffer_load_ushort v210, v27, s[60:63], s17 offen offset:192\n"
"buffer_load_ushort v211, v27, s[60:63], s17 offen offset:704\n"
"buffer_load_ushort v212, v27, s[60:63], s87 offen offset:448\n"
"buffer_load_ushort v213, v27, s[60:63], s87 offen offset:960\n"
"buffer_load_ushort v214, v27, s[60:63], s17 offen offset:448\n"
"buffer_load_ushort v215, v27, s[60:63], s17 offen offset:960\n"
"s_waitcnt vmcnt(0)\n"
"v_lshl_or_b32 v208, v212, 16, v208\n"
"v_lshl_or_b32 v209, v213, 16, v209\n"
"v_lshl_or_b32 v210, v214, 16, v210\n"
"v_lshl_or_b32 v211, v215, 16, v211\n"
"s_nop 1\n"
"v_mfma_f32_16x16x32_bf16 v[88:91], v[208:211], v[46:49], v[88:91]\n"
"v_mfma_f32_16x16x32_bf16 v[120:123], v[208:211], v[50:53], v[120:123]\n"
"v_mfma_f32_16x16x32_bf16 v[152:155], v[208:211], v[54:57], v[152:155]\n"
"v_mfma_f32_16x16x32_bf16 v[184:187], v[208:211], v[58:61], v[184:187]\n"
"s_nop 3\n"
"buffer_load_ushort v208, v27, s[60:63], s87 offen offset:224\n"
"buffer_load_ushort v209, v27, s[60:63], s87 offen offset:736\n"
"buffer_load_ushort v210, v27, s[60:63], s17 offen offset:224\n"
"buffer_load_ushort v211, v27, s[60:63], s17 offen offset:736\n"
"buffer_load_ushort v212, v27, s[60:63], s87 offen offset:480\n"
"buffer_load_ushort v213, v27, s[60:63], s87 offen offset:992\n"
"buffer_load_ushort v214, v27, s[60:63], s17 offen offset:480\n"
"buffer_load_ushort v215, v27, s[60:63], s17 offen offset:992\n"
"s_waitcnt vmcnt(0)\n"
"v_lshl_or_b32 v208, v212, 16, v208\n"
"v_lshl_or_b32 v209, v213, 16, v209\n"
"v_lshl_or_b32 v210, v214, 16, v210\n"
"v_lshl_or_b32 v211, v215, 16, v211\n"
"s_nop 1\n"
"v_mfma_f32_16x16x32_bf16 v[92:95], v[208:211], v[46:49], v[92:95]\n"
"v_mfma_f32_16x16x32_bf16 v[124:127], v[208:211], v[50:53], v[124:127]\n"
"v_mfma_f32_16x16x32_bf16 v[156:159], v[208:211], v[54:57], v[156:159]\n"
"v_mfma_f32_16x16x32_bf16 v[188:191], v[208:211], v[58:61], v[188:191]\n"
"s_nop 3\n"
"s_add_u32 s87, s87, 8192\n"
"s_cmp_lt_u32 s87, 131072\n"
"s_cbranch_scc1 LFB2_%=\n"
"s_nop 7\n"
"v_rcp_f32 v46, v232\n"
"s_xor_b32 s16, s82, 0\n"
"s_add_u32 s16, s16, s81\n"
"s_mul_i32 s16, s16, 136192\n"
"s_lshl_b32 s17, s5, 12\n"
"s_add_u32 s16, s16, s17\n"
"v_pk_mul_f32 v[64:65], v[64:65], v[46:47] op_sel_hi:[1,0]\n"
"v_pk_mul_f32 v[66:67], v[66:67], v[46:47] op_sel_hi:[1,0]\n"
"v_pk_mul_f32 v[68:69], v[68:69], v[46:47] op_sel_hi:[1,0]\n"
"v_pk_mul_f32 v[70:71], v[70:71], v[46:47] op_sel_hi:[1,0]\n"
"v_cvt_pk_bf16_f32 v64, v64, v65\n"
"v_cvt_pk_bf16_f32 v65, v66, v67\n"
"v_cvt_pk_bf16_f32 v66, v68, v69\n"
"v_cvt_pk_bf16_f32 v67, v70, v71\n"
"buffer_store_dwordx4 v[64:67], v4, s[64:67], s16 offen offset:0 sc1\n"
"v_pk_mul_f32 v[72:73], v[72:73], v[46:47] op_sel_hi:[1,0]\n"
"v_pk_mul_f32 v[74:75], v[74:75], v[46:47] op_sel_hi:[1,0]\n"
"v_pk_mul_f32 v[76:77], v[76:77], v[46:47] op_sel_hi:[1,0]\n"
"v_pk_mul_f32 v[78:79], v[78:79], v[46:47] op_sel_hi:[1,0]\n"
"v_cvt_pk_bf16_f32 v72, v72, v73\n"
"v_cvt_pk_bf16_f32 v73, v74, v75\n"
"v_cvt_pk_bf16_f32 v74, v76, v77\n"
"v_cvt_pk_bf16_f32 v75, v78, v79\n"
"buffer_store_dwordx4 v[72:75], v4, s[64:67], s16 offen offset:1024 sc1\n"
"v_pk_mul_f32 v[80:81], v[80:81], v[46:47] op_sel_hi:[1,0]\n"
"v_pk_mul_f32 v[82:83], v[82:83], v[46:47] op_sel_hi:[1,0]\n"
"v_pk_mul_f32 v[84:85], v[84:85], v[46:47] op_sel_hi:[1,0]\n"
"v_pk_mul_f32 v[86:87], v[86:87], v[46:47] op_sel_hi:[1,0]\n"
"v_cvt_pk_bf16_f32 v80, v80, v81\n"
"v_cvt_pk_bf16_f32 v81, v82, v83\n"
"v_cvt_pk_bf16_f32 v82, v84, v85\n"
"v_cvt_pk_bf16_f32 v83, v86, v87\n"
"buffer_store_dwordx4 v[80:83], v4, s[64:67], s16 offen offset:2048 sc1\n"
"v_pk_mul_f32 v[88:89], v[88:89], v[46:47] op_sel_hi:[1,0]\n"
"v_pk_mul_f32 v[90:91], v[90:91], v[46:47] op_sel_hi:[1,0]\n"
"v_pk_mul_f32 v[92:93], v[92:93], v[46:47] op_sel_hi:[1,0]\n"
"v_pk_mul_f32 v[94:95], v[94:95], v[46:47] op_sel_hi:[1,0]\n"
"v_cvt_pk_bf16_f32 v88, v88, v89\n"
"v_cvt_pk_bf16_f32 v89, v90, v91\n"
"v_cvt_pk_bf16_f32 v90, v92, v93\n"
"v_cvt_pk_bf16_f32 v91, v94, v95\n"
"buffer_store_dwordx4 v[88:91], v4, s[64:67], s16 offen offset:3072 sc1\n"
"v_rcp_f32 v48, v236\n"
"s_xor_b32 s16, s82, 1\n"
"s_add_u32 s16, s16, s81\n"
"s_mul_i32 s16, s16, 136192\n"
"s_lshl_b32 s17, s5, 12\n"
"s_add_u32 s16, s16, s17\n"
"v_pk_mul_f32 v[96:97], v[96:97], v[48:49] op_sel_hi:[1,0]\n"
"v_pk_mul_f32 v[98:99], v[98:99], v[48:49] op_sel_hi:[1,0]\n"
"v_pk_mul_f32 v[100:101], v[100:101], v[48:49] op_sel_hi:[1,0]\n"
"v_pk_mul_f32 v[102:103], v[102:103], v[48:49] op_sel_hi:[1,0]\n"
"v_cvt_pk_bf16_f32 v96, v96, v97\n"
"v_cvt_pk_bf16_f32 v97, v98, v99\n"
"v_cvt_pk_bf16_f32 v98, v100, v101\n"
"v_cvt_pk_bf16_f32 v99, v102, v103\n"
"buffer_store_dwordx4 v[96:99], v4, s[64:67], s16 offen offset:0 sc1\n"
"v_pk_mul_f32 v[104:105], v[104:105], v[48:49] op_sel_hi:[1,0]\n"
"v_pk_mul_f32 v[106:107], v[106:107], v[48:49] op_sel_hi:[1,0]\n"
"v_pk_mul_f32 v[108:109], v[108:109], v[48:49] op_sel_hi:[1,0]\n"
"v_pk_mul_f32 v[110:111], v[110:111], v[48:49] op_sel_hi:[1,0]\n"
"v_cvt_pk_bf16_f32 v104, v104, v105\n"
"v_cvt_pk_bf16_f32 v105, v106, v107\n"
"v_cvt_pk_bf16_f32 v106, v108, v109\n"
"v_cvt_pk_bf16_f32 v107, v110, v111\n"
"buffer_store_dwordx4 v[104:107], v4, s[64:67], s16 offen offset:1024 sc1\n"
"v_pk_mul_f32 v[112:113], v[112:113], v[48:49] op_sel_hi:[1,0]\n"
"v_pk_mul_f32 v[114:115], v[114:115], v[48:49] op_sel_hi:[1,0]\n"
"v_pk_mul_f32 v[116:117], v[116:117], v[48:49] op_sel_hi:[1,0]\n"
"v_pk_mul_f32 v[118:119], v[118:119], v[48:49] op_sel_hi:[1,0]\n"
"v_cvt_pk_bf16_f32 v112, v112, v113\n"
"v_cvt_pk_bf16_f32 v113, v114, v115\n"
"v_cvt_pk_bf16_f32 v114, v116, v117\n"
"v_cvt_pk_bf16_f32 v115, v118, v119\n"
"buffer_store_dwordx4 v[112:115], v4, s[64:67], s16 offen offset:2048 sc1\n"
"v_pk_mul_f32 v[120:121], v[120:121], v[48:49] op_sel_hi:[1,0]\n"
"v_pk_mul_f32 v[122:123], v[122:123], v[48:49] op_sel_hi:[1,0]\n"
"v_pk_mul_f32 v[124:125], v[124:125], v[48:49] op_sel_hi:[1,0]\n"
"v_pk_mul_f32 v[126:127], v[126:127], v[48:49] op_sel_hi:[1,0]\n"
"v_cvt_pk_bf16_f32 v120, v120, v121\n"
"v_cvt_pk_bf16_f32 v121, v122, v123\n"
"v_cvt_pk_bf16_f32 v122, v124, v125\n"
"v_cvt_pk_bf16_f32 v123, v126, v127\n"
"buffer_store_dwordx4 v[120:123], v4, s[64:67], s16 offen offset:3072 sc1\n"
"v_rcp_f32 v50, v240\n"
"s_xor_b32 s16, s82, 2\n"
"s_add_u32 s16, s16, s81\n"
"s_mul_i32 s16, s16, 136192\n"
"s_lshl_b32 s17, s5, 12\n"
"s_add_u32 s16, s16, s17\n"
"v_pk_mul_f32 v[128:129], v[128:129], v[50:51] op_sel_hi:[1,0]\n"
"v_pk_mul_f32 v[130:131], v[130:131], v[50:51] op_sel_hi:[1,0]\n"
"v_pk_mul_f32 v[132:133], v[132:133], v[50:51] op_sel_hi:[1,0]\n"
"v_pk_mul_f32 v[134:135], v[134:135], v[50:51] op_sel_hi:[1,0]\n"
"v_cvt_pk_bf16_f32 v128, v128, v129\n"
"v_cvt_pk_bf16_f32 v129, v130, v131\n"
"v_cvt_pk_bf16_f32 v130, v132, v133\n"
"v_cvt_pk_bf16_f32 v131, v134, v135\n"
"buffer_store_dwordx4 v[128:131], v4, s[64:67], s16 offen offset:0 sc1\n"
"v_pk_mul_f32 v[136:137], v[136:137], v[50:51] op_sel_hi:[1,0]\n"
"v_pk_mul_f32 v[138:139], v[138:139], v[50:51] op_sel_hi:[1,0]\n"
"v_pk_mul_f32 v[140:141], v[140:141], v[50:51] op_sel_hi:[1,0]\n"
"v_pk_mul_f32 v[142:143], v[142:143], v[50:51] op_sel_hi:[1,0]\n"
"v_cvt_pk_bf16_f32 v136, v136, v137\n"
"v_cvt_pk_bf16_f32 v137, v138, v139\n"
"v_cvt_pk_bf16_f32 v138, v140, v141\n"
"v_cvt_pk_bf16_f32 v139, v142, v143\n"
"buffer_store_dwordx4 v[136:139], v4, s[64:67], s16 offen offset:1024 sc1\n"
"v_pk_mul_f32 v[144:145], v[144:145], v[50:51] op_sel_hi:[1,0]\n"
"v_pk_mul_f32 v[146:147], v[146:147], v[50:51] op_sel_hi:[1,0]\n"
"v_pk_mul_f32 v[148:149], v[148:149], v[50:51] op_sel_hi:[1,0]\n"
"v_pk_mul_f32 v[150:151], v[150:151], v[50:51] op_sel_hi:[1,0]\n"
"v_cvt_pk_bf16_f32 v144, v144, v145\n"
"v_cvt_pk_bf16_f32 v145, v146, v147\n"
"v_cvt_pk_bf16_f32 v146, v148, v149\n"
"v_cvt_pk_bf16_f32 v147, v150, v151\n"
"buffer_store_dwordx4 v[144:147], v4, s[64:67], s16 offen offset:2048 sc1\n"
"v_pk_mul_f32 v[152:153], v[152:153], v[50:51] op_sel_hi:[1,0]\n"
"v_pk_mul_f32 v[154:155], v[154:155], v[50:51] op_sel_hi:[1,0]\n"
"v_pk_mul_f32 v[156:157], v[156:157], v[50:51] op_sel_hi:[1,0]\n"
"v_pk_mul_f32 v[158:159], v[158:159], v[50:51] op_sel_hi:[1,0]\n"
"v_cvt_pk_bf16_f32 v152, v152, v153\n"
"v_cvt_pk_bf16_f32 v153, v154, v155\n"
"v_cvt_pk_bf16_f32 v154, v156, v157\n"
"v_cvt_pk_bf16_f32 v155, v158, v159\n"
"buffer_store_dwordx4 v[152:155], v4, s[64:67], s16 offen offset:3072 sc1\n"
"s_branch LFBOK_%=\n"
"LOFR_%=:\n"
"s_mov_b32 s14, 40000\n"
"LOFP_%=:\n"
"buffer_load_dword v26, v25, s[64:67], 0 offen sc1\n"
"s_waitcnt vmcnt(0)\n"
"v_cmp_ne_u32 s[18:19], s8, v26\n"
"s_nop 0\n"
"s_cmp_lg_u64 s[18:19], 0\n"
"s_cbranch_scc0 LOFP_ok_%=\n"
"s_sleep 1\n"
"s_sub_u32 s14, s14, 1\n"
"s_cmp_lg_u32 s14, 0\n"
"s_cbranch_scc1 LOFP_%=\n"
"LOFP_ok_%=:\n"
"s_branch LOFK_%=\n"
"LFR_%=:\n"
"s_mov_b32 s14, 40000\n"
"LFR1_%=:\n"
"buffer_load_dwordx4 v[144:147], v4, s[64:67], s32 offen offset:0 sc1\n"
"buffer_load_dwordx4 v[148:151], v4, s[64:67], s32 offen offset:1024 sc1\n"
"buffer_load_dwordx4 v[152:155], v4, s[64:67], s32 offen offset:2048 sc1\n"
"buffer_load_dwordx4 v[156:159], v4, s[64:67], s32 offen offset:3072 sc1\n"
"buffer_load_dwordx4 v[160:163], v4, s[64:67], s33 offen offset:0 sc1\n"
"buffer_load_dwordx4 v[164:167], v4, s[64:67], s33 offen offset:1024 sc1\n"
"s_waitcnt vmcnt(0)\n"
"v_max_u32 v5, v144, v145\n"
"v_max3_u32 v5, v5, v146, v147\n"
"v_max3_u32 v5, v5, v148, v149\n"
"v_max3_u32 v5, v5, v150, v151\n"
"v_max3_u32 v5, v5, v152, v153\n"
"v_max3_u32 v5, v5, v154, v155\n"
"v_max3_u32 v5, v5, v156, v157\n"
"v_max3_u32 v5, v5, v158, v159\n"
"v_max3_u32 v5, v5, v160, v161\n"
"v_max3_u32 v5, v5, v162, v163\n"
"v_max3_u32 v5, v5, v164, v165\n"
"v_max3_u32 v5, v5, v166, v167\n"
"v_cmp_eq_u32 vcc, -1, v5\n"
"s_sub_u32 s14, s14, 1\n"
"s_cmp_eq_u32 s14, 0\n"
"s_cbranch_scc1 LFK_%=\n"
"s_cbranch_vccnz LFR1_%=\n"
"s_branch LFK_%=\n"
"LE1_%=:\n"
"s_mov_b32 s83, 24190976\n"
"buffer_load_dwordx4 v[232:235], v4, s[64:67], s83 offen offset:0 sc1\n"
"buffer_load_dwordx4 v[236:239], v4, s[64:67], s83 offen offset:1024 sc1\n"
"buffer_load_dwordx4 v[240:243], v4, s[64:67], s83 offen offset:2048 sc1\n"
"buffer_load_dwordx4 v[244:247], v4, s[64:67], s83 offen offset:3072 sc1\n"
"s_branch LE1K_%=\n"
"LE2_%=:\n"
"v_cmp_ne_u32 s[18:19], s8, v232\n"
"v_cmp_ne_u32 vcc, s8, v233\n"
"s_nop 0\n"
"s_or_b64 s[18:19], s[18:19], vcc\n"
"v_cmp_ne_u32 vcc, s8, v234\n"
"s_nop 0\n"
"s_or_b64 s[18:19], s[18:19], vcc\n"
"v_cmp_ne_u32 vcc, s8, v235\n"
"s_nop 0\n"
"s_or_b64 s[18:19], s[18:19], vcc\n"
"v_cmp_ne_u32 vcc, s8, v236\n"
"s_nop 0\n"
"s_or_b64 s[18:19], s[18:19], vcc\n"
"v_cmp_ne_u32 vcc, s8, v237\n"
"s_nop 0\n"
"s_or_b64 s[18:19], s[18:19], vcc\n"
"v_cmp_ne_u32 vcc, s8, v238\n"
"s_nop 0\n"
"s_or_b64 s[18:19], s[18:19], vcc\n"
"v_cmp_ne_u32 vcc, s8, v239\n"
"s_nop 0\n"
"s_or_b64 s[18:19], s[18:19], vcc\n"
"v_cmp_ne_u32 vcc, s8, v240\n"
"s_nop 0\n"
"s_or_b64 s[18:19], s[18:19], vcc\n"
"v_cmp_ne_u32 vcc, s8, v241\n"
"s_nop 0\n"
"s_or_b64 s[18:19], s[18:19], vcc\n"
"v_cmp_ne_u32 vcc, s8, v242\n"
"s_nop 0\n"
"s_or_b64 s[18:19], s[18:19], vcc\n"
"v_cmp_ne_u32 vcc, s8, v243\n"
"s_nop 0\n"
"s_or_b64 s[18:19], s[18:19], vcc\n"
"v_cmp_ne_u32 vcc, s8, v244\n"
"s_nop 0\n"
"s_or_b64 s[18:19], s[18:19], vcc\n"
"v_cmp_ne_u32 vcc, s8, v245\n"
"s_nop 0\n"
"s_or_b64 s[18:19], s[18:19], vcc\n"
"v_cmp_ne_u32 vcc, s8, v246\n"
"s_nop 0\n"
"s_or_b64 s[18:19], s[18:19], vcc\n"
"v_cmp_ne_u32 vcc, s8, v247\n"
"s_nop 0\n"
"s_or_b64 s[18:19], s[18:19], vcc\n"
"s_nop 0\n"
"s_cmp_lg_u64 s[18:19], 0\n"
"s_cbranch_scc0 LE3_%=\n"
"s_mov_b32 s14, 40000\n"
"LEP_%=:\n"
"buffer_load_dwordx4 v[232:235], v4, s[64:67], s83 offen offset:0 sc1\n"
"buffer_load_dwordx4 v[236:239], v4, s[64:67], s83 offen offset:1024 sc1\n"
"buffer_load_dwordx4 v[240:243], v4, s[64:67], s83 offen offset:2048 sc1\n"
"buffer_load_dwordx4 v[244:247], v4, s[64:67], s83 offen offset:3072 sc1\n"
"s_waitcnt vmcnt(0)\n"
"v_cmp_ne_u32 s[18:19], s8, v232\n"
"v_cmp_ne_u32 vcc, s8, v233\n"
"s_nop 0\n"
"s_or_b64 s[18:19], s[18:19], vcc\n"
"v_cmp_ne_u32 vcc, s8, v234\n"
"s_nop 0\n"
"s_or_b64 s[18:19], s[18:19], vcc\n"
"v_cmp_ne_u32 vcc, s8, v235\n"
"s_nop 0\n"
"s_or_b64 s[18:19], s[18:19], vcc\n"
"v_cmp_ne_u32 vcc, s8, v236\n"
"s_nop 0\n"
"s_or_b64 s[18:19], s[18:19], vcc\n"
"v_cmp_ne_u32 vcc, s8, v237\n"
"s_nop 0\n"
"s_or_b64 s[18:19], s[18:19], vcc\n"
"v_cmp_ne_u32 vcc, s8, v238\n"
"s_nop 0\n"
"s_or_b64 s[18:19], s[18:19], vcc\n"
"v_cmp_ne_u32 vcc, s8, v239\n"
"s_nop 0\n"
"s_or_b64 s[18:19], s[18:19], vcc\n"
"v_cmp_ne_u32 vcc, s8, v240\n"
"s_nop 0\n"
"s_or_b64 s[18:19], s[18:19], vcc\n"
"v_cmp_ne_u32 vcc, s8, v241\n"
"s_nop 0\n"
"s_or_b64 s[18:19], s[18:19], vcc\n"
"v_cmp_ne_u32 vcc, s8, v242\n"
"s_nop 0\n"
"s_or_b64 s[18:19], s[18:19], vcc\n"
"v_cmp_ne_u32 vcc, s8, v243\n"
"s_nop 0\n"
"s_or_b64 s[18:19], s[18:19], vcc\n"
"v_cmp_ne_u32 vcc, s8, v244\n"
"s_nop 0\n"
"s_or_b64 s[18:19], s[18:19], vcc\n"
"v_cmp_ne_u32 vcc, s8, v245\n"
"s_nop 0\n"
"s_or_b64 s[18:19], s[18:19], vcc\n"
"v_cmp_ne_u32 vcc, s8, v246\n"
"s_nop 0\n"
"s_or_b64 s[18:19], s[18:19], vcc\n"
"v_cmp_ne_u32 vcc, s8, v247\n"
"s_nop 0\n"
"s_or_b64 s[18:19], s[18:19], vcc\n"
"s_nop 0\n"
"s_cmp_lg_u64 s[18:19], 0\n"
"s_cbranch_scc0 LEP_ok_%=\n"
"s_sleep 1\n"
"s_sub_u32 s14, s14, 1\n"
"s_cmp_lg_u32 s14, 0\n"
"s_cbranch_scc1 LEP_%=\n"
"LEP_ok_%=:\n"
"LE3_%=:\n"
"v_mov_b32 v5, 24199168\n"
"s_mov_b64 exec, 1\n"
"buffer_store_dword v7, v5, s[64:67], 0 offen sc1\n"
"s_mov_b64 exec, -1\n"
"s_branch LE2K_%=\n"
"LFIN_%=:\n"
      :
      : "s"(__builtin_amdgcn_kernarg_segment_ptr()), "s"(blockIdx.x), "v"(threadIdx.x), "s"((unsigned)(size_t)lds)
      : "v1","v2","v3","v4","v5","v6","v7","v8","v9","v10","v11","v12","v13","v14","v15","v16","v17","v18","v19","v20","v21","v22","v23","v24","v25","v26","v27","v28","v29","v30","v31","v32","v33","v34","v35","v36","v37","v38","v39","v40","v41","v42","v43","v44","v45","v46","v47","v48","v49","v50","v51","v52","v53","v54","v55","v56","v57","v58","v59","v60","v61","v62","v63","v64","v65","v66","v67","v68","v69","v70","v71","v72","v73","v74","v75","v76","v77","v78","v79","v80","v81","v82","v83","v84","v85","v86","v87","v88","v89","v90","v91","v92","v93","v94","v95","v96","v97","v98","v99","v100","v101","v102","v103","v104","v105","v106","v107","v108","v109","v110","v111","v112","v113","v114","v115","v116","v117","v118","v119","v120","v121","v122","v123","v124","v125","v126","v127","v128","v129","v130","v131","v132","v133","v134","v135","v136","v137","v138","v139","v140","v141","v142","v143","v144","v145","v146","v147","v148","v149","v150","v151","v152","v153","v154","v155","v156","v157","v158","v159","v160","v161","v162","v163","v164","v165","v166","v167","v168","v169","v170","v171","v172","v173","v174","v175","v176","v177","v178","v179","v180","v181","v182","v183","v184","v185","v186","v187","v188","v189","v190","v191","v192","v193","v194","v195","v196","v197","v198","v199","v200","v201","v202","v203","v204","v205","v206","v207","v208","v209","v210","v211","v212","v213","v214","v215","v216","v217","v218","v219","v220","v221","v222","v223","v224","v225","v226","v227","v228","v229","v230","v231","v232","v233","v234","v235","v236","v237","v238","v239","v240","v241","v242","v243","v244","v245","v246","v247","v248","v249","v250","v251","v252","v253","v254","v255","a0","a1","a2","a3","a4","a5","a6","a7","a8","a9","a10","a11","a12","a13","a14","a15","a16","a17","a18","a19","a20","a21","a22","a23","a24","a25","a26","a27","a28","a29","a30","a31","a32","a33","a34","a35","a36","a37","a38","a39","a40","a41","a42","a43","a44","a45","a46","a47","a48","a49","a50","a51","a52","a53","a54","a55","a56","a57","a58","a59","a60","a61","a62","a63","a64","a65","a66","a67","a68","a69","a70","a71","a72","a73","a74","a75","a76","a77","a78","a79","a80","a81","a82","a83","a84","a85","a86","a87","a88","a89","a90","a91","a92","a93","a94","a95","a96","a97","a98","a99","a100","a101","a102","a103","a104","a105","a106","a107","a108","a109","a110","a111","a112","a113","a114","a115","a116","a117","a118","a119","a120","a121","a122","a123","a124","a125","a126","a127","a128","a129","a130","a131","a132","a133","a134","a135","a136","a137","a138","a139","a140","a141","a142","a143","a144","a145","a146","a147","a148","a149","a150","a151","a152","a153","a154","a155","a156","a157","a158","a159","a160","a161","a162","a163","a164","a165","a166","a167","a168","a169","a170","a171","a172","a173","a174","a175","a176","a177","a178","a179","a180","a181","a182","a183","a184","a185","a186","a187","a188","a189","a190","a191","a192","a193","a194","a195","a196","a197","a198","a199","a200","a201","a202","a203","a204","a205","a206","a207","a208","a209","a210","a211","a212","a213","a214","a215","a216","a217","a218","a219","a220","a221","a222","a223","a224","a225","a226","a227","a228","a229","a230","a231","a232","a233","a234","a235","a236","a237","a238","a239","a240","a241","a242","a243","a244","a245","a246","a247","a248","a249","a250","a251","a252","a253","a254","a255","s4","s5","s6","s7","s8","s9","s10","s11","s12","s13","s14","s15","s16","s17","s18","s19","s20","s21","s22","s23","s24","s25","s26","s27","s28","s29","s30","s31","s32","s33","s34","s35","s36","s37","s38","s39","s40","s41","s42","s43","s44","s45","s46","s47","s48","s49","s50","s51","s52","s53","s54","s55","s56","s57","s58","s59","s60","s61","s62","s63","s64","s65","s66","s67","s68","s69","s70","s71","s72","s73","s74","s75","s76","s77","s78","s79","s80","s81","s82","s83","s84","s85","s86","s87","s88","s89","s90","s91","s92","s93","s94","s95","s96","s97","s98","s99","vcc","scc","memory");
}

extern "C" void kernel_launch(void* const* d_in, const int* in_sizes, int n_in, void* d_out, int out_size, void* d_ws,
                              size_t ws_size, hipStream_t stream) {
  fused_block_kernel<<<256, 256, 0, stream>>>(d_in[0], d_in[1], d_in[2], d_in[3], d_in[4], d_in[6], d_in[7], d_in[8],
                                              d_in[9], d_out, d_ws);
}
```
